```python
import torch

def _softmax(x, axis=-1):
    return torch.softmax(x, dim=axis)

def _gelu(x):
    return torch.nn.functional.gelu(x, approximate='tanh')

def _silu(x):
    return torch.nn.functional.silu(x)

def _sigmoid(x):
    return torch.sigmoid(x)

def _layernorm(x, axis=-1, eps=1e-05):
    m = x.mean(dim=axis, keepdim=True)
    v = x.var(dim=axis, keepdim=True, unbiased=False)
    return (x - m) / torch.sqrt(v + eps)

def _scatter_add(tgt, idx, src, axis=0):
    return tgt.clone().index_add_(axis, idx, src)

def _segment_sum(src, seg, n):
    out = torch.zeros((n,) + src.shape[1:], dtype=src.dtype)
    return out.index_add_(0, seg, src)

def _pad(x, pw):
    flat = tuple((v for pair in reversed(pw) for v in pair))
    return torch.nn.functional.pad(x, flat)
kernel_name = 'concat_heads__spectral_gfnet__memory_aug_recurrent_mem_tr__prompt'

def setup_inputs():

    _w = lambda a: (lambda t: t.to(torch.bfloat16) if t.is_floating_point() else t)(torch.as_tensor(a))

    def _A_setup():
        B, S, D = (1, 2048, 640)
        Df = D // 2 + 1
        return {'x': _w((lambda *s: torch.randn(*s, dtype=torch.bfloat16) * 0.05)(B, S, D)), 'w_r': _w((lambda *s: torch.randn(*s, dtype=torch.bfloat16) * 3.2)(S, Df) * 0.02), 'w_i': _w((lambda *s: torch.randn(*s, dtype=torch.bfloat16) * 3.2)(S, Df) * 0.02)}
    _A_in = _A_setup()

    def _B_setup():
        B, S, Dm = (1, 2048, 640)
        H, Dh = (32, 128)
        M, seg = (8, 32)
        seg = min(seg, S)
        S = S // seg * seg
        return {'x': _w((lambda *s: torch.randn(*s, dtype=torch.bfloat16) * 0.05)(B, S, Dm)), 'mem0': _w((lambda *s: torch.randn(*s, dtype=torch.bfloat16) * 0.4)(B, M, Dm)), 'Wq': _w((lambda *s: torch.randn(*s, dtype=torch.bfloat16) * 6.4)(Dm, H, Dh) * Dm ** (-0.5)), 'Wk': _w((lambda *s: torch.randn(*s, dtype=torch.bfloat16) * 6.4)(Dm, H, Dh) * Dm ** (-0.5)), 'Wv': _w((lambda *s: torch.randn(*s, dtype=torch.bfloat16) * 0.4)(Dm, H, Dh) * Dm ** (-0.5)), 'Wo': _w((lambda *s: torch.randn(*s, dtype=torch.bfloat16) * 0.4)(H, Dh, Dm) * (H * Dh) ** (-0.5)), 'seg_len': seg}
    _B_in = _B_setup()
    return {'A_x': _A_in['x'], 'A_w_r': _A_in['w_r'], 'A_w_i': _A_in['w_i'], 'B_x': _B_in['x'], 'B_mem0': _B_in['mem0'], 'B_Wq': _B_in['Wq'], 'B_Wk': _B_in['Wk'], 'B_Wv': _B_in['Wv'], 'B_Wo': _B_in['Wo'], 'B_seg_len': _B_in['seg_len']}

def reference(A_x, A_w_r, A_w_i, B_x, B_mem0, B_Wq, B_Wk, B_Wv, B_Wo, B_seg_len):

    def _A_ref(x, w_r, w_i):
        B, S, D = x.shape
        X = (lambda x, axes=(-2, -1): torch.fft.rfft2(x.float(), dim=axes))(x, axes=(-2, -1))
        W = w_r + 1j * w_i
        X = X * W
        y = (lambda x, s, axes=(-2, -1): torch.fft.irfft2(x, s=s, dim=axes).to(torch.bfloat16))(X, (S, D), axes=(-2, -1))
        return y

    def _B_ref(x, mem0, Wq, Wk, Wv, Wo, seg_len):
        B, S, Dm = x.shape
        H, Dh = (Wq.shape[1], Wq.shape[2])
        M = mem0.shape[1]
        scale = Dh ** (-0.5)
        n_seg = S // seg_len
        L = M + seg_len
        idx = torch.arange(L)
        mask = (idx[:, None] < M) | (idx[:, None] >= idx[None, :])
        mem = mem0
        outs = []
        for s in range(n_seg):
            xs = x[:, s * seg_len:(s + 1) * seg_len, :].clone()
            h = torch.concatenate([mem, xs], axis=1)
            q = torch.einsum('btd,dhe->bhte', h, Wq)
            k = torch.einsum('btd,dhe->bhte', h, Wk)
            v = torch.einsum('btd,dhe->bhte', h, Wv)
            scores = torch.einsum('bhqe,bhke->bhqk', q, k) * scale
            scores = torch.where(mask[None, None], scores, -1000000000.0)
            p = _softmax(scores, axis=-1)
            o = torch.einsum('bhqk,bhke->bhqe', p, v)
            o = torch.einsum('bhte,hed->btd', o, Wo)
            mem = o[:, :M, :].clone()
            outs.append(o[:, M:, :])
        return torch.concatenate(outs, axis=1)
    a = _A_ref(A_x, A_w_r, A_w_i)
    b = _B_ref(B_x, B_mem0, B_Wq, B_Wk, B_Wv, B_Wo, B_seg_len)
    return torch.concatenate([a, b], axis=-1)
```

```cpp
#include <hip/hip_runtime.h>
#include <hip/hip_bf16.h>

typedef __bf16 bf16x8 __attribute__((ext_vector_type(8)));
typedef __bf16 bf16x16 __attribute__((ext_vector_type(16)));
typedef __bf16 bf16x2 __attribute__((ext_vector_type(2)));
typedef short s16x4 __attribute__((ext_vector_type(4)));
typedef float f32x2 __attribute__((ext_vector_type(2)));
typedef float f32x4 __attribute__((ext_vector_type(4)));
typedef float f32x16 __attribute__((ext_vector_type(16)));
typedef unsigned u32x2 __attribute__((ext_vector_type(2)));
typedef unsigned u32x4 __attribute__((ext_vector_type(4)));
typedef unsigned u32x8 __attribute__((ext_vector_type(8)));
#define LAS __attribute__((address_space(3)))
#define GAS __attribute__((address_space(1)))
#define DEV __device__ __forceinline__

constexpr int S = 2048, D = 640, DF = 321, H = 32, DH = 128, NSEG = 64, NCT = 40;
constexpr unsigned SENT = 0xFFFFFFFFu;
constexpr int SPIN_CAP = 1 << 15;

constexpr size_t MB = 1 << 20;
constexpr size_t OFF_COMM0 = 0;
constexpr size_t OFF_COMM1 = 2 * MB;
constexpr size_t OFF_CTRL = 4 * MB;
constexpr size_t OFF_QX = 5 * MB;
constexpr size_t OFF_KX = OFF_QX + 16 * MB;
constexpr size_t OFF_U = OFF_KX + 16 * MB;
constexpr size_t OFF_T = OFF_U + 80 * MB;
constexpr size_t OFF_PX = OFF_T + 20 * MB;
constexpr size_t OFF_PXM = OFF_PX + 4 * MB;
constexpr size_t OFF_X1 = OFF_PXM + 1 * MB;
constexpr size_t OFF_Y1 = OFF_X1 + 6 * MB;
constexpr size_t OFF_WT = OFF_Y1 + 6 * MB;
constexpr size_t WS_NEED = OFF_WT + 3 * MB;

constexpr unsigned MEM_SLOT = 10240;
constexpr unsigned PM_BASE = 688128;
DEV unsigned physo(unsigned o) { return ((o >> 13) << 14) + (o & 8191u); }

struct Params {
    const unsigned short* ax; const unsigned short* wr; const unsigned short* wi;
    const unsigned short* bx; const unsigned short* mem0;
    const unsigned short* wq; const unsigned short* wk; const unsigned short* wv; const unsigned short* wo;
    unsigned short* out; char* ws;
};

DEV __amdgpu_buffer_rsrc_t mkrsrc(const void* p, unsigned bytes) { return __builtin_amdgcn_make_buffer_rsrc((void*)p, 0, bytes, 0x00020000); }
DEV u32x4 ld16_sc1(__amdgpu_buffer_rsrc_t r, unsigned voff, unsigned soff) { return __builtin_amdgcn_raw_buffer_load_b128(r, voff, soff, 16); }
DEV u32x2 ld8_sc1(__amdgpu_buffer_rsrc_t r, unsigned voff, unsigned soff) { return __builtin_amdgcn_raw_buffer_load_b64(r, voff, soff, 16); }
DEV void st16_sc1(u32x4 v, __amdgpu_buffer_rsrc_t r, unsigned voff, unsigned soff) { __builtin_amdgcn_raw_buffer_store_b128(v, r, voff, soff, 16); }
DEV void st8_sc1(u32x2 v, __amdgpu_buffer_rsrc_t r, unsigned voff, unsigned soff) { __builtin_amdgcn_raw_buffer_store_b64(v, r, voff, soff, 16); }
DEV void st4_sc1(unsigned v, __amdgpu_buffer_rsrc_t r, unsigned voff, unsigned soff) { __builtin_amdgcn_raw_buffer_store_b32(v, r, voff, soff, 16); }
DEV unsigned pk_bf16(float a, float b) { f32x2 f = {a, b}; bf16x2 p = __builtin_convertvector(f, bf16x2); return __builtin_bit_cast(unsigned, p); }
DEV float bf_lo(unsigned u) { return __builtin_bit_cast(float, u << 16); }
DEV float bf_hi(unsigned u) { return __builtin_bit_cast(float, u & 0xFFFF0000u); }
DEV unsigned nosent(unsigned u) { return u < 0xFFFFFFFEu ? u : 0xFFFFFFFEu; }
DEV unsigned mx4(u32x4 v) { return max(max(v.x, v.y), max(v.z, v.w)); }
DEV bf16x8 as_bf8(u32x4 v) { return __builtin_bit_cast(bf16x8, v); }
DEV u32x2 tr_read(const char* lds, unsigned off) { s16x4 v = __builtin_amdgcn_ds_read_tr16_b64_v4i16((LAS s16x4*)(lds + off)); return __builtin_bit_cast(u32x2, v); }
DEV u32x4 lds_ld16(const char* lds, unsigned off) { return *(const u32x4*)(lds + off); }
DEV void lds_st16(char* lds, unsigned off, u32x4 v) { *(u32x4*)(lds + off) = v; }
DEV void glds16(const void* g, char* lds) { __builtin_amdgcn_global_load_lds((const GAS unsigned*)g, (LAS unsigned*)lds, 16, 0, 0); }
#define WAIT_VM(n) asm volatile("s_waitcnt vmcnt(" #n ")" ::: "memory")
#define WAIT_LGKM0() asm volatile("s_waitcnt lgkmcnt(0)" ::: "memory")
DEV void raw_barrier() { WAIT_LGKM0(); __builtin_amdgcn_s_barrier(); asm volatile("" ::: "memory"); }

DEV unsigned swz(unsigned row) { return ((row & 3u) << 2) | ((row >> 2) & 3u); }
DEV unsigned img_off(unsigned row, unsigned ch) { return 256u * row + 16u * (ch ^ swz(row)); }
DEV void stage_img(const unsigned short* src, size_t pitch, int nrows, char* img, int wave, int lane) {
    for (int i = wave; i < nrows / 4; i += 8) {
        const unsigned row = 4 * i + (lane >> 4), ch = (lane & 15) ^ swz(row);
        glds16(src + (size_t)row * pitch + ch * 8, img + i * 1024);
    }
}
DEV u32x2 tr16(const char* img, unsigned r0, unsigned c, int lane) {
    const unsigned q = (lane & 15) >> 2, p = lane & 3;
    return tr_read(img, img_off(r0 + q, 2 * c + (p >> 1)) + 8 * (p & 1));
}
DEV u32x2 tr32(const char* img, unsigned r0, unsigned c, int lane) {
    const unsigned blk = (lane >> 4) & 1, q = (lane & 15) >> 2, p = lane & 3;
    return tr_read(img, img_off(r0 + q, 4 * c + 2 * blk + (p >> 1)) + 8 * (p & 1));
}
DEV bf16x16 load_bsp(const char* img, unsigned rb, unsigned c, int lane) {
    const unsigned g = lane >> 4;
    u32x2 a = tr16(img, rb + 8 * g, c, lane), b = tr16(img, rb + 8 * g + 4, c, lane), d = tr16(img, rb + 32 + 8 * g, c, lane), e = tr16(img, rb + 32 + 8 * g + 4, c, lane);
    u32x8 r = {a.x, a.y, b.x, b.y, d.x, d.y, e.x, e.y};
    return __builtin_bit_cast(bf16x16, r);
}
DEV f32x4 smf(u32x4 a, bf16x16 b, f32x4 c, int idx) { return __builtin_amdgcn_smfmac_f32_16x16x64_bf16(as_bf8(a), b, c, idx, 0, 0); }
DEV f32x4 mf16(u32x4 a, u32x4 b, f32x4 c) { return __builtin_amdgcn_mfma_f32_16x16x32_bf16(as_bf8(a), as_bf8(b), c, 0, 0, 0); }
DEV f32x16 mf32(u32x4 a, u32x4 b, f32x16 c) { return __builtin_amdgcn_mfma_f32_32x32x16_bf16(as_bf8(a), as_bf8(b), c, 0, 0, 0); }
DEV float u2f(unsigned u) { return __builtin_bit_cast(float, u); }
DEV unsigned f2u(float f) { return __builtin_bit_cast(unsigned, f); }
DEV float xmax16_32(float x) {
    auto r = __builtin_amdgcn_permlane16_swap(f2u(x), f2u(x), false, false);
    x = fmaxf(u2f(r[0]), u2f(r[1]));
    auto r2 = __builtin_amdgcn_permlane32_swap(f2u(x), f2u(x), false, false);
    return fmaxf(u2f(r2[0]), u2f(r2[1]));
}
DEV float xsum16_32(float x) {
    auto r = __builtin_amdgcn_permlane16_swap(f2u(x), f2u(x), false, false);
    x = u2f(r[0]) + u2f(r[1]);
    auto r2 = __builtin_amdgcn_permlane32_swap(f2u(x), f2u(x), false, false);
    return u2f(r2[0]) + u2f(r2[1]);
}

__global__ void __launch_bounds__(256) k_prep(Params p) {
    const int tid = threadIdx.x, b = blockIdx.x, nb = gridDim.x;
    char* ws = p.ws;
    unsigned* ctrl = (unsigned*)(ws + OFF_CTRL);
    const u32x4 sv = {SENT, SENT, SENT, SENT};
    if (b == 0) {
        if (tid < 64) {
            const unsigned xcc = __builtin_amdgcn_s_getreg(20 | (3 << 11)) & 0xf;
            for (int pg = 0; pg < 2; ++pg) {
                unsigned best[2];
                for (int c = 0; c < 2; ++c) {
                    char* base = ws + (pg ? OFF_COMM1 : OFF_COMM0) + 2 * MB - 16384 + c * 8192;
                    const __amdgpu_buffer_rsrc_t r = mkrsrc(base, 8192);
                    st16_sc1(sv, r, tid * 128, 0);
                    WAIT_VM(0);
                    unsigned bst = 1u << 30;
                    for (int i = 0; i < 6; ++i) {
                        const char* q = base + i * 1280;
                        unsigned long long t0, t1; unsigned v;
                        asm volatile("s_memtime %0\n\ts_waitcnt lgkmcnt(0)\n\tglobal_load_dword %2, %3, off sc1\n\ts_waitcnt vmcnt(0)\n\ts_memtime %1\n\ts_waitcnt lgkmcnt(0)"
                                     : "=&s"(t0), "=&s"(t1), "=&v"(v) : "v"(q) : "memory");
                        bst = min(bst, (unsigned)(t1 - t0));
                    }
                    best[c] = bst;
                }
                unsigned side = best[0] < best[1] ? 0u : 1u;
                if (xcc >= 4) side ^= 1u;
                if (tid == 0) { ctrl[pg] = side; ctrl[16] = 0; ctrl[4 + 2 * pg] = best[0]; ctrl[5 + 2 * pg] = best[1]; }
            }
        }
        __syncthreads();
    }
    {
        const __amdgpu_buffer_rsrc_t r = mkrsrc(ws + OFF_COMM0, 4 * MB);
        for (unsigned i = (b * 256 + tid) * 16; i < 4 * MB; i += nb * 256 * 16) {
            u32x4 v = sv;
            if (i < 32768) {
                const unsigned lo = ((i >> 14) << 13) + (i & 8191u);
                if (lo < MEM_SLOT) {
                    const unsigned st = lo >> 10, ln = (lo >> 4) & 63, ga = ln >> 4, m = (ln & 15) >> 1, t = ln & 1;
                    const unsigned short* src = p.mem0 + m * D + 64 * st + 16 * ga + 2 * t;
                    v.x = nosent(*(const unsigned*)(src)); v.y = nosent(*(const unsigned*)(src + 4)); v.z = nosent(*(const unsigned*)(src + 8)); v.w = nosent(*(const unsigned*)(src + 12));
                }
            }
            st16_sc1(v, r, i, 0);
        }
    }
    {
        const __amdgpu_buffer_rsrc_t r = mkrsrc(ws + OFF_T, 20 * MB);
        for (unsigned i = (b * 256 + tid) * 16; i < 20 * MB; i += nb * 256 * 16) st16_sc1(sv, r, i, 0);
    }
}

constexpr int XT_BYTES = 40960;
DEV void stage_x(const unsigned short* x, int seg, char* img, int wave, int lane) {
#pragma unroll
    for (int it = 0; it < 5; ++it) {
        const int i = wave + 8 * it;
        const int j = i >> 2, tok = 8 * (i & 3) + (lane >> 3), c = (lane & 7) ^ ((tok >> 1) & 7);
        glds16(x + (size_t)(seg * 32 + tok) * D + j * 64 + c * 8, img + i * 1024);
    }
}
DEV unsigned xfrag_off(int ks, int lane) {
    const int tok = lane & 31, hi = lane >> 5, c = 2 * (ks & 3) + hi;
    return (ks >> 2) * 4096 + tok * 128 + ((c ^ ((tok >> 1) & 7)) << 4);
}

DEV void role_gemm_qk(const Params& p, int h, int par, char* lds) {
    const int tid = threadIdx.x, lane = tid & 63, wave = __builtin_amdgcn_readfirstlane(tid >> 6);
    u32x4 w[40];
    {
        const int c = wave & 3;
#pragma unroll
        for (int bt = 0; bt < 5; ++bt) {
            char* img = lds + (bt & 1) * 65536;
            if (bt == 0) {
                stage_img(p.wq + h * DH, H * DH, 128, img, wave, lane);
                stage_img(p.wk + h * DH, H * DH, 128, img + 32768, wave, lane);
            }
            if (bt + 1 < 5) {
                char* nx = lds + ((bt + 1) & 1) * 65536;
                stage_img(p.wq + (size_t)(bt + 1) * 128 * H * DH + h * DH, H * DH, 128, nx, wave, lane);
                stage_img(p.wk + (size_t)(bt + 1) * 128 * H * DH + h * DH, H * DH, 128, nx + 32768, wave, lane);
                WAIT_VM(8);
            } else WAIT_VM(0);
            raw_barrier();
            const char* my = img + (wave >> 2) * 32768;
#pragma unroll
            for (int ks = 0; ks < 8; ++ks) {
                u32x2 a = tr32(my, 16 * ks + 8 * (lane >> 5), c, lane), b = tr32(my, 16 * ks + 8 * (lane >> 5) + 4, c, lane);
                w[bt * 8 + ks] = u32x4{a.x, a.y, b.x, b.y};
            }
            raw_barrier();
        }
    }
    const __amdgpu_buffer_rsrc_t ro = mkrsrc(p.ws + (wave < 4 ? OFF_QX : OFF_KX), 16 * MB);
    stage_x(p.bx, par, lds, wave, lane);
    stage_x(p.bx, par + 2, lds + XT_BYTES, wave, lane);
    for (int i = 0; i < 32; ++i) {
        const int seg = par + 2 * i;
        char* img = lds + (i % 3) * XT_BYTES;
        if (i == 0) WAIT_VM(5); else if (i < 31) WAIT_VM(7); else WAIT_VM(2);
        raw_barrier();
        if (i + 2 < 32) stage_x(p.bx, seg + 4, lds + ((i + 2) % 3) * XT_BYTES, wave, lane);
        f32x16 acc = {};
#pragma unroll
        for (int ks = 0; ks < 40; ++ks) acc = mf32(w[ks], lds_ld16(img, xfrag_off(ks, lane)), acc);
#pragma unroll
        for (int a = 0; a < 4; a += 2) {
            unsigned x0 = pk_bf16(acc[4 * a], acc[4 * a + 1]), x1 = pk_bf16(acc[4 * a + 2], acc[4 * a + 3]);
            unsigned y0 = pk_bf16(acc[4 * a + 4], acc[4 * a + 5]), y1 = pk_bf16(acc[4 * a + 6], acc[4 * a + 7]);
            auto r0 = __builtin_amdgcn_permlane32_swap(x0, y0, false, false);
            auto r1 = __builtin_amdgcn_permlane32_swap(x1, y1, false, false);
            u32x4 v = {r0[0], r1[0], r0[1], r1[1]};
            st16_sc1(v, ro, ((4 * (wave & 3) + a) * 32 + lane) * 16, (seg * H + h) * 8192);
        }
    }
}

DEV void role_gemm_vu(const Params& p, int h, int par, char* lds) {
    const int tid = threadIdx.x, lane = tid & 63, wave = __builtin_amdgcn_readfirstlane(tid >> 6);
    const int et = wave & 3, kh = wave >> 2, g = lane >> 4;
    u32x4 wv[20], wo[5][4];
    {
#pragma unroll
        for (int bt = 0; bt < 5; ++bt) {
            char* img = lds + (bt & 1) * 65536;
            if (bt == 0) {
                stage_img(p.wv + h * DH, H * DH, 64, img, wave, lane);
                stage_img(p.wv + (size_t)320 * H * DH + h * DH, H * DH, 64, img + 16384, wave, lane);
                stage_img(p.wo + (size_t)h * DH * D, D, 128, img + 32768, wave, lane);
            }
            if (bt + 1 < 5) {
                char* nx = lds + ((bt + 1) & 1) * 65536;
                stage_img(p.wv + (size_t)(bt + 1) * 64 * H * DH + h * DH, H * DH, 64, nx, wave, lane);
                stage_img(p.wv + (size_t)(320 + (bt + 1) * 64) * H * DH + h * DH, H * DH, 64, nx + 16384, wave, lane);
                stage_img(p.wo + (size_t)h * DH * D + (bt + 1) * 128, D, 128, nx + 32768, wave, lane);
                WAIT_VM(8);
            } else WAIT_VM(0);
            raw_barrier();
            const char* my = img + kh * 16384;
#pragma unroll
            for (int ks = 0; ks < 4; ++ks) {
                u32x2 a = tr32(my, 16 * ks + 8 * (lane >> 5), et, lane), b = tr32(my, 16 * ks + 8 * (lane >> 5) + 4, et, lane);
                wv[bt * 4 + ks] = u32x4{a.x, a.y, b.x, b.y};
            }
#pragma unroll
            for (int ks = 0; ks < 4; ++ks) {
                const unsigned r0 = 32 * ks + 16 * (g >> 1) + 4 * (g & 1);
                u32x2 a = tr16(img + 32768, r0, wave, lane), b = tr16(img + 32768, r0 + 8, wave, lane);
                wo[bt][ks] = u32x4{a.x, a.y, b.x, b.y};
            }
            raw_barrier();
        }
    }
    char* xch = lds + 2 * XT_BYTES;
    char* vimg = xch + 16384;
    const __amdgpu_buffer_rsrc_t ru = mkrsrc(p.ws + OFF_U, 80 * MB);
    stage_x(p.bx, par, lds, wave, lane);
    stage_x(p.bx, par + 2, lds + XT_BYTES, wave, lane);
    for (int i = 0; i <= 32; ++i) {
        const int seg = par + 2 * i;
        const char* img = lds + (i & 1) * XT_BYTES;
        if (i <= 1) WAIT_VM(5); else if (i < 31) WAIT_VM(10); else WAIT_VM(5);
        raw_barrier();
        f32x4 k0 = {}, k1 = {};
        if (i < 32) {
            f32x16 acc = {};
#pragma unroll
            for (int ks = 0; ks < 20; ++ks) acc = mf32(wv[ks], lds_ld16(img + kh * 20480, xfrag_off(ks, lane)), acc);
            f32x4 s0, s1;
            if (kh == 0) { s0 = f32x4{acc[8], acc[9], acc[10], acc[11]}; s1 = f32x4{acc[12], acc[13], acc[14], acc[15]}; }
            else { s0 = f32x4{acc[0], acc[1], acc[2], acc[3]}; s1 = f32x4{acc[4], acc[5], acc[6], acc[7]}; }
            *(f32x4*)(xch + wave * 2048 + lane * 16) = s0;
            *(f32x4*)(xch + wave * 2048 + 1024 + lane * 16) = s1;
            if (kh == 0) { k0 = f32x4{acc[0], acc[1], acc[2], acc[3]}; k1 = f32x4{acc[4], acc[5], acc[6], acc[7]}; }
            else { k0 = f32x4{acc[8], acc[9], acc[10], acc[11]}; k1 = f32x4{acc[12], acc[13], acc[14], acc[15]}; }
        }
        if (i > 0) {
            u32x4 vf[2][4];
#pragma unroll
            for (int tt = 0; tt < 2; ++tt)
#pragma unroll
                for (int ks = 0; ks < 4; ++ks) vf[tt][ks] = lds_ld16(vimg, ((ks * 4 + g) * 32 + 16 * tt + (lane & 15)) * 16);
#pragma unroll
            for (int bt = 0; bt < 5; ++bt) {
                f32x4 d0 = {}, d1 = {};
#pragma unroll
                for (int ks = 0; ks < 4; ++ks) { d0 = mf16(vf[0][ks], wo[bt][ks], d0); d1 = mf16(vf[1][ks], wo[bt][ks], d1); }
                u32x4 v = {pk_bf16(d0[0], d0[1]), pk_bf16(d0[2], d0[3]), pk_bf16(d1[0], d1[1]), pk_bf16(d1[2], d1[3])};
                st16_sc1(v, ru, lane * 16 + (8 * bt + wave) * 32768, ((seg - 2) * NCT * H + h) * 1024);
            }
        }
        raw_barrier();
        if (i < 32) {
            if (i + 2 < 32) stage_x(p.bx, seg + 4, lds + (i & 1) * XT_BYTES, wave, lane);
            const f32x4 o0 = *(const f32x4*)(xch + (wave ^ 4) * 2048 + lane * 16), o1 = *(const f32x4*)(xch + (wave ^ 4) * 2048 + 1024 + lane * 16);
            k0 += o0; k1 += o1;
            u32x4 v = {pk_bf16(k0[0], k0[1]), pk_bf16(k0[2], k0[3]), pk_bf16(k1[0], k1[1]), pk_bf16(k1[2], k1[3])};
            lds_st16(vimg, ((et * 4 + 2 * kh + (lane >> 5)) * 32 + (lane & 31)) * 16, v);
        }
    }
}

__global__ void __launch_bounds__(512, 2) k_gemm(Params p) {
    extern __shared__ __attribute__((aligned(16))) char lds[];
    const int b = blockIdx.x;
    if (b < 64) role_gemm_qk(p, b >> 1, b & 1, lds);
    else role_gemm_vu(p, (b - 64) >> 1, b & 1, lds);
}

DEV void gather_mem(__amdgpu_buffer_rsrc_t rc0, int s, unsigned side0, char* mimg, int wave, int lane, unsigned* tmo, int& cap) {
    if (wave >= 3) {
        const unsigned f = 2 * (wave - 3);
        const unsigned o0 = physo(s * MEM_SLOT + f * 1024) + side0, o1 = physo(s * MEM_SLOT + f * 1024 + 1024) + side0;
        u32x4 a, b;
        for (int sp = 0;; ++sp) {
            asm volatile("" ::: "memory");
            a = ld16_sc1(rc0, lane * 16, o0); b = ld16_sc1(rc0, lane * 16, o1);
            if (__all(max(mx4(a), mx4(b)) != SENT)) break;
            if (sp > cap) { *tmo = 1; cap = 0; break; }
        }
        lds_st16(mimg, f * 1024 + lane * 16, a);
        lds_st16(mimg, f * 1024 + 1024 + lane * 16, b);
    }
}

constexpr float SCALE = 0.08838834764831845f;
constexpr float LOG2E = 1.4426950408889634f;
constexpr float MASKV = -999292928.0f;
DEV void round_scores(f32x4& d) {
    unsigned a = pk_bf16(d[0], d[1]), b = pk_bf16(d[2], d[3]);
    a = pk_bf16(bf_lo(a) * SCALE, bf_hi(a) * SCALE); b = pk_bf16(bf_lo(b) * SCALE, bf_hi(b) * SCALE);
    d = f32x4{bf_lo(a), bf_hi(a), bf_lo(b), bf_hi(b)};
}
DEV float max4(f32x4 d) { return fmaxf(fmaxf(d[0], d[1]), fmaxf(d[2], d[3])); }
DEV void exp4(f32x4& d, float mx) {
#pragma unroll
    for (int i = 0; i < 4; ++i) d[i] = __builtin_amdgcn_exp2f((d[i] - mx) * LOG2E);
}
DEV float sum4(f32x4 d) { return (d[0] + d[1]) + (d[2] + d[3]); }

DEV void role_qk(const Params& p, int h, char* lds) {
    const int tid = threadIdx.x, lane = tid & 63, wave = __builtin_amdgcn_readfirstlane(tid >> 6);
    const int g = lane >> 4, l16 = lane & 15;
    unsigned* ctrl = (unsigned*)(p.ws + OFF_CTRL);
    bf16x16 wq[10], wk[10];
#pragma unroll
    for (int bt = 0; bt < 5; ++bt) {
        char* img = lds + (bt & 1) * 65536;
        if (bt == 0) {
            stage_img(p.wq + h * DH, H * DH, 128, img, wave, lane);
            stage_img(p.wk + h * DH, H * DH, 128, img + 32768, wave, lane);
        }
        if (bt + 1 < 5) {
            char* nx = lds + ((bt + 1) & 1) * 65536;
            stage_img(p.wq + (size_t)(bt + 1) * 128 * H * DH + h * DH, H * DH, 128, nx, wave, lane);
            stage_img(p.wk + (size_t)(bt + 1) * 128 * H * DH + h * DH, H * DH, 128, nx + 32768, wave, lane);
            WAIT_VM(8);
        } else WAIT_VM(0);
        raw_barrier();
#pragma unroll
        for (int u = 0; u < 2; ++u) { wq[2 * bt + u] = load_bsp(img, 64 * u, wave, lane); wk[2 * bt + u] = load_bsp(img + 32768, 64 * u, wave, lane); }
        raw_barrier();
    }
    const unsigned side0 = __builtin_amdgcn_readfirstlane(ctrl[0]) << 13, side1 = __builtin_amdgcn_readfirstlane(ctrl[1]) << 13;
    const __amdgpu_buffer_rsrc_t rc0 = mkrsrc(p.ws + OFF_COMM0, 2 * MB), rc1 = mkrsrc(p.ws + OFF_COMM1, 2 * MB);
    const __amdgpu_buffer_rsrc_t rq = mkrsrc(p.ws + OFF_QX, 32 * MB);
    const __amdgpu_buffer_rsrc_t rpx = mkrsrc(p.ws + OFF_PX, 5 * MB);
    char* mimg = lds;
    char* ximg = lds + 10240;
    char* qk = lds + 16384;
    const int idx = (lane & 1) ? 0xEEEE : 0x4444;
    int cap = SPIN_CAP;
    {
        u32x4 a = ld16_sc1(rq, tid * 16, h * 8192), b = ld16_sc1(rq, tid * 16, 16 * MB + h * 8192);
        lds_st16(qk, tid * 16, a); lds_st16(qk, 8192 + tid * 16, b);
    }
    for (int s = 0; s < NSEG; ++s) {
        gather_mem(rc0, s, side0, mimg, wave, lane, ctrl + 16, cap);
        raw_barrier();
        u32x4 pa, pb;
        if (s + 1 < NSEG) { pa = ld16_sc1(rq, tid * 16, ((s + 1) * H + h) * 8192); pb = ld16_sc1(rq, tid * 16, 16 * MB + ((s + 1) * H + h) * 8192); }
        f32x4 aq = {}, ak = {};
#pragma unroll
        for (int st = 0; st < 10; ++st) {
            const u32x4 a = lds_ld16(mimg, st * 1024 + lane * 16);
            aq = smf(a, wq[st], aq, idx); ak = smf(a, wk[st], ak, idx);
        }
        {
            const unsigned e = 16 * wave + l16;
            *(unsigned*)(ximg + e * 32 + g * 4) = pk_bf16(aq[0] + aq[1], aq[2] + aq[3]);
            *(unsigned*)(ximg + e * 32 + 16 + g * 4) = pk_bf16(ak[0] + ak[1], ak[2] + ak[3]);
        }
        const char* kimg = qk + (s & 1) * 16384 + 8192;
        const char* qimg = qk + (s & 1) * 16384;
        raw_barrier();
        if (wave <= 2) {
            u32x4 F[4];
#pragma unroll
            for (int ks = 0; ks < 4; ++ks) {
                const unsigned e0 = 32 * ks + 8 * g + (l16 >> 2);
                u32x2 a = tr_read(ximg, e0 * 32 + (lane & 3) * 8), b = tr_read(ximg, (e0 + 4) * 32 + (lane & 3) * 8);
                F[ks] = u32x4{a.x, a.y, b.x, b.y};
            }
            f32x4 d0 = {}, d1 = {}, dm = {};
            if (wave == 0) {
                if (s + 1 < NSEG) {
                    const unsigned gq = l16 >> 2, r = l16 & 3, key0 = 16 * (r >> 1) + 8 * (gq >> 1) + 2 * (gq & 1) + (r & 1);
#pragma unroll
                    for (int ks = 0; ks < 4; ++ks) {
                        d0 = mf16(lds_ld16(kimg, ((4 * ks + g) * 32 + key0) * 16), F[ks], d0);
                        d1 = mf16(lds_ld16(kimg, ((4 * ks + g) * 32 + key0 + 4) * 16), F[ks], d1);
                        dm = mf16(F[ks], F[ks], dm);
                    }
                    round_scores(d0); round_scores(d1); round_scores(dm);
                    float mx = fmaxf(max4(d0), max4(d1));
                    if (g >= 2) mx = fmaxf(mx, max4(dm));
                    mx = xmax16_32(mx);
                    exp4(d0, mx); exp4(d1, mx); exp4(dm, mx);
                    float sm = sum4(d0) + sum4(d1);
                    if (g >= 2) sm += sum4(dm);
                    sm = xsum16_32(sm);
                    const float inv = 1.0f / sm;
                    if (l16 < 8) {
                        u32x4 v = {nosent(pk_bf16(d0[0] * inv, d0[1] * inv)), nosent(pk_bf16(d0[2] * inv, d0[3] * inv)), nosent(pk_bf16(d1[0] * inv, d1[1] * inv)), nosent(pk_bf16(d1[2] * inv, d1[3] * inv))};
                        st16_sc1(v, rc1, ((g >> 1) * 16 + 2 * l16 + (g & 1)) * 16, physo(s * 16384 + h * 512) + side1);
                        if (g >= 2) {
                            u32x2 w = {nosent(pk_bf16(dm[0] * inv, dm[1] * inv)), nosent(pk_bf16(dm[2] * inv, dm[3] * inv))};
                            st8_sc1(w, rc0, (2 * l16 + g - 2) * 8, physo(PM_BASE + s * 4096 + h * 128) + side0);
                        }
                    }
                }
            } else {
                const int tt = wave - 1;
#pragma unroll
                for (int ks = 0; ks < 4; ++ks) {
                    const u32x4 qf = lds_ld16(qimg, ((4 * ks + g) * 32 + 16 * tt + l16) * 16);
                    d0 = mf16(lds_ld16(kimg, ((4 * ks + g) * 32 + l16) * 16), qf, d0);
                    if (tt) d1 = mf16(lds_ld16(kimg, ((4 * ks + g) * 32 + 16 + l16) * 16), qf, d1);
                    dm = mf16(F[ks], qf, dm);
                }
                round_scores(d0); round_scores(d1); round_scores(dm);
                const int tok = 16 * tt + l16;
#pragma unroll
                for (int r = 0; r < 4; ++r) { if (4 * g + r > tok) d0[r] = MASKV; if (16 + 4 * g + r > tok) d1[r] = MASKV; }
                float mx = fmaxf(max4(d0), max4(d1));
                if (g >= 2) mx = fmaxf(mx, max4(dm));
                mx = xmax16_32(mx);
                exp4(d0, mx); exp4(d1, mx); exp4(dm, mx);
                float sm = sum4(d0) + sum4(d1);
                if (g >= 2) sm += sum4(dm);
                sm = xsum16_32(sm);
                const float inv = 1.0f / sm;
                u32x4 v = {pk_bf16(d0[0] * inv, d0[1] * inv), pk_bf16(d0[2] * inv, d0[3] * inv), pk_bf16(d1[0] * inv, d1[1] * inv), pk_bf16(d1[2] * inv, d1[3] * inv)};
                st16_sc1(v, rpx, lane * 16, ((s * H + h) * 2 + tt) * 1024);
                if (g >= 2) {
                    st4_sc1(pk_bf16(dm[0] * inv, dm[1] * inv), rpx, tok * 16 + (g - 2) * 4, 4 * MB + (s * H + h) * 512);
                    st4_sc1(pk_bf16(dm[2] * inv, dm[3] * inv), rpx, tok * 16 + 8 + (g - 2) * 4, 4 * MB + (s * H + h) * 512);
                }
            }
        }
        if (s + 1 < NSEG) { char* nq = qk + ((s + 1) & 1) * 16384; lds_st16(nq, tid * 16, pa); lds_st16(nq, 8192 + tid * 16, pb); }
    }
}

DEV void role_vo(const Params& p, int h, char* lds) {
    const int tid = threadIdx.x, lane = tid & 63, wave = __builtin_amdgcn_readfirstlane(tid >> 6);
    const int g = lane >> 4, l16 = lane & 15;
    unsigned* ctrl = (unsigned*)(p.ws + OFF_CTRL);
    bf16x16 wv[10], wo[5][2];
#pragma unroll
    for (int bt = 0; bt < 5; ++bt) {
        char* img = lds + (bt & 1) * 65536;
        if (bt == 0) {
            stage_img(p.wv + h * DH, H * DH, 128, img, wave, lane);
            stage_img(p.wo + (size_t)h * DH * D, D, 128, img + 32768, wave, lane);
        }
        if (bt + 1 < 5) {
            char* nx = lds + ((bt + 1) & 1) * 65536;
            stage_img(p.wv + (size_t)(bt + 1) * 128 * H * DH + h * DH, H * DH, 128, nx, wave, lane);
            stage_img(p.wo + (size_t)h * DH * D + (bt + 1) * 128, D, 128, nx + 32768, wave, lane);
            WAIT_VM(8);
        } else WAIT_VM(0);
        raw_barrier();
#pragma unroll
        for (int u = 0; u < 2; ++u) { wv[2 * bt + u] = load_bsp(img, 64 * u, wave, lane); wo[bt][u] = load_bsp(img + 32768, 64 * u, wave, lane); }
        raw_barrier();
    }
    const unsigned side0 = __builtin_amdgcn_readfirstlane(ctrl[0]) << 13;
    const __amdgpu_buffer_rsrc_t rc0 = mkrsrc(p.ws + OFF_COMM0, 2 * MB);
    const __amdgpu_buffer_rsrc_t rt = mkrsrc(p.ws + OFF_T, 20 * MB);
    char* mimg = lds;
    char* vimg = lds + 10240;
    const int idx = (lane & 1) ? 0xEEEE : 0x4444;
    int cap = SPIN_CAP;
    for (int s = 0; s < NSEG; ++s) {
        gather_mem(rc0, s, side0, mimg, wave, lane, ctrl + 16, cap);
        raw_barrier();
        f32x4 av = {};
#pragma unroll
        for (int st = 0; st < 10; ++st) av = smf(lds_ld16(mimg, st * 1024 + lane * 16), wv[st], av, idx);
        {
            const unsigned e = 16 * wave + l16, t = (e >> 1) & 1, rho = 2 * (e >> 2) + (e & 1);
            const unsigned pk = pk_bf16(av[0] + av[1], av[2] + av[3]);
            *(unsigned short*)(vimg + rho * 32 + (4 * g + t) * 2) = (unsigned short)pk;
            *(unsigned short*)(vimg + rho * 32 + (4 * g + 2 + t) * 2) = (unsigned short)(pk >> 16);
        }
        raw_barrier();
        u32x4 A[2];
#pragma unroll
        for (int ks = 0; ks < 2; ++ks) {
            const unsigned r0 = 32 * ks + 8 * g + (l16 >> 2);
            u32x2 a = tr_read(vimg, r0 * 32 + (lane & 3) * 8), b = tr_read(vimg, (r0 + 4) * 32 + (lane & 3) * 8);
            A[ks] = u32x4{a.x, a.y, b.x, b.y};
        }
        unsigned tw[5];
#pragma unroll
        for (int bt = 0; bt < 5; ++bt) {
            f32x4 d = {};
            d = smf(A[0], wo[bt][0], d, idx); d = smf(A[1], wo[bt][1], d, idx);
            tw[bt] = nosent(pk_bf16(d[0] + d[1], d[2] + d[3]));
        }
        {
            auto a = __builtin_amdgcn_permlane16_swap(tw[0], tw[1], false, false);
            auto b = __builtin_amdgcn_permlane16_swap(tw[2], tw[3], false, false);
            auto c = __builtin_amdgcn_permlane32_swap(a[0], b[0], false, false);
            auto d = __builtin_amdgcn_permlane32_swap(a[1], b[1], false, false);
            u32x4 v = {c[0], c[1], d[0], d[1]};
            st16_sc1(v, rt, (8 * g + wave) * 8192 + l16 * 16, (s * NCT * H + h) * 256);
            auto e = __builtin_amdgcn_permlane16_swap(tw[4], tw[4], false, false);
            auto f = __builtin_amdgcn_permlane32_swap(e[0], e[0], false, false);
            auto k = __builtin_amdgcn_permlane32_swap(e[1], e[1], false, false);
            u32x4 v4 = {f[0], f[1], k[0], k[1]};
            if (g == 0) st16_sc1(v4, rt, (32 + wave) * 8192 + l16 * 16, (s * NCT * H + h) * 256);
        }
    }
}

DEV void role_comb(const Params& p, int c, char* lds) {
    const int tid = threadIdx.x, lane = tid & 63, wave = __builtin_amdgcn_readfirstlane(tid >> 6);
    const int g = lane >> 4, l16 = lane & 15;
    unsigned* ctrl = (unsigned*)(p.ws + OFF_CTRL);
    const unsigned side0 = __builtin_amdgcn_readfirstlane(ctrl[0]) << 13, side1 = __builtin_amdgcn_readfirstlane(ctrl[1]) << 13;
    const __amdgpu_buffer_rsrc_t rc0 = mkrsrc(p.ws + OFF_COMM0, 2 * MB), rc1 = mkrsrc(p.ws + OFF_COMM1, 2 * MB);
    const __amdgpu_buffer_rsrc_t rt = mkrsrc(p.ws + OFF_T, 20 * MB), ru = mkrsrc(p.ws + OFF_U, 80 * MB);
    const int idx = (lane & 1) ? 0xEEEE : 0x4444;
    int cap = SPIN_CAP;
    float* red = (float*)lds;
    u32x4 uf[4];
#pragma unroll
    for (int j = 0; j < 4; ++j) uf[j] = ld16_sc1(ru, lane * 16 + (4 * wave + j) * 1024, c * 32768);
    for (int s = 0; s + 1 < NSEG; ++s) {
        u32x4 px0, px1, tf; u32x2 pm0 = {0, 0}, pm1 = {0, 0};
        const unsigned ox = physo(s * 16384 + wave * 2048) + side1, om = physo(PM_BASE + s * 4096 + wave * 512) + side0;
        const unsigned ot = ((s * NCT + c) * H + 4 * wave) * 256;
        for (int sp = 0;; ++sp) {
            asm volatile("" ::: "memory");
            tf = ld16_sc1(rt, lane * 16, ot);
            px0 = ld16_sc1(rc1, lane * 16, ox); px1 = ld16_sc1(rc1, 1024 + lane * 16, ox);
            if (g < 2) { pm0 = ld8_sc1(rc0, g * 256 + l16 * 8, om); pm1 = ld8_sc1(rc0, g * 256 + 128 + l16 * 8, om); }
            unsigned m = max(max(mx4(px0), mx4(px1)), max(mx4(tf), max(max(pm0.x, pm0.y), max(pm1.x, pm1.y))));
            if (__all(m != SENT)) break;
            if (sp > cap) { ctrl[16] = 1; cap = 0; break; }
        }
        f32x4 d = {};
        {
            u32x8 b0 = {uf[0].x, uf[0].y, uf[0].z, uf[0].w, uf[1].x, uf[1].y, uf[1].z, uf[1].w};
            u32x8 b1 = {uf[2].x, uf[2].y, uf[2].z, uf[2].w, uf[3].x, uf[3].y, uf[3].z, uf[3].w};
            u32x8 b2 = {tf.x, tf.y, tf.z, tf.w, 0, 0, 0, 0};
            d = smf(px0, __builtin_bit_cast(bf16x16, b0), d, idx);
            d = smf(px1, __builtin_bit_cast(bf16x16, b1), d, idx);
            d = smf(u32x4{pm0.x, pm0.y, pm1.x, pm1.y}, __builtin_bit_cast(bf16x16, b2), d, idx);
        }
        red[wave * 128 + (2 * g) * 16 + l16] = d[0] + d[1];
        red[wave * 128 + (2 * g + 1) * 16 + l16] = d[2] + d[3];
        raw_barrier();
        if (wave == 0) {
            const int m = lane >> 3, t = (lane >> 2) & 1, i = lane & 3;
            f32x2 acc = {0.f, 0.f};
#pragma unroll
            for (int w = 0; w < 8; ++w) acc += *(const f32x2*)(red + w * 128 + m * 16 + 4 * i + 2 * t);
            const unsigned pk = nosent(pk_bf16(acc[0], acc[1]));
            u32x4 v;
            v.x = pk;
            v.y = __builtin_amdgcn_update_dpp(0, pk, 0x55, 0xf, 0xf, false);
            v.z = __builtin_amdgcn_update_dpp(0, pk, 0xAA, 0xf, 0xf, false);
            v.w = __builtin_amdgcn_update_dpp(0, pk, 0xFF, 0xf, 0xf, false);
            if (i == 0) st16_sc1(v, rc0, (lane >> 2) * 16, physo((s + 1) * MEM_SLOT + c * 256) + side0);
        }
        if (s + 2 < NSEG) {
#pragma unroll
            for (int j = 0; j < 4; ++j) uf[j] = ld16_sc1(ru, lane * 16 + (4 * wave + j) * 1024, ((s + 1) * NCT + c) * 32768);
        }
        raw_barrier();
    }
}

__global__ void __launch_bounds__(512, 2) k_chain(Params p) {
    extern __shared__ __attribute__((aligned(16))) char lds[];
    int b = blockIdx.x;
    if ((b & 7) >= 4) return;
    b = (b >> 3) * 4 + (b & 7);
    if (b < 32) role_qk(p, b, lds);
    else if (b < 64) role_vo(p, b - 32, lds);
    else if (b < 104) role_comb(p, b - 64, lds);
}

DEV void task_out(const Params& p, int s, int cg, char* lds) {
    const int tid = threadIdx.x, lane = tid & 63, wave = __builtin_amdgcn_readfirstlane(tid >> 6);
    const int g = lane >> 4, l16 = lane & 15;
    const __amdgpu_buffer_rsrc_t rpx = mkrsrc(p.ws + OFF_PX, 5 * MB);
    const __amdgpu_buffer_rsrc_t rt = mkrsrc(p.ws + OFF_T, 20 * MB), ru = mkrsrc(p.ws + OFF_U, 80 * MB);
    {
        u32x4 v[10];
#pragma unroll
        for (int i = 0; i < 8; ++i) v[i] = ld16_sc1(rpx, tid * 16 + i * 8192, s * 65536);
#pragma unroll
        for (int i = 0; i < 2; ++i) v[8 + i] = ld16_sc1(rpx, tid * 16 + i * 8192, 4 * MB + s * 16384);
#pragma unroll
        for (int i = 0; i < 10; ++i) lds_st16(lds, tid * 16 + i * 8192, v[i]);
    }
    const int c = cg * 8 + wave;
    f32x4 d0 = {}, d1 = {};
    u32x4 tf[8];
#pragma unroll
    for (int j = 0; j < 8; ++j) tf[j] = ld16_sc1(rt, lane * 16 + j * 1024, (s * NCT + c) * 8192);
    u32x4 uf[16];
#pragma unroll
    for (int j = 0; j < 16; ++j) uf[j] = ld16_sc1(ru, lane * 16 + j * 1024, (s * NCT + c) * 32768);
    __syncthreads();
#pragma unroll
    for (int j = 0; j < 8; ++j) {
        d0 = mf16(tf[j], lds_ld16(lds, 65536 + (4 * j + g) * 512 + l16 * 16), d0);
        d1 = mf16(tf[j], lds_ld16(lds, 65536 + (4 * j + g) * 512 + 256 + l16 * 16), d1);
    }
#pragma unroll
    for (int hb = 0; hb < 2; ++hb) {
#pragma unroll
        for (int j = 0; j < 16; ++j) {
            const int hh = hb * 16 + j;
            d0 = mf16(uf[j], lds_ld16(lds, hh * 2048 + lane * 16), d0);
            d1 = mf16(uf[j], lds_ld16(lds, hh * 2048 + 1024 + lane * 16), d1);
            if (hb == 0) uf[j] = ld16_sc1(ru, lane * 16 + (16 + j) * 1024, (s * NCT + c) * 32768);
        }
    }
    unsigned short* o = p.out + (size_t)(s * 32 + l16) * (2 * D) + D + c * 16 + 4 * g;
    *(u32x2*)o = u32x2{pk_bf16(d0[0], d0[1]), pk_bf16(d0[2], d0[3])};
    *(u32x2*)(o + 16 * 2 * D) = u32x2{pk_bf16(d1[0], d1[1]), pk_bf16(d1[2], d1[3])};
    __syncthreads();
}
__global__ void __launch_bounds__(512, 2) k_out(Params p) {
    extern __shared__ __attribute__((aligned(16))) char lds[];
    task_out(p, blockIdx.x / 5, blockIdx.x % 5, lds);
}

struct cf { float x, y; };
DEV cf cmul(cf a, cf b) { return cf{a.x * b.x - a.y * b.y, a.x * b.y + a.y * b.x}; }
DEV cf cmulc(cf a, cf b) { return cf{a.x * b.x + a.y * b.y, a.y * b.x - a.x * b.y}; }
DEV cf cadd(cf a, cf b) { return cf{a.x + b.x, a.y + b.y}; }
DEV cf csub(cf a, cf b) { return cf{a.x - b.x, a.y - b.y}; }
template <int SG> DEV cf mulmi(cf a) { return SG < 0 ? cf{a.y, -a.x} : cf{-a.y, a.x}; }
template <int SG> DEV void dft4(cf& a, cf& b, cf& c, cf& d) {
    cf t0 = cadd(a, c), t1 = csub(a, c), t2 = cadd(b, d), t3 = mulmi<SG>(csub(b, d));
    a = cadd(t0, t2); c = csub(t0, t2); b = cadd(t1, t3); d = csub(t1, t3);
}
template <int SG> DEV void dft8(cf* v) {
    constexpr float R = 0.70710678118654752f;
    dft4<SG>(v[0], v[2], v[4], v[6]);
    dft4<SG>(v[1], v[3], v[5], v[7]);
    cf o1 = cf{(v[3].x - SG * v[3].y) * R, (v[3].y + SG * v[3].x) * R};
    cf o2 = mulmi<SG>(v[5]);
    cf o3 = cf{(-v[7].x - SG * v[7].y) * R, (-v[7].y + SG * v[7].x) * R};
    cf e0 = v[0], e1 = v[2], e2 = v[4], e3 = v[6], o0 = v[1];
    v[0] = cadd(e0, o0); v[4] = csub(e0, o0);
    v[1] = cadd(e1, o1); v[5] = csub(e1, o1);
    v[2] = cadd(e2, o2); v[6] = csub(e2, o2);
    v[3] = cadd(e3, o3); v[7] = csub(e3, o3);
}
template <int SG> DEV void dft16(cf* v) {
    constexpr float C1 = 0.92387953251128674f, S1 = 0.38268343236508977f, R = 0.70710678118654752f;
#pragma unroll
    for (int b = 0; b < 4; ++b) dft4<SG>(v[b], v[4 + b], v[8 + b], v[12 + b]);
    const cf w1 = {C1, SG * S1}, w2 = {R, SG * R}, w3 = {S1, SG * C1}, w6 = {-R, SG * R}, w9 = {-C1, -SG * S1};
    v[5] = cmul(v[5], w1); v[6] = cmul(v[6], w2); v[7] = cmul(v[7], w3);
    v[9] = cmul(v[9], w2); v[10] = mulmi<SG>(v[10]); v[11] = cmul(v[11], w6);
    v[13] = cmul(v[13], w3); v[14] = cmul(v[14], w6); v[15] = cmul(v[15], w9);
#pragma unroll
    for (int c = 0; c < 4; ++c) dft4<SG>(v[4 * c], v[4 * c + 1], v[4 * c + 2], v[4 * c + 3]);
    cf t;
    t = v[1]; v[1] = v[4]; v[4] = t;  t = v[2]; v[2] = v[8]; v[8] = t;  t = v[3]; v[3] = v[12]; v[12] = t;
    t = v[6]; v[6] = v[9]; v[9] = t;  t = v[7]; v[7] = v[13]; v[13] = t; t = v[11]; v[11] = v[14]; v[14] = t;
}
template <int SG> DEV void dft5(cf* v) {
    constexpr float C1 = 0.30901699437494742f, C2 = -0.80901699437494742f, S1 = 0.95105651629515357f, S2 = 0.58778525229247313f;
    cf a1 = cadd(v[1], v[4]), b1 = csub(v[1], v[4]), a2 = cadd(v[2], v[3]), b2 = csub(v[2], v[3]);
    cf r1 = {v[0].x + C1 * a1.x + C2 * a2.x, v[0].y + C1 * a1.y + C2 * a2.y};
    cf r2 = {v[0].x + C2 * a1.x + C1 * a2.x, v[0].y + C2 * a1.y + C1 * a2.y};
    cf i1 = mulmi<SG>(cf{S1 * b1.x + S2 * b2.x, S1 * b1.y + S2 * b2.y});
    cf i2 = mulmi<SG>(cf{S2 * b1.x - S1 * b2.x, S2 * b1.y - S1 * b2.y});
    v[0] = cadd(v[0], cadd(a1, a2));
    v[1] = cadd(r1, i1); v[4] = csub(r1, i1); v[2] = cadd(r2, i2); v[3] = csub(r2, i2);
}
DEV cf twid(int num, int den) { float s, c; sincospif(-2.0f * (float)num / (float)den, &s, &c); return cf{c, s}; }

constexpr int RP = 648;
template <int SG> DEV void fft640x8(cf* a, cf* b, const cf* tab, int tid) {
    if (tid < 320) {
        const int q = tid / 40, j = tid % 40; cf v[16];
#pragma unroll
        for (int r = 0; r < 16; ++r) v[r] = a[q * RP + j + 40 * r];
        dft16<SG>(v);
#pragma unroll
        for (int r = 0; r < 16; ++r) b[q * RP + 16 * j + (r ^ (j & 15))] = v[r];
    }
    __syncthreads();
    for (int t = tid; t < 640; t += 512) {
        const int q = t / 80, j = t % 80, k = j & 15; cf v[8];
#pragma unroll
        for (int r = 0; r < 8; ++r) { const int n = j + 80 * r; v[r] = b[q * RP + (n & ~15) + ((n & 15) ^ ((n >> 4) & 15))]; }
#pragma unroll
        for (int r = 1; r < 8; ++r) { const cf w = tab[5 * k * r]; v[r] = SG < 0 ? cmul(v[r], w) : cmulc(v[r], w); }
        dft8<SG>(v);
#pragma unroll
        for (int r = 0; r < 8; ++r) a[q * RP + (j >> 4) * 128 + k + 16 * r] = v[r];
    }
    __syncthreads();
    for (int t = tid; t < 1024; t += 512) {
        const int q = t >> 7, j = t & 127; cf v[5];
#pragma unroll
        for (int r = 0; r < 5; ++r) v[r] = a[q * RP + j + 128 * r];
#pragma unroll
        for (int r = 1; r < 5; ++r) { const cf w = tab[j * r]; v[r] = SG < 0 ? cmul(v[r], w) : cmulc(v[r], w); }
        dft5<SG>(v);
#pragma unroll
        for (int r = 0; r < 5; ++r) b[q * RP + j + 128 * r] = v[r];
    }
    __syncthreads();
}
DEV void make_tab640(cf* tab, int tid) { for (int i = tid; i < 640; i += 512) tab[i] = twid(i, 640); }

DEV void task_fft1(const Params& p, int t, char* lds) {
    const int tid = threadIdx.x;
    cf* a = (cf*)lds; cf* b = a + 8 * RP; cf* tab = b + 8 * RP;
    make_tab640(tab, tid);
    for (int i = tid; i < 8 * 80; i += 512) {
        const int q = i / 80, ch = i % 80;
        const u32x4 u = *(const u32x4*)(p.ax + (size_t)(16 * t + 2 * q) * D + ch * 8), w = *(const u32x4*)(p.ax + (size_t)(16 * t + 2 * q + 1) * D + ch * 8);
        cf* d = a + q * RP + ch * 8;
        d[0] = cf{bf_lo(u.x), bf_lo(w.x)}; d[1] = cf{bf_hi(u.x), bf_hi(w.x)}; d[2] = cf{bf_lo(u.y), bf_lo(w.y)}; d[3] = cf{bf_hi(u.y), bf_hi(w.y)};
        d[4] = cf{bf_lo(u.z), bf_lo(w.z)}; d[5] = cf{bf_hi(u.z), bf_hi(w.z)}; d[6] = cf{bf_lo(u.w), bf_lo(w.w)}; d[7] = cf{bf_hi(u.w), bf_hi(w.w)};
    }
    __syncthreads();
    fft640x8<-1>(a, b, tab, tid);
    const __amdgpu_buffer_rsrc_t rx = mkrsrc(p.ws + OFF_X1, 6 * MB);
    for (int i = tid; i < DF * 8; i += 512) {
        const int k = i >> 3, q = i & 7;
        const cf z = b[q * RP + k], y = b[q * RP + (k ? 640 - k : 0)];
        f32x4 o = {0.5f * (z.x + y.x), 0.5f * (z.y - y.y), 0.5f * (z.y + y.y), 0.5f * (y.x - z.x)};
        st16_sc1(__builtin_bit_cast(u32x4, o), rx, (k * S + 16 * t + 2 * q) * 8, 0);
    }
    unsigned* wt = (unsigned*)a;
    for (int i = tid; i < 16 * DF; i += 512) { const int r = i / DF, k = i % DF; wt[k * 17 + r] = (unsigned)p.wr[(size_t)(16 * t + r) * DF + k] | ((unsigned)p.wi[(size_t)(16 * t + r) * DF + k] << 16); }
    __syncthreads();
    const __amdgpu_buffer_rsrc_t rw = mkrsrc(p.ws + OFF_WT, 3 * MB);
    for (int i = tid; i < DF * 4; i += 512) { const int k = i >> 2, r4 = (i & 3) * 4;
        u32x4 v = {wt[k * 17 + r4], wt[k * 17 + r4 + 1], wt[k * 17 + r4 + 2], wt[k * 17 + r4 + 3]};
        st16_sc1(v, rw, (k * S + 16 * t + r4) * 4, 0); }
    __syncthreads();
}

constexpr int CP = 2048 + 128;
DEV int cpad(int i) { return i + (i >> 4); }
DEV void task_fft2(const Params& p, int t, char* lds) {
    const int tid = threadIdx.x, col = tid >> 7, m = tid & 127;
    const int k = 4 * t + col;
    const bool act = k < DF;
    cf* buf = (cf*)lds + col * CP;
    const __amdgpu_buffer_rsrc_t rx = mkrsrc(p.ws + OFF_X1, (unsigned)(DF * S * 8)), ry = mkrsrc(p.ws + OFF_Y1, (unsigned)(DF * S * 8)), rw = mkrsrc(p.ws + OFF_WT, (unsigned)(DF * S * 4));
    cf v[16];
#pragma unroll
    for (int n0 = 0; n0 < 16; ++n0) { u32x2 u = ld8_sc1(rx, (k * S + 128 * n0 + m) * 8, 0); v[n0] = __builtin_bit_cast(cf, u); }
    const int f0b = m & 15, n2 = m >> 4;
    const int f1c = m >> 4;
    unsigned wv[16];
#pragma unroll
    for (int u = 0; u < 2; ++u)
#pragma unroll
        for (int f2 = 0; f2 < 8; ++f2) wv[u * 8 + f2] = __builtin_amdgcn_raw_buffer_load_b32(rw, (k * S + f0b + 16 * (f1c + 8 * u) + 256 * f2) * 4, 0, 16);
    cf t1[16], t2[16];
    t1[1] = twid(m, 2048); t2[1] = twid(n2, 128);
#pragma unroll
    for (int i = 2; i < 16; ++i) { t1[i] = twid(m * i, 2048); t2[i] = twid(n2 * i, 128); }
    dft16<-1>(v);
#pragma unroll
    for (int f0 = 1; f0 < 16; ++f0) v[f0] = cmul(v[f0], t1[f0]);
#pragma unroll
    for (int f0 = 0; f0 < 16; ++f0) buf[cpad(m * 16 + f0)] = v[f0];
    __syncthreads();
#pragma unroll
    for (int n1 = 0; n1 < 16; ++n1) v[n1] = buf[cpad((8 * n1 + n2) * 16 + f0b)];
    dft16<-1>(v);
#pragma unroll
    for (int f1 = 1; f1 < 16; ++f1) v[f1] = cmul(v[f1], t2[f1]);
    __syncthreads();
#pragma unroll
    for (int f1 = 0; f1 < 16; ++f1) buf[cpad((f1 * 8 + n2) * 16 + f0b)] = v[f1];
    __syncthreads();
#pragma unroll
    for (int u = 0; u < 2; ++u)
#pragma unroll
        for (int j = 0; j < 8; ++j) v[u * 8 + j] = buf[cpad(((f1c + 8 * u) * 8 + j) * 16 + f0b)];
    dft8<-1>(v); dft8<-1>(v + 8);
    constexpr float NRM = 1.0f / (2048.0f * 640.0f);
#pragma unroll
    for (int i = 0; i < 16; ++i) v[i] = cmul(v[i], cf{bf_lo(wv[i]) * NRM, bf_hi(wv[i]) * NRM});
    dft8<1>(v); dft8<1>(v + 8);
#pragma unroll
    for (int u = 0; u < 2; ++u)
#pragma unroll
        for (int j = 0; j < 8; ++j) buf[cpad(((f1c + 8 * u) * 8 + j) * 16 + f0b)] = v[u * 8 + j];
    __syncthreads();
#pragma unroll
    for (int f1 = 0; f1 < 16; ++f1) v[f1] = buf[cpad((f1 * 8 + n2) * 16 + f0b)];
#pragma unroll
    for (int f1 = 1; f1 < 16; ++f1) v[f1] = cmulc(v[f1], t2[f1]);
    dft16<1>(v);
#pragma unroll
    for (int n1 = 0; n1 < 16; ++n1) buf[cpad((8 * n1 + n2) * 16 + f0b)] = v[n1];
    __syncthreads();
#pragma unroll
    for (int f0 = 0; f0 < 16; ++f0) v[f0] = buf[cpad(m * 16 + f0)];
#pragma unroll
    for (int f0 = 1; f0 < 16; ++f0) v[f0] = cmulc(v[f0], t1[f0]);
    dft16<1>(v);
    if (act) {
#pragma unroll
        for (int n0 = 0; n0 < 16; ++n0) st8_sc1(__builtin_bit_cast(u32x2, v[n0]), ry, (k * S + 128 * n0 + m) * 8, 0);
    }
    __syncthreads();
}

DEV void task_fft3(const Params& p, int t, char* lds) {
    const int tid = threadIdx.x;
    cf* a = (cf*)lds; cf* b = a + 8 * RP; cf* tab = b + 8 * RP;
    make_tab640(tab, tid);
    const __amdgpu_buffer_rsrc_t ry = mkrsrc(p.ws + OFF_Y1, 6 * MB);
    for (int i = tid; i < DF * 8; i += 512) {
        const int k = i >> 3, q = i & 7;
        f32x4 o = __builtin_bit_cast(f32x4, ld16_sc1(ry, (k * S + 16 * t + 2 * q) * 8, 0));
        if (k == 0 || k == 320) { o[1] = 0.f; o[3] = 0.f; }
        a[q * RP + k] = cf{o[0] - o[3], o[1] + o[2]};
        if (k > 0 && k < 320) a[q * RP + 640 - k] = cf{o[0] + o[3], o[2] - o[1]};
    }
    __syncthreads();
    fft640x8<1>(a, b, tab, tid);
    for (int i = tid; i < 8 * 80; i += 512) {
        const int q = i / 80, ch = i % 80;
        const cf* d = b + q * RP + ch * 8;
        u32x4 u = {pk_bf16(d[0].x, d[1].x), pk_bf16(d[2].x, d[3].x), pk_bf16(d[4].x, d[5].x), pk_bf16(d[6].x, d[7].x)};
        u32x4 w = {pk_bf16(d[0].y, d[1].y), pk_bf16(d[2].y, d[3].y), pk_bf16(d[4].y, d[5].y), pk_bf16(d[6].y, d[7].y)};
        *(u32x4*)(p.out + (size_t)(16 * t + 2 * q) * (2 * D) + ch * 8) = u;
        *(u32x4*)(p.out + (size_t)(16 * t + 2 * q + 1) * (2 * D) + ch * 8) = w;
    }
    __syncthreads();
}
__global__ void __launch_bounds__(512, 2) k_fft1(Params p) { extern __shared__ __attribute__((aligned(16))) char lds[]; task_fft1(p, blockIdx.x, lds); }
__global__ void __launch_bounds__(512, 2) k_fft2(Params p) { extern __shared__ __attribute__((aligned(16))) char lds[]; task_fft2(p, blockIdx.x, lds); }
__global__ void __launch_bounds__(512, 2) k_fft3(Params p) { extern __shared__ __attribute__((aligned(16))) char lds[]; task_fft3(p, blockIdx.x, lds); }

extern "C" void kernel_launch(void* const* d_in, const int* in_sizes, int n_in, void* d_out, int out_size, void* d_ws, size_t ws_size, hipStream_t stream) {
    Params p;
    p.ax = (const unsigned short*)d_in[0]; p.wr = (const unsigned short*)d_in[1]; p.wi = (const unsigned short*)d_in[2];
    p.bx = (const unsigned short*)d_in[3]; p.mem0 = (const unsigned short*)d_in[4];
    p.wq = (const unsigned short*)d_in[5]; p.wk = (const unsigned short*)d_in[6]; p.wv = (const unsigned short*)d_in[7]; p.wo = (const unsigned short*)d_in[8];
    p.out = (unsigned short*)d_out;
    p.ws = (char*)(((size_t)d_ws + 2 * MB - 1) & ~(size_t)(2 * MB - 1));
    constexpr int LDS = 131072;
    (void)hipFuncSetAttribute((const void*)k_gemm, hipFuncAttributeMaxDynamicSharedMemorySize, LDS);
    (void)hipFuncSetAttribute((const void*)k_chain, hipFuncAttributeMaxDynamicSharedMemorySize, LDS);
    (void)hipFuncSetAttribute((const void*)k_out, hipFuncAttributeMaxDynamicSharedMemorySize, LDS);
    (void)hipFuncSetAttribute((const void*)k_fft1, hipFuncAttributeMaxDynamicSharedMemorySize, LDS);
    (void)hipFuncSetAttribute((const void*)k_fft2, hipFuncAttributeMaxDynamicSharedMemorySize, LDS);
    (void)hipFuncSetAttribute((const void*)k_fft3, hipFuncAttributeMaxDynamicSharedMemorySize, LDS);
    k_prep<<<512, 256, 0, stream>>>(p);
    k_gemm<<<128, 512, LDS, stream>>>(p);
    k_fft1<<<128, 512, 90112, stream>>>(p);
    k_fft2<<<81, 512, 4 * CP * 8, stream>>>(p);
    k_fft3<<<128, 512, 90112, stream>>>(p);
    k_chain<<<256, 512, LDS, stream>>>(p);
    k_out<<<320, 512, 81920, stream>>>(p);
}
```

```cpp
#include <hip/hip_runtime.h>
#include <hip/hip_bf16.h>

typedef __bf16 bf16x8 __attribute__((ext_vector_type(8)));
typedef __bf16 bf16x16 __attribute__((ext_vector_type(16)));
typedef __bf16 bf16x2 __attribute__((ext_vector_type(2)));
typedef short s16x4 __attribute__((ext_vector_type(4)));
typedef float f32x2 __attribute__((ext_vector_type(2)));
typedef float f32x4 __attribute__((ext_vector_type(4)));
typedef float f32x16 __attribute__((ext_vector_type(16)));
typedef unsigned u32x2 __attribute__((ext_vector_type(2)));
typedef unsigned u32x4 __attribute__((ext_vector_type(4)));
typedef unsigned u32x8 __attribute__((ext_vector_type(8)));
#define LAS __attribute__((address_space(3)))
#define GAS __attribute__((address_space(1)))
#define DEV __device__ __forceinline__

constexpr int S = 2048, D = 640, DF = 321, H = 32, DH = 128, NSEG = 64, NCT = 40;
constexpr unsigned SENT = 0xFFFFFFFFu;
constexpr int SPIN_CAP = 1 << 15;

constexpr size_t MB = 1 << 20;
constexpr size_t OFF_CTRL = 0;
constexpr size_t OFF_COMM0 = 2 * MB;
constexpr size_t OFF_QX = 6 * MB;
constexpr size_t OFF_KX = OFF_QX + 16 * MB;
constexpr size_t OFF_U = OFF_KX + 16 * MB;
constexpr size_t OFF_T = OFF_U + 80 * MB;
constexpr int NTPG = 22;
constexpr size_t OFF_PX = OFF_T + 44 * MB;
constexpr size_t OFF_PXM = OFF_PX + 4 * MB;
constexpr size_t OFF_X1 = OFF_PXM + 1 * MB;
constexpr size_t OFF_Y1 = OFF_X1 + 6 * MB;
constexpr size_t OFF_WT = OFF_Y1 + 6 * MB;
constexpr size_t WS_NEED = OFF_WT + 3 * MB;

constexpr int RM = 4, RP = 2;
DEV unsigned mema_off(int sl, int r) { return (sl * 4 + r) * 8192; }
DEV unsigned memb_off(int sl, int r) { return (16 + sl) * 8192 + r * 2048; }
DEV unsigned mem_off(int sl, int r, int c) { return c < 32 ? mema_off(sl, r) + c * 256 : memb_off(sl, r) + (c - 32) * 256; }
DEV unsigned px_base(int sl, int r) { return (20 + (sl * 2 + r) * 2) * 8192; }
DEV unsigned pm_base(int sl, int r) { return (36 + sl) * 8192 + r * 4096; }
constexpr unsigned ANN_BASE = 40 * 8192, STA_BASE = 41 * 8192, STB_BASE = 42 * 8192, COMM_CHUNKS = 44;
DEV unsigned rowi(unsigned m, unsigned t) { return 4 * (m >> 1) + 2 * t + (m & 1); }
DEV unsigned physo(unsigned o) { return ((o >> 13) << 14) + (o & 8191u); }
DEV unsigned t_off(int s, unsigned long long tmask) { const int pg = s / 3; return pg * (2u << 20) + (s - 3 * pg) * (NCT * 16384) + (((unsigned)(tmask >> pg) & 1u) << 13); }
constexpr int FL_TNEAR = 0, FL_TFAR = 1, FL_QUEUE = 2, FL_READY = 3, FL_F1 = 4, FL_F2 = 5, FL_TMO = 6, FL_QOUT = 7, FL_SIDE = 8, FL_GQ = 32, FL_GU = 96, FL_PX = 160, FL_CB = 224, FL_END = 264;
constexpr int FLAG_BYTES = FL_END * 128;
constexpr int TUNE_W = 16384;
struct Params {
    const unsigned short* ax; const unsigned short* wr; const unsigned short* wi;
    const unsigned short* bx; const unsigned short* mem0;
    const unsigned short* wq; const unsigned short* wk; const unsigned short* wv; const unsigned short* wo;
    unsigned short* out; char* ws;
};

DEV __amdgpu_buffer_rsrc_t mkrsrc(const void* p, unsigned bytes) { return __builtin_amdgcn_make_buffer_rsrc((void*)p, 0, bytes, 0x00020000); }
DEV u32x4 ld16_sc1(__amdgpu_buffer_rsrc_t r, unsigned voff, unsigned soff) { return __builtin_amdgcn_raw_buffer_load_b128(r, voff, soff, 16); }
DEV u32x2 ld8_sc1(__amdgpu_buffer_rsrc_t r, unsigned voff, unsigned soff) { return __builtin_amdgcn_raw_buffer_load_b64(r, voff, soff, 16); }
DEV void st16_sc1(u32x4 v, __amdgpu_buffer_rsrc_t r, unsigned voff, unsigned soff) { __builtin_amdgcn_raw_buffer_store_b128(v, r, voff, soff, 16); }
DEV void st8_sc1(u32x2 v, __amdgpu_buffer_rsrc_t r, unsigned voff, unsigned soff) { __builtin_amdgcn_raw_buffer_store_b64(v, r, voff, soff, 16); }
DEV void st4_sc1(unsigned v, __amdgpu_buffer_rsrc_t r, unsigned voff, unsigned soff) { __builtin_amdgcn_raw_buffer_store_b32(v, r, voff, soff, 16); }
DEV unsigned pk_bf16(float a, float b) { f32x2 f = {a, b}; bf16x2 p = __builtin_convertvector(f, bf16x2); return __builtin_bit_cast(unsigned, p); }
DEV float bf_lo(unsigned u) { return __builtin_bit_cast(float, u << 16); }
DEV float bf_hi(unsigned u) { return __builtin_bit_cast(float, u & 0xFFFF0000u); }
DEV unsigned nosent(unsigned u) { return u < 0xFFFFFFFEu ? u : 0xFFFFFFFEu; }
DEV unsigned mx4(u32x4 v) { return max(max(v.x, v.y), max(v.z, v.w)); }
DEV bf16x8 as_bf8(u32x4 v) { return __builtin_bit_cast(bf16x8, v); }
DEV u32x2 tr_read(const char* lds, unsigned off) { s16x4 v = __builtin_amdgcn_ds_read_tr16_b64_v4i16((LAS s16x4*)(lds + off)); return __builtin_bit_cast(u32x2, v); }
DEV u32x4 lds_ld16(const char* lds, unsigned off) { return *(const u32x4*)(lds + off); }
DEV void lds_st16(char* lds, unsigned off, u32x4 v) { *(u32x4*)(lds + off) = v; }
DEV void glds16(const void* g, char* lds) { __builtin_amdgcn_global_load_lds((const GAS unsigned*)g, (LAS unsigned*)lds, 16, 0, 0); }
#define WAIT_VM(n) asm volatile("s_waitcnt vmcnt(" #n ")" ::: "memory")
#define WAIT_LGKM0() asm volatile("s_waitcnt lgkmcnt(0)" ::: "memory")
DEV void raw_barrier() { asm volatile("" ::: "memory"); __builtin_amdgcn_s_waitcnt(0xC07F); __builtin_amdgcn_s_barrier(); asm volatile("" ::: "memory"); }
DEV float max3f(float a, float b, float c) { return __builtin_fmaxf(__builtin_fmaxf(a, b), c); }
template <class T> DEV T lds_vol_ld(const char* p) { return *(volatile LAS T*)p; }
template <class T> DEV void lds_vol_st(char* p, T v) { *(volatile LAS T*)p = v; }

DEV unsigned swz(unsigned row) { return ((row & 3u) << 2) | ((row >> 2) & 3u); }
DEV unsigned img_off(unsigned row, unsigned ch) { return 256u * row + 16u * (ch ^ swz(row)); }
DEV void bdma16(__amdgpu_buffer_rsrc_t r, char* lds, unsigned voff, unsigned soff) { __builtin_amdgcn_raw_ptr_buffer_load_lds(r, (LAS void*)lds, 16, voff, soff, 0, 0); }
template <int NROWS>
DEV void stage_img(__amdgpu_buffer_rsrc_t r, unsigned elem0, unsigned pitch, char* img, int wave, int lane) {
    const unsigned row0 = 4 * wave + (lane >> 4), voff = (row0 * pitch + (((lane & 15) ^ swz(row0)) << 3)) * 2;
#pragma unroll
    for (int it = 0; it < NROWS / 32; ++it) bdma16(r, img + (wave + 8 * it) * 1024, voff, (elem0 + it * 32 * pitch) * 2);
}
DEV u32x2 tr16(const char* img, unsigned r0, unsigned c, int lane) {
    const unsigned q = (lane & 15) >> 2, p = lane & 3;
    return tr_read(img, img_off(r0 + q, 2 * c + (p >> 1)) + 8 * (p & 1));
}
DEV u32x2 tr32(const char* img, unsigned r0, unsigned c, int lane) {
    const unsigned blk = (lane >> 4) & 1, q = (lane & 15) >> 2, p = lane & 3;
    return tr_read(img, img_off(r0 + q, 4 * c + 2 * blk + (p >> 1)) + 8 * (p & 1));
}
DEV bf16x16 load_bsp(const char* img, unsigned rb, unsigned c, int lane) {
    const unsigned g = lane >> 4;
    u32x2 a = tr16(img, rb + 8 * g, c, lane), b = tr16(img, rb + 8 * g + 4, c, lane), d = tr16(img, rb + 32 + 8 * g, c, lane), e = tr16(img, rb + 32 + 8 * g + 4, c, lane);
    u32x8 r = {a.x, a.y, b.x, b.y, d.x, d.y, e.x, e.y};
    return __builtin_bit_cast(bf16x16, r);
}
DEV u32x2 tr16p(const char* img, unsigned k0, unsigned c, int lane) {
    const unsigned q = (lane & 15) >> 2, p = lane & 3;
    const unsigned row = (k0 & ~15u) + 8 * (q >> 1) + 2 * ((k0 & 15u) >> 2) + (q & 1);
    return tr_read(img, img_off(row, 2 * c + (p >> 1)) + 8 * (p & 1));
}
DEV bf16x16 load_bsp_perm(const char* img, unsigned rb, unsigned c, int lane) {
    const unsigned g = lane >> 4;
    u32x2 a = tr16p(img, rb + 8 * g, c, lane), b = tr16p(img, rb + 8 * g + 4, c, lane), d = tr16p(img, rb + 32 + 8 * g, c, lane), e = tr16p(img, rb + 32 + 8 * g + 4, c, lane);
    u32x8 r = {a.x, a.y, b.x, b.y, d.x, d.y, e.x, e.y};
    return __builtin_bit_cast(bf16x16, r);
}
DEV f32x4 smf(u32x4 a, bf16x16 b, f32x4 c, int idx) { return __builtin_amdgcn_smfmac_f32_16x16x64_bf16(as_bf8(a), b, c, idx, 0, 0); }
DEV f32x4 mf16(u32x4 a, u32x4 b, f32x4 c) { return __builtin_amdgcn_mfma_f32_16x16x32_bf16(as_bf8(a), as_bf8(b), c, 0, 0, 0); }
DEV f32x16 mf32(u32x4 a, u32x4 b, f32x16 c) { return __builtin_amdgcn_mfma_f32_32x32x16_bf16(as_bf8(a), as_bf8(b), c, 0, 0, 0); }
DEV float u2f(unsigned u) { return __builtin_bit_cast(float, u); }
DEV unsigned f2u(float f) { return __builtin_bit_cast(unsigned, f); }
DEV float xmax16_32(float x) {
    auto r = __builtin_amdgcn_permlane16_swap(f2u(x), f2u(x), false, false);
    x = fmaxf(u2f(r[0]), u2f(r[1]));
    auto r2 = __builtin_amdgcn_permlane32_swap(f2u(x), f2u(x), false, false);
    return fmaxf(u2f(r2[0]), u2f(r2[1]));
}
DEV float xsum16_32(float x) {
    auto r = __builtin_amdgcn_permlane16_swap(f2u(x), f2u(x), false, false);
    x = u2f(r[0]) + u2f(r[1]);
    auto r2 = __builtin_amdgcn_permlane32_swap(f2u(x), f2u(x), false, false);
    return u2f(r2[0]) + u2f(r2[1]);
}

#define ST(role, s, slot) do {} while (0)
#define MT(x) do {} while (0)
#define MTW() do {} while (0)
#define PACC(acc, a, b) do {} while (0)
#define PDUMP(role, x, y, z, w) do {} while (0)
#define WS(k) do {} while (0)
#define WSDECL do {} while (0)
#define WSDUMP() do {} while (0)
DEV unsigned* flag_ptr(const Params& p, int line) { return (unsigned*)(p.ws + OFF_CTRL) + line * 32; }
DEV void flag_set(const Params& p, int line, unsigned v) { __hip_atomic_store(flag_ptr(p, line), v, __ATOMIC_RELAXED, __HIP_MEMORY_SCOPE_AGENT); }
DEV unsigned flag_add(const Params& p, int line) { return __hip_atomic_fetch_add(flag_ptr(p, line), 1u, __ATOMIC_RELAXED, __HIP_MEMORY_SCOPE_AGENT); }
DEV unsigned flag_get(const Params& p, int line) { return __hip_atomic_load(flag_ptr(p, line), __ATOMIC_RELAXED, __HIP_MEMORY_SCOPE_AGENT); }
DEV unsigned flags_wait(const Params& p, int line0, int stride, int n, unsigned want, int lane, int& cap) {
    unsigned v = 0;
    for (int sp = 0;; ++sp) {
        v = flag_get(p, line0 + stride * (lane < n ? lane : 0));
        if (__all(v >= want)) break;
        if (sp > cap) { flag_set(p, FL_TMO, 1u); cap = 0; break; }
        __builtin_amdgcn_s_sleep(4);
    }
    asm volatile("" ::: "memory");
    return v;
}
#define TUNEW(i, dflt) (dflt)
DEV void probe_page(const Params& p, int pg, int lane) {
    const unsigned xcc = __builtin_amdgcn_s_getreg(20 | (3 << 11)) & 0xf;
    unsigned best[2] = {1u << 30, 1u << 30};
    char* base = p.ws + (pg < 1 ? OFF_COMM0 : OFF_T + (pg - 1) * 2 * MB) + 2 * MB - 16384;
    const __amdgpu_buffer_rsrc_t r = mkrsrc(base, 16384);
    const u32x4 sv = {SENT, SENT, SENT, SENT};
    st16_sc1(sv, r, lane * 128, 0); st16_sc1(sv, r, lane * 128, 8192);
    WAIT_VM(0);
#pragma unroll 1
    for (int i = 0; i < 8; ++i) {
#pragma unroll
        for (int c = 0; c < 2; ++c) {
            const char* q = base + c * 8192 + i * 768;
            unsigned long long t0, t1; unsigned v;
            asm volatile("s_memtime %0\n\ts_waitcnt lgkmcnt(0)\n\tglobal_load_dword %2, %3, off sc1\n\ts_waitcnt vmcnt(0)\n\ts_memtime %1\n\ts_waitcnt lgkmcnt(0)"
                         : "=&s"(t0), "=&s"(t1), "=&v"(v) : "v"(q) : "memory");
            best[c] = min(best[c], (unsigned)(t1 - t0));
        }
    }
    unsigned side = best[0] < best[1] ? 0u : 1u;
    if (xcc >= 4) side ^= 1u;
    if (lane == 0) flag_set(p, FL_SIDE + pg, 1u + side);
}
DEV unsigned long long wait_sides(const Params& p, int lane, int& cap) {
    const unsigned v = flags_wait(p, FL_SIDE, 1, 1 + NTPG, 1u, lane, cap);
    return __ballot(lane < 1 + NTPG && v == 2u);
}

constexpr unsigned WBYTES = D * H * DH * 2;
constexpr int XT_BYTES = 40960;
DEV void stage_x(__amdgpu_buffer_rsrc_t rx, int seg, char* img, int wave, int lane) {
    const int tok = 8 * (wave & 3) + (lane >> 3), c = (lane & 7) ^ ((tok >> 1) & 7);
    const unsigned voff = (tok * D + (wave >> 2) * 64 + c * 8) * 2;
#pragma unroll
    for (int it = 0; it < 5; ++it) bdma16(rx, img + (wave + 8 * it) * 1024, voff, (seg * 32 * D + it * 128) * 2);
}
DEV unsigned xfrag_off(int ks, int lane) {
    const int tok = lane & 31, hi = lane >> 5, c = 2 * (ks & 3) + hi;
    return (ks >> 2) * 4096 + tok * 128 + ((c ^ ((tok >> 1) & 7)) << 4);
}

DEV void role_gemm_qk(const Params& p, int h, int par, char* lds) {
    const int tid = threadIdx.x, lane = tid & 63, wave = __builtin_amdgcn_readfirstlane(tid >> 6);
    u32x4 w[40];
    const __amdgpu_buffer_rsrc_t rw0 = mkrsrc(p.wq, WBYTES), rw1 = mkrsrc(p.wk, WBYTES), rx = mkrsrc(p.bx, S * D * 2);
    {
        const int c = wave & 3;
#pragma unroll
        for (int bt = 0; bt < 5; ++bt) {
            char* img = lds + (bt & 1) * 65536;
            if (bt == 0) {
                stage_img<128>(rw0, h * DH, H * DH, img, wave, lane);
                stage_img<128>(rw1, h * DH, H * DH, img + 32768, wave, lane);
            }
            if (bt + 1 < 5) {
                char* nx = lds + ((bt + 1) & 1) * 65536;
                stage_img<128>(rw0, (bt + 1) * 128 * H * DH + h * DH, H * DH, nx, wave, lane);
                stage_img<128>(rw1, (bt + 1) * 128 * H * DH + h * DH, H * DH, nx + 32768, wave, lane);
                WAIT_VM(8);
            } else WAIT_VM(0);
            raw_barrier();
            const char* my = img + (wave >> 2) * 32768;
#pragma unroll
            for (int ks = 0; ks < 8; ++ks) {
                u32x2 a = tr32(my, 16 * ks + 8 * (lane >> 5), c, lane), b = tr32(my, 16 * ks + 8 * (lane >> 5) + 4, c, lane);
                w[bt * 8 + ks] = u32x4{a.x, a.y, b.x, b.y};
            }
            raw_barrier();
        }
    }
    const __amdgpu_buffer_rsrc_t ro = mkrsrc(p.ws + (wave < 4 ? OFF_QX : OFF_KX), TUNEW(4, 0) ? 0 : 16 * MB);
    stage_x(rx, par, lds, wave, lane);
    stage_x(rx, par + 2, lds + XT_BYTES, wave, lane);
    for (int i = 0; i < 32; ++i) {
        const int seg = par + 2 * i;
        char* img = lds + (i % 3) * XT_BYTES;
        if (i == 0) WAIT_VM(5); else if (i <= 2) WAIT_VM(0); else if (i < 31) WAIT_VM(7); else WAIT_VM(2);
        raw_barrier();
        if (tid == 0 && i >= 1 && i != 3) flag_set(p, FL_GQ + 2 * h + par, i <= 2 ? i : i - 1);
        if (i + 2 < 32) stage_x(rx, seg + 4, lds + ((i + 2) % 3) * XT_BYTES, wave, lane);
        f32x16 acc = {};
        {
            u32x4 xf[2][4];
#pragma unroll
            for (int j = 0; j < 4; ++j) xf[0][j] = lds_ld16(img, xfrag_off(j, lane));
#pragma unroll
            for (int bq = 0; bq < 10; ++bq) {
                if (bq + 1 < 10) {
#pragma unroll
                    for (int j = 0; j < 4; ++j) xf[(bq + 1) & 1][j] = lds_ld16(img, xfrag_off(4 * (bq + 1) + j, lane));
                }
                __builtin_amdgcn_sched_barrier(0);
#pragma unroll
                for (int j = 0; j < 4; ++j) acc = mf32(w[4 * bq + j], xf[bq & 1][j], acc);
                __builtin_amdgcn_sched_barrier(0);
            }
        }
#pragma unroll
        for (int a = 0; a < 4; a += 2) {
            unsigned x0 = pk_bf16(acc[4 * a], acc[4 * a + 1]), x1 = pk_bf16(acc[4 * a + 2], acc[4 * a + 3]);
            unsigned y0 = pk_bf16(acc[4 * a + 4], acc[4 * a + 5]), y1 = pk_bf16(acc[4 * a + 6], acc[4 * a + 7]);
            auto r0 = __builtin_amdgcn_permlane32_swap(x0, y0, false, false);
            auto r1 = __builtin_amdgcn_permlane32_swap(x1, y1, false, false);
            u32x4 v = {r0[0], r1[0], r0[1], r1[1]};
            st16_sc1(v, ro, ((4 * (wave & 3) + a) * 32 + lane) * 16, (seg * H + h) * 8192);
        }
    }
    WAIT_VM(0);
    raw_barrier();
    if (tid == 0) flag_set(p, FL_GQ + 2 * h + par, 32);
}

DEV void role_gemm_vu(const Params& p, int h, int par, char* lds) {
    const int tid = threadIdx.x, lane = tid & 63, wave = __builtin_amdgcn_readfirstlane(tid >> 6);
    const int et = wave & 3, kh = wave >> 2, g = lane >> 4;
    u32x4 wv[20], wo[5][4];
    const __amdgpu_buffer_rsrc_t rw0 = mkrsrc(p.wv, WBYTES), rw1 = mkrsrc(p.wo, WBYTES), rx = mkrsrc(p.bx, S * D * 2);
    {
#pragma unroll
        for (int bt = 0; bt < 5; ++bt) {
            char* img = lds + (bt & 1) * 65536;
            if (bt == 0) {
                stage_img<64>(rw0, h * DH, H * DH, img, wave, lane);
                stage_img<64>(rw0, 320 * H * DH + h * DH, H * DH, img + 16384, wave, lane);
                stage_img<128>(rw1, h * DH * D, D, img + 32768, wave, lane);
            }
            if (bt + 1 < 5) {
                char* nx = lds + ((bt + 1) & 1) * 65536;
                stage_img<64>(rw0, (bt + 1) * 64 * H * DH + h * DH, H * DH, nx, wave, lane);
                stage_img<64>(rw0, (320 + (bt + 1) * 64) * H * DH + h * DH, H * DH, nx + 16384, wave, lane);
                stage_img<128>(rw1, h * DH * D + (bt + 1) * 128, D, nx + 32768, wave, lane);
                WAIT_VM(8);
            } else WAIT_VM(0);
            raw_barrier();
            const char* my = img + kh * 16384;
#pragma unroll
            for (int ks = 0; ks < 4; ++ks) {
                u32x2 a = tr32(my, 16 * ks + 8 * (lane >> 5), et, lane), b = tr32(my, 16 * ks + 8 * (lane >> 5) + 4, et, lane);
                wv[bt * 4 + ks] = u32x4{a.x, a.y, b.x, b.y};
            }
#pragma unroll
            for (int ks = 0; ks < 4; ++ks) {
                const unsigned r0 = 32 * ks + 16 * (g >> 1) + 4 * (g & 1);
                u32x2 a = tr16(img + 32768, r0, wave, lane), b = tr16(img + 32768, r0 + 8, wave, lane);
                wo[bt][ks] = u32x4{a.x, a.y, b.x, b.y};
            }
            raw_barrier();
        }
    }
    char* xch = lds + 2 * XT_BYTES;
    char* vimg = xch + 16384;
    const __amdgpu_buffer_rsrc_t ru = mkrsrc(p.ws + OFF_U, TUNEW(4, 0) ? 0 : 80 * MB);
    stage_x(rx, par, lds, wave, lane);
    stage_x(rx, par + 2, lds + XT_BYTES, wave, lane);
    for (int i = 0; i <= 32; ++i) {
        const int seg = par + 2 * i;
        const char* img = lds + (i & 1) * XT_BYTES;
        if (i <= 1) WAIT_VM(5); else if (i <= 3) WAIT_VM(0); else if (i < 31) WAIT_VM(10); else WAIT_VM(5);
        raw_barrier();
        if (tid == 0 && i >= 2 && i != 4) flag_set(p, FL_GU + 2 * h + par, i <= 3 ? i - 1 : i - 2);
        f32x4 k0 = {}, k1 = {};
        if (i < 32) {
            f32x16 acc = {};
            {
                u32x4 xf[2][4];
#pragma unroll
                for (int j = 0; j < 4; ++j) xf[0][j] = lds_ld16(img + kh * 20480, xfrag_off(j, lane));
#pragma unroll
                for (int bq = 0; bq < 5; ++bq) {
                    if (bq + 1 < 5) {
#pragma unroll
                        for (int j = 0; j < 4; ++j) xf[(bq + 1) & 1][j] = lds_ld16(img + kh * 20480, xfrag_off(4 * (bq + 1) + j, lane));
                    }
                    __builtin_amdgcn_sched_barrier(0);
#pragma unroll
                    for (int j = 0; j < 4; ++j) acc = mf32(wv[4 * bq + j], xf[bq & 1][j], acc);
                    __builtin_amdgcn_sched_barrier(0);
                }
            }
            f32x4 s0, s1;
            if (kh == 0) { s0 = f32x4{acc[8], acc[9], acc[10], acc[11]}; s1 = f32x4{acc[12], acc[13], acc[14], acc[15]}; }
            else { s0 = f32x4{acc[0], acc[1], acc[2], acc[3]}; s1 = f32x4{acc[4], acc[5], acc[6], acc[7]}; }
            *(f32x4*)(xch + wave * 2048 + lane * 16) = s0;
            *(f32x4*)(xch + wave * 2048 + 1024 + lane * 16) = s1;
            if (kh == 0) { k0 = f32x4{acc[0], acc[1], acc[2], acc[3]}; k1 = f32x4{acc[4], acc[5], acc[6], acc[7]}; }
            else { k0 = f32x4{acc[8], acc[9], acc[10], acc[11]}; k1 = f32x4{acc[12], acc[13], acc[14], acc[15]}; }
        }
        if (i > 0) {
            u32x4 vf[2][4];
#pragma unroll
            for (int tt = 0; tt < 2; ++tt)
#pragma unroll
                for (int ks = 0; ks < 4; ++ks) vf[tt][ks] = lds_ld16(vimg, ((ks * 4 + g) * 32 + 16 * tt + (lane & 15)) * 16);
#pragma unroll
            for (int bt = 0; bt < 5; ++bt) {
                f32x4 d0 = {}, d1 = {};
#pragma unroll
                for (int ks = 0; ks < 4; ++ks) { d0 = mf16(vf[0][ks], wo[bt][ks], d0); d1 = mf16(vf[1][ks], wo[bt][ks], d1); }
                u32x4 v = {pk_bf16(d0[0], d0[1]), pk_bf16(d0[2], d0[3]), pk_bf16(d1[0], d1[1]), pk_bf16(d1[2], d1[3])};
                st16_sc1(v, ru, lane * 16 + (8 * bt + wave) * 32768, ((seg - 2) * NCT * H + h) * 1024);
            }
        }
        raw_barrier();
        if (i < 32) {
            if (i + 2 < 32) stage_x(rx, seg + 4, lds + (i & 1) * XT_BYTES, wave, lane);
            const f32x4 o0 = *(const f32x4*)(xch + (wave ^ 4) * 2048 + lane * 16), o1 = *(const f32x4*)(xch + (wave ^ 4) * 2048 + 1024 + lane * 16);
            k0 += o0; k1 += o1;
            u32x4 v = {pk_bf16(k0[0], k0[1]), pk_bf16(k0[2], k0[3]), pk_bf16(k1[0], k1[1]), pk_bf16(k1[2], k1[3])};
            lds_st16(vimg, ((et * 4 + 2 * kh + (lane >> 5)) * 32 + (lane & 31)) * 16, v);
        }
    }
    WAIT_VM(0);
    raw_barrier();
    if (tid == 0) flag_set(p, FL_GU + 2 * h + par, 32);
}

DEV unsigned now_rt() { return (unsigned)__builtin_amdgcn_s_memrealtime(); }
DEV unsigned clamp_rt(unsigned t) { const unsigned n = now_rt(); const int d = (int)(t - n); return (d > 400 || d < 0) ? n : t; }
DEV void wait_rt(unsigned t) {
    int d;
    while ((d = (int)(t - now_rt())) > 3) {}
    if (d > 0) asm volatile("s_nop 3");
    if (d > 1) asm volatile("s_nop 3");
    if (d > 2) asm volatile("s_nop 3");
    asm volatile("" ::: "memory");
}
#define PIN_S(x) do { x = __builtin_amdgcn_readfirstlane(x); asm volatile("" : "+s"(x)); } while (0)
#define SB() __builtin_amdgcn_sched_barrier(0)
DEV unsigned mx5(unsigned m, u32x4 v) { return max(max(max(m, v.x), v.y), max(v.z, v.w)); }
DEV unsigned read_ann(__amdgpu_buffer_rsrc_t r, unsigned soff, int cap) {
    unsigned v = 0;
    for (int sp = 0; sp <= cap; ++sp) {
        asm volatile("" ::: "memory");
        v = __builtin_amdgcn_readfirstlane(__builtin_amdgcn_raw_buffer_load_b32(r, 0, soff, 16));
        if (v != SENT) break;
    }
    return v;
}
DEV float wave_max(float x) {
    x = fmaxf(x, u2f(__builtin_amdgcn_update_dpp(0, f2u(x), 0x128, 0xf, 0xf, true)));
    x = fmaxf(x, u2f(__builtin_amdgcn_update_dpp(0, f2u(x), 0x124, 0xf, 0xf, true)));
    x = fmaxf(x, u2f(__builtin_amdgcn_update_dpp(0, f2u(x), 0x122, 0xf, 0xf, true)));
    x = fmaxf(x, u2f(__builtin_amdgcn_update_dpp(0, f2u(x), 0x121, 0xf, 0xf, true)));
    return xmax16_32(x);
}
DEV void dma_mem(__amdgpu_buffer_rsrc_t rc0, char* mimg, int lane, unsigned oa, unsigned ob) {
    const unsigned oa2 = oa + 4096;
    __builtin_amdgcn_raw_ptr_buffer_load_lds(rc0, (LAS void*)mimg, 16, lane * 16, oa, 0, 16);
    __builtin_amdgcn_raw_ptr_buffer_load_lds(rc0, (LAS void*)mimg, 16, lane * 16, oa, 1024, 16);
    __builtin_amdgcn_raw_ptr_buffer_load_lds(rc0, (LAS void*)mimg, 16, lane * 16, oa, 2048, 16);
    __builtin_amdgcn_raw_ptr_buffer_load_lds(rc0, (LAS void*)mimg, 16, lane * 16, oa, 3072, 16);
    __builtin_amdgcn_raw_ptr_buffer_load_lds(rc0, (LAS void*)(mimg + 4096), 16, lane * 16, oa2, 0, 16);
    __builtin_amdgcn_raw_ptr_buffer_load_lds(rc0, (LAS void*)(mimg + 4096), 16, lane * 16, oa2, 1024, 16);
    __builtin_amdgcn_raw_ptr_buffer_load_lds(rc0, (LAS void*)(mimg + 4096), 16, lane * 16, oa2, 2048, 16);
    __builtin_amdgcn_raw_ptr_buffer_load_lds(rc0, (LAS void*)(mimg + 4096), 16, lane * 16, oa2, 3072, 16);
    __builtin_amdgcn_raw_ptr_buffer_load_lds(rc0, (LAS void*)(mimg + 8192), 16, lane * 16, ob, 0, 16);
    __builtin_amdgcn_raw_ptr_buffer_load_lds(rc0, (LAS void*)(mimg + 8192), 16, lane * 16, ob, 1024, 16);
}
DEV void fill_mimg(char* mimg, int lane) {
    WAIT_VM(0);
#pragma unroll
    for (int i = 0; i < 10; ++i) lds_st16(mimg, i * 1024 + lane * 16, u32x4{SENT, SENT, SENT, SENT});
    __builtin_amdgcn_s_waitcnt(0xC07F);
}
DEV bf16x16 cat8(u32x4 a, u32x4 b) { u32x8 r = {a.x, a.y, a.z, a.w, b.x, b.y, b.z, b.w}; return __builtin_bit_cast(bf16x16, r); }
DEV f32x4 zero4() { return f32x4{0.f, 0.f, 0.f, 0.f}; }
#define RDF(i) af[i] = lds_ld16(mimg, (i) * 1024 + lane * 16)
DEV bool any_nan(float a, float b) { return __builtin_amdgcn_fcmpf(a, b, 8  ) != 0; }
#define PROJ_READ(WAITN, TICK) do { \
        WS(0); \
        WAITN(4); \
        for (int sp_ = 0;; ++sp_) { \
            asm volatile("" ::: "memory"); \
            const unsigned pr_ = *(const unsigned*)(mimg + (lane < NCT ? lane : 0) * 256 + 128 + (lane & 31) * 4); \
            RDF(4); RDF(5); \
            if (__all(pr_ != SENT) || sp_ > cap) break; \
            TICK; \
        } \
        if (wave == 0 && ISQK) MT(tsp); \
        WS(1); \
        WAITN(0); \
        RDF(0); RDF(1); RDF(2); RDF(3); RDF(6); RDF(7); RDF(8); RDF(9); SB(); \
    } while (0)
#define WAIT_VALID(TICK) do { \
        for (int sp_ = 0;; ++sp_) { \
            asm volatile("" ::: "memory"); \
            unsigned m_ = 0; \
            _Pragma("unroll") for (int st = 0; st < 10; ++st) m_ = mx5(m_, lds_ld16(mimg, st * 1024 + lane * 16)); \
            if (__all(m_ != SENT)) break; \
            if (sp_ > cap) { flag_set(p, FL_TMO, 1u); cap = 0; break; } \
            TICK; \
        } } while (0)
#define NOWAIT(n) (void)0
#define POLL_TICK do { if (tick == tnext) { tnext += 6; if (tick > 24) WAIT_VM(0); dma_mem(rc0, mimg, lane, oa, ob); } ++tick; } while (0)

constexpr float SCALE = 0.08838834764831845f;
constexpr float LOG2E = 1.4426950408889634f;
constexpr float MASKV = -999292928.0f;
DEV void round_scores(f32x4& d) {
    unsigned a = pk_bf16(d[0], d[1]), b = pk_bf16(d[2], d[3]);
    a = pk_bf16(bf_lo(a) * SCALE, bf_hi(a) * SCALE); b = pk_bf16(bf_lo(b) * SCALE, bf_hi(b) * SCALE);
    d = f32x4{bf_lo(a), bf_hi(a), bf_lo(b), bf_hi(b)};
}
DEV float max4(f32x4 d) { return fmaxf(fmaxf(d[0], d[1]), fmaxf(d[2], d[3])); }
DEV void exp4(f32x4& d, float mx) {
#pragma unroll
    for (int i = 0; i < 4; ++i) d[i] = __builtin_amdgcn_exp2f((d[i] - mx) * LOG2E);
}
DEV float sum4(f32x4 d) { return (d[0] + d[1]) + (d[2] + d[3]); }
DEV unsigned pk_pair(f32x4 d) { return pk_bf16(d[0] + d[2], d[1] + d[3]); }

DEV void role_qk(const Params& p, int h, char* lds) {
    constexpr bool ISQK = true; (void)ISQK;
    const int tid = threadIdx.x, lane = tid & 63, wave = __builtin_amdgcn_readfirstlane(tid >> 6);
    const int g = lane >> 4, l16 = lane & 15;
    unsigned* ctrl = (unsigned*)(p.ws + OFF_CTRL) + TUNE_W; (void)ctrl;
    bf16x16 wq[10], wk[10];
    const __amdgpu_buffer_rsrc_t rw0 = mkrsrc(p.wq, WBYTES), rw1 = mkrsrc(p.wk, WBYTES);
#pragma unroll
    for (int bt = 0; bt < 5; ++bt) {
        char* img = lds + (bt & 1) * 65536;
        if (bt == 0) {
            stage_img<128>(rw0, h * DH, H * DH, img, wave, lane);
            stage_img<128>(rw1, h * DH, H * DH, img + 32768, wave, lane);
        }
        if (bt + 1 < 5) {
            char* nx = lds + ((bt + 1) & 1) * 65536;
            stage_img<128>(rw0, (bt + 1) * 128 * H * DH + h * DH, H * DH, nx, wave, lane);
            stage_img<128>(rw1, (bt + 1) * 128 * H * DH + h * DH, H * DH, nx + 32768, wave, lane);
            WAIT_VM(8);
        } else WAIT_VM(0);
        raw_barrier();
#pragma unroll
        for (int u = 0; u < 2; ++u) { wq[2 * bt + u] = load_bsp_perm(img, 64 * u, wave, lane); wk[2 * bt + u] = load_bsp_perm(img + 32768, 64 * u, wave, lane); }
        raw_barrier();
    }
    int cap = SPIN_CAP;
    const unsigned side0 = (flags_wait(p, FL_SIDE, 0, 1, 1u, lane, cap) == 2u ? 1u : 0u) << 13;
    flags_wait(p, FL_READY, 0, 1, NCT, lane, cap);
    const __amdgpu_buffer_rsrc_t rc0 = mkrsrc(p.ws + OFF_COMM0 + __builtin_amdgcn_readfirstlane(side0), 2 * MB - 8192);
    const __amdgpu_buffer_rsrc_t rq = mkrsrc(p.ws + OFF_QX, 32 * MB);
    const __amdgpu_buffer_rsrc_t rpx = mkrsrc(p.ws + OFF_PX, 5 * MB);
    char* mimg = lds;
    char* ximg = lds + 40960;
    char* misc = lds + 46080;
    unsigned* hist = (unsigned*)(misc + 32);
    char* qk = lds + 49152;
    const int idx = (lane & 2) ? 0xEEEE : 0x4444;
    unsigned long long tsp = 0; (void)tsp;
    WSDECL;
    {
        flags_wait(p, FL_GQ + 2 * h, 0, 1, 1u, lane, cap);
        u32x4 a = ld16_sc1(rq, tid * 16, h * 8192), b = ld16_sc1(rq, tid * 16, 16 * MB + h * 8192);
        lds_st16(qk, tid * 16, a); lds_st16(qk, 8192 + tid * 16, b);
        if (tid == 0) { lds_vol_st<int>(misc, 90); lds_vol_st<int>(misc + 4, 60); }
    }
    raw_barrier();
    u32x4 af[10];
    f32x4 aq, ak;
    char* xw = ximg + (16 * wave + l16) * 32 + g * 4;
#define Q_DO(st) aq = smf(af[st], wq[st], (st) == 4 ? zero4() : aq, idx)
#define K_DO(st) ak = smf(af[st], wk[st], (st) == 4 ? zero4() : ak, idx)
#define QK_DO(st) do { Q_DO(st); K_DO(st); } while (0)
#define QK_ALL() do { QK_DO(4); QK_DO(5); QK_DO(0); QK_DO(1); QK_DO(2); QK_DO(3); QK_DO(6); QK_DO(7); QK_DO(8); QK_DO(9); } while (0)
#define QK_PROJ(WAITN, TICK, HOOK, HOOK2) do { \
        float q0_, q1_; \
        for (int pass_ = 0;; ++pass_) { \
            PROJ_READ(WAITN, TICK); \
            HOOK; \
            QK_ALL(); SB(); \
            HOOK2; \
            q0_ = aq[0] + aq[2]; q1_ = aq[1] + aq[3]; \
            if (!any_nan(q0_, q1_) || pass_) break; \
            WAIT_VALID(TICK); \
        } \
        *(unsigned*)xw = pk_bf16(q0_, q1_); *(unsigned*)(xw + 16) = pk_pair(ak); \
        WS(4); } while (0)
    if (wave == 0) {
        const unsigned gq = l16 >> 2, r = l16 & 3, key0 = 16 * (r >> 1) + 8 * (gq >> 1) + 2 * (gq & 1) + (r & 1);
        const unsigned koff = 8192 + (g * 32 + key0) * 16;
        const bool hi8 = l16 >= 8;
        const unsigned px_off = ((g >> 1) * 16 + rowi(l16 & 7, g & 1)) * 16 + (l16 >> 3) * 8;
        const unsigned pm_off = rowi(l16 & 7, g & 1) * 16 + (h & 1) * 8 + (g >> 1) * 4 + (l16 >> 3) * 4096;
        const unsigned foff = (8 * g + (l16 >> 2)) * 32 + (lane & 1) * 8;
        unsigned long long t1 = 0, t2 = 0, t3 = 0, t4 = 0; unsigned a12 = 0, a23 = 0, a34 = 0, a01 = 0; (void)t1; (void)t2; (void)t3; (void)t4; (void)a12; (void)a23; (void)a34; (void)a01;
        for (int s = 0; s < NSEG; ++s) {
            const char* kimg = qk + (s & 1) * 16384 + koff;
            unsigned opx = physo(px_base(s & 3, 0) + h * 512), opx1 = opx + 32768, opm = physo(pm_base(s & 3, 0) + (h >> 1) * 256);
            PIN_S(opx); PIN_S(opx1); PIN_S(opm);
            raw_barrier();
            MT(t1);
            u32x4 Fq[4], Fk[4], kf[8];
#define RD_KF do { _Pragma("unroll") for (int ks = 0; ks < 4; ++ks) { kf[2 * ks] = lds_ld16(kimg, ks * 2048); kf[2 * ks + 1] = lds_ld16(kimg, ks * 2048 + 64); } SB(); } while (0)
            QK_PROJ(NOWAIT, (void)0, (void)0, RD_KF);
            MT(t2);
            raw_barrier();
            MT(t3); WS(5);
            if (s + 1 < NSEG) {
#pragma unroll
                for (int ks = 0; ks < 4; ++ks) { u32x2 a = tr_read(ximg + foff, ks * 1024), b = tr_read(ximg + foff, ks * 1024 + 128); Fq[ks] = u32x4{a.x, a.y, b.x, b.y}; }
#pragma unroll
                for (int ks = 0; ks < 4; ++ks) { u32x2 c = tr_read(ximg + foff, ks * 1024 + 16), d = tr_read(ximg + foff, ks * 1024 + 128 + 16); Fk[ks] = u32x4{c.x, c.y, d.x, d.y}; }
                SB();
                f32x4 d0 = mf16(kf[0], Fq[0], zero4()), d1 = mf16(kf[1], Fq[0], zero4());
                d0 = mf16(kf[2], Fq[1], d0); d1 = mf16(kf[3], Fq[1], d1);
                d0 = mf16(kf[4], Fq[2], d0); d1 = mf16(kf[5], Fq[2], d1); d0 = mf16(kf[6], Fq[3], d0); d1 = mf16(kf[7], Fq[3], d1);
                SB();
                f32x4 dm = mf16(Fk[0], Fq[0], zero4());
                dm = mf16(Fk[1], Fq[1], dm); dm = mf16(Fk[2], Fq[2], dm); dm = mf16(Fk[3], Fq[3], dm);
                SB();
                float x0 = hi8 ? d1[0] : d0[0], x1 = hi8 ? d1[1] : d0[1], x2 = hi8 ? d1[2] : d0[2], x3 = hi8 ? d1[3] : d0[3];
                float mx = max3f(max3f(x0, x1, x2), x3, x3);
                mx = max3f(mx, mx, u2f(__builtin_amdgcn_update_dpp(0, f2u(mx), 0x128, 0xf, 0xf, true)));
                SB();
                float y0 = g >= 2 ? dm[2] : dm[0], y1 = g >= 2 ? dm[3] : dm[1];
                mx = xmax16_32(max3f(mx, y0, y1));
                constexpr float CS = SCALE * LOG2E;
                const float nm = -mx * CS;
                x0 = __builtin_amdgcn_exp2f(__builtin_fmaf(x0, CS, nm)); x1 = __builtin_amdgcn_exp2f(__builtin_fmaf(x1, CS, nm));
                x2 = __builtin_amdgcn_exp2f(__builtin_fmaf(x2, CS, nm)); x3 = __builtin_amdgcn_exp2f(__builtin_fmaf(x3, CS, nm));
                y0 = __builtin_amdgcn_exp2f(__builtin_fmaf(y0, CS, nm)); y1 = __builtin_amdgcn_exp2f(__builtin_fmaf(y1, CS, nm));
                float sm = (x0 + x1) + (x2 + x3);
                sm = sm + u2f(__builtin_amdgcn_update_dpp(0, f2u(sm), 0x128, 0xf, 0xf, true)) + (y0 + y1);
                const float inv = __builtin_amdgcn_rcpf(xsum16_32(sm));
                const u32x2 pxv = {nosent(pk_bf16(x0 * inv, x1 * inv)), nosent(pk_bf16(x2 * inv, x3 * inv))};
                st8_sc1(pxv, rc0, px_off, opx);
                st4_sc1(nosent(pk_bf16(y0 * inv, y1 * inv)), rc0, pm_off, opm);
                st8_sc1(pxv, rc0, px_off, opx1);
                MT(t4); WS(7); WSDUMP();
                { const unsigned ta = now_rt(); if (lane == 0) st4_sc1(nosent(ta), rc0, s * 4, physo(STA_BASE + h * 256)); }
                MTW(); PACC(a12, t1, t2); PACC(a23, t2, t3); PACC(a34, t3, t4); PACC(a01, t1, tsp); PDUMP(h, a12, a23, a34, a01);
            }
        }
    } else if (wave <= 2) {
        const int tt = wave - 1;
        const int tok = 16 * tt + l16;
        for (int s = 0; s < NSEG; ++s) {
            raw_barrier();
            QK_PROJ(NOWAIT, (void)0, (void)0, (void)0);
            raw_barrier();
            WS(7); WSDUMP();
            __builtin_amdgcn_s_sleep(2);
            const char* kimg = qk + (s & 1) * 16384 + 8192 + (g * 32 + l16) * 16;
            const char* qimg = qk + (s & 1) * 16384 + (g * 32 + tok) * 16;
            f32x4 d0 = {}, d1 = {}, dm = {};
#pragma unroll
            for (int ks = 0; ks < 4; ++ks) {
                const unsigned e0 = 32 * ks + 8 * g + (l16 >> 2);
                u32x2 a = tr_read(ximg, e0 * 32 + (lane & 3) * 8), b = tr_read(ximg, (e0 + 4) * 32 + (lane & 3) * 8);
                const u32x4 qf = lds_ld16(qimg, ks * 2048);
                d0 = mf16(lds_ld16(kimg, ks * 2048), qf, d0);
                d1 = mf16(lds_ld16(kimg, ks * 2048 + 256), qf, d1);
                dm = mf16(u32x4{a.x, a.y, b.x, b.y}, qf, dm);
            }
            round_scores(d0); round_scores(d1); round_scores(dm);
#pragma unroll
            for (int r = 0; r < 4; ++r) { if (4 * g + r > tok) d0[r] = MASKV; if (16 + 4 * g + r > tok) d1[r] = MASKV; }
            if (g < 2) dm = f32x4{-3.0e38f, -3.0e38f, -3.0e38f, -3.0e38f};
            const float mx = xmax16_32(fmaxf(fmaxf(max4(d0), max4(d1)), max4(dm)));
            exp4(d0, mx); exp4(d1, mx); exp4(dm, mx);
            const float inv = 1.0f / xsum16_32(sum4(d0) + sum4(d1) + sum4(dm));
            u32x4 v = {pk_bf16(d0[0] * inv, d0[1] * inv), pk_bf16(d0[2] * inv, d0[3] * inv), pk_bf16(d1[0] * inv, d1[1] * inv), pk_bf16(d1[2] * inv, d1[3] * inv)};
            st16_sc1(v, rpx, lane * 16, ((s * H + h) * 2 + tt) * 1024);
            if (g >= 2) {
                st4_sc1(pk_bf16(dm[0] * inv, dm[1] * inv), rpx, tok * 16 + (g - 2) * 4, 4 * MB + (s * H + h) * 512);
                st4_sc1(pk_bf16(dm[2] * inv, dm[3] * inv), rpx, tok * 16 + 8 + (g - 2) * 4, 4 * MB + (s * H + h) * 512);
            }
            WAIT_VM(0);
            if (lane == 0) flag_set(p, FL_PX + 2 * h + tt, s + 1);
        }
    } else if (wave < 7) {
        unsigned gk0 = 0u, gk1 = flags_wait(p, FL_GQ + 2 * h + 1, 0, 1, 1u, lane, cap);
        const int tfirst = wave == 6 ? TUNEW(5, 0) : wave == 5 ? TUNEW(5, 0) + 2 : -1;
        const float mg1 = 0.25f * (float)TUNEW(0, 36), mg2 = 0.25f * (float)TUNEW(1, 16);
        float d1e = 90.0f, d2e = 60.0f;
        for (int s = 0; s < NSEG; ++s) {
            unsigned oa = physo(mema_off(s & 3, 0)), ob = physo(memb_off(s & 3, 0));
            PIN_S(oa); PIN_S(ob);
            int tick = 0, tnext = tfirst;
            raw_barrier();
            QK_PROJ(NOWAIT, POLL_TICK, (void)0, (void)0);
            WAIT_VM(0);
            const unsigned o = (tid - 192) * 16, so = (min(s + 1, NSEG - 1) * H + h) * 8192;
            const u32x4 a0 = ld16_sc1(rq, o, so), a1 = ld16_sc1(rq, o + 4096, so), b0 = ld16_sc1(rq, o, 16 * MB + so), b1 = ld16_sc1(rq, o + 4096, 16 * MB + so);
            SB();
            raw_barrier();
            WS(7); WSDUMP();
            __builtin_amdgcn_s_sleep(6);
            char* nq = qk + ((s + 1) & 1) * 16384;
            lds_st16(nq, o, a0); lds_st16(nq, o + 4096, a1); lds_st16(nq, 8192 + o, b0); lds_st16(nq, 8192 + 4096 + o, b1);
            if (h == 0 && wave == 4 && s >= 2) {
                const unsigned tb = hist[2 * (s - 1)], tx = hist[2 * (s - 1) + 1];
                const unsigned va = __builtin_amdgcn_raw_buffer_load_b32(rc0, (lane & 31) * 256, physo(STA_BASE) + (s - 1) * 4, 16);
                const unsigned vb = __builtin_amdgcn_raw_buffer_load_b32(rc0, physo((lane < NCT ? lane : 0) * 256), physo(STB_BASE) + (s - 1) * 4, 16);
                if (__all(va != SENT && vb != SENT)) {
                    const float a = wave_max((float)(int)(va - tb)), b = wave_max((float)(int)(vb - tx));
                    if (tb != SENT) d1e += 0.5f * (fminf(a, d1e + 2.0f) - d1e);
                    d2e += 0.5f * (fminf(b, d2e + 2.0f) - d2e);
                    if (lane == 0) { lds_vol_st<int>(misc, (int)(d1e + mg1)); lds_vol_st<int>(misc + 4, (int)(d2e + mg2)); }
                }
            }
            if (wave == 3 && s >= 1) {
                st16_sc1(u32x4{SENT, SENT, SENT, SENT}, rc0, (lane & 31) * 16 + (lane >> 5) * 32768, physo(px_base((s - 1) & 3, 0) + h * 512));
                if (lane < 32) st8_sc1(u32x2{SENT, SENT}, rc0, l16 * 16 + (h & 1) * 8 + (g & 1) * 4096, physo(pm_base((s - 1) & 3, 0) + (h >> 1) * 256));
                WAIT_VM(0);
            }
            if (s + 2 < NSEG && ((s & 1) ? gk1 : gk0) < (unsigned)((s + 2) >> 1) + 1u) {
                const unsigned v = flags_wait(p, FL_GQ + 2 * h + (s & 1), 0, 1, ((s + 2) >> 1) + 1, lane, cap);
                if (s & 1) gk1 = v; else gk0 = v;
            }
        }
    } else {
        int nfail = 0;
        const int tfirst = TUNEW(5, 0) + 4;
        for (int s = 0; s < NSEG; ++s) {
            fill_mimg(mimg, lane);
            unsigned oa = physo(mema_off(s & 3, 0)), ob = physo(memb_off(s & 3, 0)), oann = physo(ANN_BASE + s * 128);
            PIN_S(oa); PIN_S(ob); PIN_S(oann);
            const unsigned tm = s > 0 ? clamp_rt(read_ann(rc0, oann - 128 + 4, cap)) : now_rt();
            const int dd1 = __builtin_amdgcn_readfirstlane(lds_vol_ld<int>(misc)), dd2 = __builtin_amdgcn_readfirstlane(lds_vol_ld<int>(misc + 4));
            int tick = 0, tnext = tfirst;
            wait_rt(tm);
            dma_mem(rc0, mimg, lane, oa, ob);
            const unsigned c0 = (unsigned)__builtin_amdgcn_s_memtime();
            raw_barrier();
#define QK_ANN do { if (h == 0) { \
                const unsigned te = tm + ((((unsigned)__builtin_amdgcn_s_memtime() - c0) * 2731u) >> 16), x1 = nosent(te + dd1), x2 = nosent(x1 + dd2); \
                if (lane == 0) { st8_sc1(u32x2{x1, x2}, rc0, 0, oann); hist[2 * s] = pass_ ? SENT : te; hist[2 * s + 1] = x1; } } } while (0)
            QK_PROJ(NOWAIT, POLL_TICK, QK_ANN, (void)0);
            raw_barrier();
            WS(7); WSDUMP();
            if (s >= 4) nfail += tick;
        }
    }
}

DEV void role_vo(const Params& p, int h, char* lds) {
    constexpr bool ISQK = false; (void)ISQK;
    const int tid = threadIdx.x, lane = tid & 63, wave = __builtin_amdgcn_readfirstlane(tid >> 6);
    const int g = lane >> 4, l16 = lane & 15;
    unsigned* ctrl = (unsigned*)(p.ws + OFF_CTRL) + TUNE_W; (void)ctrl;
    bf16x16 wv[10], wo[5][2];
    const __amdgpu_buffer_rsrc_t rw0 = mkrsrc(p.wv, WBYTES), rw1 = mkrsrc(p.wo, WBYTES);
#pragma unroll
    for (int bt = 0; bt < 5; ++bt) {
        char* img = lds + (bt & 1) * 65536;
        if (bt == 0) {
            stage_img<128>(rw0, h * DH, H * DH, img, wave, lane);
            stage_img<128>(rw1, h * DH * D, D, img + 32768, wave, lane);
        }
        if (bt + 1 < 5) {
            char* nx = lds + ((bt + 1) & 1) * 65536;
            stage_img<128>(rw0, (bt + 1) * 128 * H * DH + h * DH, H * DH, nx, wave, lane);
            stage_img<128>(rw1, h * DH * D + (bt + 1) * 128, D, nx + 32768, wave, lane);
            WAIT_VM(8);
        } else WAIT_VM(0);
        raw_barrier();
#pragma unroll
        for (int u = 0; u < 2; ++u) { wv[2 * bt + u] = load_bsp_perm(img, 64 * u, wave, lane); wo[bt][u] = load_bsp(img + 32768, 64 * u, wave, lane); }
        raw_barrier();
    }
    int cap = SPIN_CAP;
    const unsigned long long sides = wait_sides(p, lane, cap), tmask = sides >> 1;
    flags_wait(p, FL_READY, 0, 1, NCT, lane, cap);
    const __amdgpu_buffer_rsrc_t rc0 = mkrsrc(p.ws + OFF_COMM0 + (((unsigned)sides & 1u) << 13), 2 * MB - 8192);
    const __amdgpu_buffer_rsrc_t rt = mkrsrc(p.ws + OFF_T, 44 * MB);
    char* mimg = lds;
    char* vimg = lds + 40960;
    const int idx = (lane & 2) ? 0xEEEE : 0x4444;
    unsigned long long tsp = 0; (void)tsp;
    WSDECL;
    u32x4 af[10];
    f32x4 av;
    const unsigned e_ = 16 * wave + l16;
    const unsigned vw_off = (2 * (e_ >> 2) + (e_ & 1)) * 32 + (4 * g + 2 * ((e_ >> 1) & 1)) * 2;
    const unsigned vr_off = (8 * g + (l16 >> 2)) * 32 + (lane & 3) * 8;
    const unsigned t_voff = (8 * g + wave) * 16384 + l16 * 16, t_voff4 = g == 0 ? (32 + wave) * 16384 + l16 * 16 : 0x7FFFFFF0u;
    unsigned long long t1 = 0, t2 = 0, t3 = 0; unsigned v12 = 0, v23 = 0; (void)t1; (void)t2; (void)t3; (void)v12; (void)v23;
#define VO_DO(st) av = smf(af[st], wv[st], (st) == 4 ? zero4() : av, idx)
#define VO_ALL() do { VO_DO(4); VO_DO(5); VO_DO(0); VO_DO(1); VO_DO(2); VO_DO(3); VO_DO(6); VO_DO(7); VO_DO(8); VO_DO(9); } while (0)
#define VO_TAIL(WAITN, TICK) do { \
        float v0_, v1_; \
        for (int pass_ = 0;; ++pass_) { \
            PROJ_READ(WAITN, TICK); \
            VO_ALL(); SB(); \
            v0_ = av[0] + av[2]; v1_ = av[1] + av[3]; \
            if (!any_nan(v0_, v1_) || pass_) break; \
            WAIT_VALID(TICK); \
        } \
        *(unsigned*)(vimg + vw_off) = pk_bf16(v0_, v1_); \
        if (wave == 5 || wave == 6) WAIT_VM(0); \
        WS(4); \
        raw_barrier(); \
        WS(5); \
        MT(t2); \
        u32x4 A[2]; \
        _Pragma("unroll") for (int ks = 0; ks < 2; ++ks) { \
            u32x2 a = tr_read(vimg + vr_off, ks * 1024), b = tr_read(vimg + vr_off, ks * 1024 + 128); \
            A[ks] = u32x4{a.x, a.y, b.x, b.y}; \
        } \
        unsigned tw[5]; \
        _Pragma("unroll") for (int bt = 0; bt < 5; ++bt) { \
            f32x4 d = {}; \
            d = smf(A[0], wo[bt][0], d, idx); d = smf(A[1], wo[bt][1], d, idx); \
            tw[bt] = nosent(pk_pair(d)); \
        } \
        auto a = __builtin_amdgcn_permlane16_swap(tw[0], tw[1], false, false); \
        auto b = __builtin_amdgcn_permlane16_swap(tw[2], tw[3], false, false); \
        auto c = __builtin_amdgcn_permlane32_swap(a[0], b[0], false, false); \
        auto d = __builtin_amdgcn_permlane32_swap(a[1], b[1], false, false); \
        st16_sc1(u32x4{c[0], c[1], d[0], d[1]}, rt, t_voff, ot); \
        auto e = __builtin_amdgcn_permlane16_swap(tw[4], tw[4], false, false); \
        auto f = __builtin_amdgcn_permlane32_swap(e[0], e[0], false, false); \
        auto k = __builtin_amdgcn_permlane32_swap(e[1], e[1], false, false); \
        st16_sc1(u32x4{f[0], f[1], k[0], k[1]}, rt, t_voff4, ot); \
        WS(7); WSDUMP(); \
        MT(t3); MTW(); PACC(v12, t1, t2); PACC(v23, t2, t3); if (wave == 6) PDUMP(32 + h, v12, v23, 0u, 0u); \
    } while (0)
    const int tfirst = wave == 6 ? TUNEW(5, 0) : wave == 5 ? TUNEW(5, 0) + 2 : wave == 7 ? TUNEW(5, 0) + 4 : -1;
    for (int s = 0; s < NSEG; ++s) {
        if (wave == 7) fill_mimg(mimg, lane);
        unsigned ot = t_off(s, tmask) + h * 256;
        unsigned oa = physo(mema_off(s & 3, 0)), ob = physo(memb_off(s & 3, 0));
        PIN_S(oa); PIN_S(ob); PIN_S(ot);
        int tick = 0, tnext = tfirst;
        if (wave == 7) {
            if (s > 0) wait_rt(clamp_rt(read_ann(rc0, physo(ANN_BASE + (s - 1) * 128 + 4), cap)));
            dma_mem(rc0, mimg, lane, oa, ob);
        }
        raw_barrier();
        MT(t1);
        VO_TAIL(NOWAIT, POLL_TICK);
    }
}

DEV void role_comb(const Params& p, int c, char* lds) {
    const int tid = threadIdx.x, lane = tid & 63, wave = __builtin_amdgcn_readfirstlane(tid >> 6);
    const int g = lane >> 4, l16 = lane & 15;
    unsigned* ctrl = (unsigned*)(p.ws + OFF_CTRL) + TUNE_W; (void)ctrl;
    const __amdgpu_buffer_rsrc_t rt = mkrsrc(p.ws + OFF_T, 44 * MB), ru = mkrsrc(p.ws + OFF_U, 80 * MB);
    const int idx = (lane & 2) ? 0xEEEE : 0x4444;
    int cap = SPIN_CAP;
    const u32x4 sv = {SENT, SENT, SENT, SENT};
    {
        const __amdgpu_buffer_rsrc_t r = mkrsrc(p.ws + OFF_COMM0, COMM_CHUNKS * 16384);
        for (unsigned i = (c * 512 + tid) * 16; i < COMM_CHUNKS * 16384; i += NCT * 8192) st16_sc1(sv, r, i, 0);
    }
    const unsigned long long sides = wait_sides(p, lane, cap), tmask = sides >> 1;
    const __amdgpu_buffer_rsrc_t rc0 = mkrsrc(p.ws + OFF_COMM0 + (((unsigned)sides & 1u) << 13), 2 * MB - 8192);
    st16_sc1(sv, rt, tid * 16, t_off(0, tmask) + c * 16384);
    st16_sc1(sv, rt, tid * 16, t_off(1, tmask) + c * 16384);
    WAIT_VM(0);
    raw_barrier();
    if (tid == 0) flag_add(p, FL_READY);
    flags_wait(p, FL_READY, 0, 1, NCT, lane, cap);
    const unsigned rstr = c < 32 ? 16384u : 2048u;
    if (tid < 64) {
        const unsigned m = 2 * (l16 >> 2) + (l16 & 1), t = (l16 >> 1) & 1;
        const u32x4 src = *(const u32x4*)(p.mem0 + m * D + 16 * c + 8 * t);
        st16_sc1(u32x4{nosent(src.x), nosent(src.y), nosent(src.z), nosent(src.w)}, rc0, l16 * 16 + g * rstr, physo(mem_off(0, 0, c)));
    }
    flags_wait(p, FL_GU, 2, H, 1u, lane, cap);
#pragma unroll
    for (int j = 0; j < 4; ++j) lds_st16(lds, tid * 16 + j * 8192, ld16_sc1(ru, tid * 16 + j * 8192, c * 32768));
    raw_barrier();
    if (wave == 0) {
        int nfail = 0;
        const unsigned st_voff = (l16 & 6) == 0 ? rowi(2 * g + (l16 & 1), l16 >> 3) * 16 : 0x7FFFFFF0u;
        unsigned long long c0 = 0, c1 = 0, c2 = 0; unsigned k01 = 0, k12 = 0; (void)c0; (void)c1; (void)c2; (void)k01; (void)k12;
        for (int s = 0; s + 1 < NSEG; ++s) {
            const char* ub = lds + (s & 1) * 32768;
            u32x4 uf[24];
#pragma unroll
            for (int j = 0; j < 24; ++j) uf[j] = lds_ld16(ub, j * 1024 + lane * 16);
            unsigned ot = t_off(s, tmask) + c * 16384;
            unsigned ox0 = physo(px_base(s & 3, 0)), ox1 = ox0 + 16384, om = physo(pm_base(s & 3, 0));
            unsigned ost = physo(mem_off((s + 1) & 3, 0, c));
            const unsigned opx_x = ox0 ^ (ox0 + 32768), opx_y = ox1 ^ (ox1 + 32768);
            PIN_S(ot); PIN_S(ox0); PIN_S(ox1); PIN_S(om); PIN_S(ost);
            unsigned tp = clamp_rt(read_ann(rc0, physo(ANN_BASE + s * 128), cap));
            int sp = 0;
            float sa, sb;
            bool chk = false;
            wait_rt(tp);
            MT(c0);
            for (;; ++sp) {
                u32x4 tf[8], pm[4], px[16];
#pragma unroll
                for (int j = 0; j < 16; ++j) { px[j] = ld16_sc1(rc0, lane * 16 + (j & 7) * 1024, j < 8 ? ox0 : ox1); SB(); }
#pragma unroll
                for (int j = 0; j < 4; ++j) { pm[j] = ld16_sc1(rc0, lane * 16 + j * 1024, om); SB(); tf[2 * j] = ld16_sc1(rt, lane * 16 + 2 * j * 1024, ot); SB(); tf[2 * j + 1] = ld16_sc1(rt, lane * 16 + (2 * j + 1) * 1024, ot); SB(); }
                f32x4 ax;
                u32x4 ul[8];
#pragma unroll
                for (int j = 0; j < 16; ++j) {
                    const u32x4 u0 = j < 12 ? uf[j < 12 ? 2 * j : 0] : ul[j < 12 ? 0 : 2 * j - 24], u1 = j < 12 ? uf[j < 12 ? 2 * j + 1 : 0] : ul[j < 12 ? 0 : 2 * j - 23];
                    ax = smf(px[j], cat8(u0, u1), j ? ax : zero4(), idx);
                    if (j < 8) ul[j] = lds_ld16(ub, (24 + j) * 1024 + lane * 16);
                    SB();
                }
#pragma unroll
                for (int j = 0; j < 4; ++j) {
                    ax = smf(pm[j], cat8(tf[2 * j], tf[2 * j + 1]), ax, idx);
                    SB();
                }
                sa = ax[0] + ax[2]; sb = ax[1] + ax[3];
                if (!any_nan(sa, sb) || chk) break;
                ox0 ^= opx_x; ox1 ^= opx_y; om ^= 4096;
                if (sp >= 2) {
                    for (int k = 0;; ++k) {
                        asm volatile("" ::: "memory");
                        unsigned m = 0;
#pragma unroll
                        for (int j = 0; j < 8; ++j) m = mx5(mx5(mx5(m, ld16_sc1(rc0, lane * 16 + j * 1024, ox0)), ld16_sc1(rc0, lane * 16 + j * 1024, ox1)), ld16_sc1(rt, lane * 16 + j * 1024, ot));
#pragma unroll
                        for (int j = 0; j < 4; ++j) m = mx5(m, ld16_sc1(rc0, lane * 16 + j * 1024, om));
                        if (__all(m != SENT)) break;
                        if (k > cap) { flag_set(p, FL_TMO, 1u); cap = 0; break; }
                    }
                    chk = true;
                }
            }
            MT(c1);
            const float va = sa, vb = sb;
            const float give = (lane & 1) ? va : vb;
            const float got = u2f(__builtin_amdgcn_update_dpp(0, f2u(give), 0xB1, 0xf, 0xf, true));
            const unsigned pk = nosent(pk_bf16((lane & 1) ? got : va, (lane & 1) ? vb : got));
            u32x4 v;
            v.x = pk;
            v.y = __builtin_amdgcn_update_dpp(0, pk, 0x102, 0xf, 0xf, true);
            v.z = __builtin_amdgcn_update_dpp(0, pk, 0x104, 0xf, 0xf, true);
            v.w = __builtin_amdgcn_update_dpp(0, pk, 0x106, 0xf, 0xf, true);
            st16_sc1(v, rc0, st_voff, ost);
            MT(c2); MTW(); PACC(k01, c0, c1); PACC(k12, c1, c2); PDUMP(64 + c, k01, k12, (unsigned)sp, 0u);
            { const unsigned tb = now_rt(); if (lane == 0) st4_sc1(nosent(tb), rc0, s * 4, physo(STB_BASE + c * 256)); }
            if (lane == 0) flag_set(p, FL_CB + c, s + 1);
            if (s >= 4) nfail += sp;
            raw_barrier();
        }
        {
            flags_wait(p, FL_GU + 1, 2, H, 32u, lane, cap);
            const unsigned ot = t_off(NSEG - 1, tmask) + c * 16384;
            for (int sp = 0;; ++sp) {
                asm volatile("" ::: "memory");
                unsigned m = 0;
#pragma unroll
                for (int j = 0; j < 8; ++j) m = mx5(m, ld16_sc1(rt, lane * 16 + j * 1024, ot));
                if (__all(m != SENT)) break;
                if (sp > cap) { flag_set(p, FL_TMO, 1u); break; }
                __builtin_amdgcn_s_sleep(2);
            }
            if (lane == 0) flag_set(p, FL_CB + c, NSEG);
        }
    } else {
        const int udly = TUNEW(3, 8);
        unsigned gk0 = 0, gk1 = 0;
        for (int s = 0; s + 1 < NSEG; ++s) {
            if (wave >= 4 && s + 2 < NSEG) {
                char* nb = lds + ((s + 1) & 1) * 32768;
                for (int i = 0; i < udly; ++i) __builtin_amdgcn_s_sleep(2);
                const unsigned want = ((s + 1) >> 1) + 1;
                if ((((s + 1) & 1) ? gk1 : gk0) < want) {
                    unsigned v = flags_wait(p, FL_GU + ((s + 1) & 1), 2, H, want, lane, cap);
#pragma unroll
                    for (int o = 32; o >= 1; o >>= 1) v = min(v, (unsigned)__shfl_xor((int)v, o));
                    v = __builtin_amdgcn_readfirstlane(v);
                    if ((s + 1) & 1) gk1 = v; else gk0 = v;
                }
                u32x4 t[8];
#pragma unroll
                for (int j = 0; j < 8; ++j) t[j] = ld16_sc1(ru, (tid - 256) * 16 + j * 4096, ((s + 1) * NCT + c) * 32768);
#pragma unroll
                for (int j = 0; j < 8; ++j) lds_st16(nb, (tid - 256) * 16 + j * 4096, t[j]);
            } else if (wave <= 2 && s + 2 < NSEG) {
                for (int i = 0; i < udly; ++i) __builtin_amdgcn_s_sleep(2);
#pragma unroll
                for (int j = 0; j < 4; ++j) st16_sc1(sv, rt, (tid - 64) * 16 + j * 2048, t_off(s + 2, tmask) + c * 16384);
                WAIT_VM(0);
            } else if (wave == 3 && s >= 1) {
                for (int i = 0; i < udly; ++i) __builtin_amdgcn_s_sleep(2);
                st16_sc1(sv, rc0, l16 * 16 + g * rstr, physo(mem_off((s - 1) & 3, 0, c)));
                WAIT_VM(0);
            }
            raw_barrier();
        }
    }
}

constexpr int NCHAIN = 104, NWG = 256;
DEV int bcast0(int v, char* lds) {
    if (threadIdx.x == 0) *(int*)lds = v;
    __syncthreads();
    const int r = __builtin_amdgcn_readfirstlane(*(int*)lds);
    __syncthreads();
    return r;
}
DEV int get_role(const Params& p, char* lds) {
    int role = 0;
    if (threadIdx.x == 0) {
        const unsigned xcc = __builtin_amdgcn_s_getreg(20 | (3 << 11)) & 0xf;
        if (xcc < 4) { const int t = (int)flag_add(p, FL_TNEAR); role = t < NCHAIN ? t : NWG - 1 - (t - NCHAIN); }
        else { const int u = (int)flag_add(p, FL_TFAR); role = u < NWG - NCHAIN ? NCHAIN + u : NCHAIN - 1 - (u - (NWG - NCHAIN)); }
    }
    return bcast0(role, lds);
}

DEV void task_out(const Params& p, int s, int cg, char* lds, int& cap) {
    const int tid = threadIdx.x, lane = tid & 63, wave = __builtin_amdgcn_readfirstlane(tid >> 6);
    const int g = lane >> 4, l16 = lane & 15;
    const __amdgpu_buffer_rsrc_t rpx = mkrsrc(p.ws + OFF_PX, 5 * MB);
    const __amdgpu_buffer_rsrc_t rt = mkrsrc(p.ws + OFF_T, 44 * MB), ru = mkrsrc(p.ws + OFF_U, 80 * MB);
    const unsigned long long tmask = wait_sides(p, lane, cap) >> 1;
    const int c = cg * 8 + wave;
    flags_wait(p, FL_GU + (s & 1), 2, H, (s >> 1) + 1, lane, cap);
    u32x4 uf[32];
#pragma unroll
    for (int j = 0; j < 32; ++j) uf[j] = ld16_sc1(ru, lane * 16 + j * 1024, (s * NCT + c) * 32768);
    if (wave == 0) { flags_wait(p, FL_CB + cg * 8, 1, 8, s + 1, lane, cap); flags_wait(p, FL_PX, 1, 64, s + 1, lane, cap); }
    __syncthreads();
    u32x4 tf[8];
    {
        u32x4 v[10];
#pragma unroll
        for (int i = 0; i < 8; ++i) v[i] = ld16_sc1(rpx, tid * 16 + i * 8192, s * 65536);
#pragma unroll
        for (int i = 0; i < 2; ++i) v[8 + i] = ld16_sc1(rpx, tid * 16 + i * 8192, 4 * MB + s * 16384);
#pragma unroll
        for (int j = 0; j < 8; ++j) tf[j] = ld16_sc1(rt, lane * 16 + j * 1024, t_off(s, tmask) + c * 16384);
#pragma unroll
        for (int i = 0; i < 10; ++i) lds_st16(lds, tid * 16 + i * 8192, v[i]);
    }
    raw_barrier();
    f32x4 d0 = {}, d1 = {};
#pragma unroll
    for (int hh = 0; hh < 32; ++hh) {
        d0 = mf16(uf[hh], lds_ld16(lds, hh * 2048 + lane * 16), d0);
        d1 = mf16(uf[hh], lds_ld16(lds, hh * 2048 + 1024 + lane * 16), d1);
    }
#pragma unroll
    for (int j = 0; j < 8; ++j) {
        d0 = mf16(tf[j], lds_ld16(lds, 65536 + (4 * j + g) * 512 + l16 * 16), d0);
        d1 = mf16(tf[j], lds_ld16(lds, 65536 + (4 * j + g) * 512 + 256 + l16 * 16), d1);
    }
    unsigned short* o = p.out + (size_t)(s * 32 + l16) * (2 * D) + D + c * 16 + 4 * g;
    *(u32x2*)o = u32x2{pk_bf16(d0[0], d0[1]), pk_bf16(d0[2], d0[3])};
    *(u32x2*)(o + 16 * 2 * D) = u32x2{pk_bf16(d1[0], d1[1]), pk_bf16(d1[2], d1[3])};
    __syncthreads();
}
struct cf { float x, y; };
DEV cf cmul(cf a, cf b) { return cf{a.x * b.x - a.y * b.y, a.x * b.y + a.y * b.x}; }
DEV cf cmulc(cf a, cf b) { return cf{a.x * b.x + a.y * b.y, a.y * b.x - a.x * b.y}; }
DEV cf cadd(cf a, cf b) { return cf{a.x + b.x, a.y + b.y}; }
DEV cf csub(cf a, cf b) { return cf{a.x - b.x, a.y - b.y}; }
template <int SG> DEV cf mulmi(cf a) { return SG < 0 ? cf{a.y, -a.x} : cf{-a.y, a.x}; }
template <int SG> DEV void dft4(cf& a, cf& b, cf& c, cf& d) {
    cf t0 = cadd(a, c), t1 = csub(a, c), t2 = cadd(b, d), t3 = mulmi<SG>(csub(b, d));
    a = cadd(t0, t2); c = csub(t0, t2); b = cadd(t1, t3); d = csub(t1, t3);
}
template <int SG> DEV void dft8(cf* v) {
    constexpr float R = 0.70710678118654752f;
    dft4<SG>(v[0], v[2], v[4], v[6]);
    dft4<SG>(v[1], v[3], v[5], v[7]);
    cf o1 = cf{(v[3].x - SG * v[3].y) * R, (v[3].y + SG * v[3].x) * R};
    cf o2 = mulmi<SG>(v[5]);
    cf o3 = cf{(-v[7].x - SG * v[7].y) * R, (-v[7].y + SG * v[7].x) * R};
    cf e0 = v[0], e1 = v[2], e2 = v[4], e3 = v[6], o0 = v[1];
    v[0] = cadd(e0, o0); v[4] = csub(e0, o0);
    v[1] = cadd(e1, o1); v[5] = csub(e1, o1);
    v[2] = cadd(e2, o2); v[6] = csub(e2, o2);
    v[3] = cadd(e3, o3); v[7] = csub(e3, o3);
}
template <int SG> DEV void dft16(cf* v) {
    constexpr float C1 = 0.92387953251128674f, S1 = 0.38268343236508977f, R = 0.70710678118654752f;
#pragma unroll
    for (int b = 0; b < 4; ++b) dft4<SG>(v[b], v[4 + b], v[8 + b], v[12 + b]);
    const cf w1 = {C1, SG * S1}, w2 = {R, SG * R}, w3 = {S1, SG * C1}, w6 = {-R, SG * R}, w9 = {-C1, -SG * S1};
    v[5] = cmul(v[5], w1); v[6] = cmul(v[6], w2); v[7] = cmul(v[7], w3);
    v[9] = cmul(v[9], w2); v[10] = mulmi<SG>(v[10]); v[11] = cmul(v[11], w6);
    v[13] = cmul(v[13], w3); v[14] = cmul(v[14], w6); v[15] = cmul(v[15], w9);
#pragma unroll
    for (int c = 0; c < 4; ++c) dft4<SG>(v[4 * c], v[4 * c + 1], v[4 * c + 2], v[4 * c + 3]);
    cf t;
    t = v[1]; v[1] = v[4]; v[4] = t;  t = v[2]; v[2] = v[8]; v[8] = t;  t = v[3]; v[3] = v[12]; v[12] = t;
    t = v[6]; v[6] = v[9]; v[9] = t;  t = v[7]; v[7] = v[13]; v[13] = t; t = v[11]; v[11] = v[14]; v[14] = t;
}
template <int SG> DEV void dft5(cf* v) {
    constexpr float C1 = 0.30901699437494742f, C2 = -0.80901699437494742f, S1 = 0.95105651629515357f, S2 = 0.58778525229247313f;
    cf a1 = cadd(v[1], v[4]), b1 = csub(v[1], v[4]), a2 = cadd(v[2], v[3]), b2 = csub(v[2], v[3]);
    cf r1 = {v[0].x + C1 * a1.x + C2 * a2.x, v[0].y + C1 * a1.y + C2 * a2.y};
    cf r2 = {v[0].x + C2 * a1.x + C1 * a2.x, v[0].y + C2 * a1.y + C1 * a2.y};
    cf i1 = mulmi<SG>(cf{S1 * b1.x + S2 * b2.x, S1 * b1.y + S2 * b2.y});
    cf i2 = mulmi<SG>(cf{S2 * b1.x - S1 * b2.x, S2 * b1.y - S1 * b2.y});
    v[0] = cadd(v[0], cadd(a1, a2));
    v[1] = cadd(r1, i1); v[4] = csub(r1, i1); v[2] = cadd(r2, i2); v[3] = csub(r2, i2);
}
DEV cf twid(int num, int den) { float s, c; sincospif(-2.0f * (float)num / (float)den, &s, &c); return cf{c, s}; }

constexpr int FRP = 648;
template <int SG> DEV void fft640x8(cf* a, cf* b, const cf* tab, int tid) {
    if (tid < 320) {
        const int q = tid / 40, j = tid % 40; cf v[16];
#pragma unroll
        for (int r = 0; r < 16; ++r) v[r] = a[q * FRP + j + 40 * r];
        dft16<SG>(v);
#pragma unroll
        for (int r = 0; r < 16; ++r) b[q * FRP + 16 * j + (r ^ (j & 15))] = v[r];
    }
    __syncthreads();
    for (int t = tid; t < 640; t += 512) {
        const int q = t / 80, j = t % 80, k = j & 15; cf v[8];
#pragma unroll
        for (int r = 0; r < 8; ++r) { const int n = j + 80 * r; v[r] = b[q * FRP + (n & ~15) + ((n & 15) ^ ((n >> 4) & 15))]; }
#pragma unroll
        for (int r = 1; r < 8; ++r) { const cf w = tab[5 * k * r]; v[r] = SG < 0 ? cmul(v[r], w) : cmulc(v[r], w); }
        dft8<SG>(v);
#pragma unroll
        for (int r = 0; r < 8; ++r) a[q * FRP + (j >> 4) * 128 + k + 16 * r] = v[r];
    }
    __syncthreads();
    for (int t = tid; t < 1024; t += 512) {
        const int q = t >> 7, j = t & 127; cf v[5];
#pragma unroll
        for (int r = 0; r < 5; ++r) v[r] = a[q * FRP + j + 128 * r];
#pragma unroll
        for (int r = 1; r < 5; ++r) { const cf w = tab[j * r]; v[r] = SG < 0 ? cmul(v[r], w) : cmulc(v[r], w); }
        dft5<SG>(v);
#pragma unroll
        for (int r = 0; r < 5; ++r) b[q * FRP + j + 128 * r] = v[r];
    }
    __syncthreads();
}
DEV void make_tab640(cf* tab, int tid) { for (int i = tid; i < 640; i += 512) tab[i] = twid(i, 640); }

DEV void task_fft1(const Params& p, int t, char* lds) {
    const int tid = threadIdx.x;
    cf* a = (cf*)lds; cf* b = a + 8 * FRP; cf* tab = b + 8 * FRP;
    make_tab640(tab, tid);
    for (int i = tid; i < 8 * 80; i += 512) {
        const int q = i / 80, ch = i % 80;
        const u32x4 u = *(const u32x4*)(p.ax + (size_t)(16 * t + 2 * q) * D + ch * 8), w = *(const u32x4*)(p.ax + (size_t)(16 * t + 2 * q + 1) * D + ch * 8);
        cf* d = a + q * FRP + ch * 8;
        d[0] = cf{bf_lo(u.x), bf_lo(w.x)}; d[1] = cf{bf_hi(u.x), bf_hi(w.x)}; d[2] = cf{bf_lo(u.y), bf_lo(w.y)}; d[3] = cf{bf_hi(u.y), bf_hi(w.y)};
        d[4] = cf{bf_lo(u.z), bf_lo(w.z)}; d[5] = cf{bf_hi(u.z), bf_hi(w.z)}; d[6] = cf{bf_lo(u.w), bf_lo(w.w)}; d[7] = cf{bf_hi(u.w), bf_hi(w.w)};
    }
    __syncthreads();
    fft640x8<-1>(a, b, tab, tid);
    const __amdgpu_buffer_rsrc_t rx = mkrsrc(p.ws + OFF_X1, 6 * MB);
    for (int i = tid; i < DF * 8; i += 512) {
        const int k = i >> 3, q = i & 7;
        const cf z = b[q * FRP + k], y = b[q * FRP + (k ? 640 - k : 0)];
        f32x4 o = {0.5f * (z.x + y.x), 0.5f * (z.y - y.y), 0.5f * (z.y + y.y), 0.5f * (y.x - z.x)};
        st16_sc1(__builtin_bit_cast(u32x4, o), rx, (k * S + 16 * t + 2 * q) * 8, 0);
    }
    unsigned* wt = (unsigned*)a;
    for (int i = tid; i < 16 * DF; i += 512) { const int r = i / DF, k = i % DF; wt[k * 17 + r] = (unsigned)p.wr[(size_t)(16 * t + r) * DF + k] | ((unsigned)p.wi[(size_t)(16 * t + r) * DF + k] << 16); }
    __syncthreads();
    const __amdgpu_buffer_rsrc_t rw = mkrsrc(p.ws + OFF_WT, 3 * MB);
    for (int i = tid; i < DF * 4; i += 512) { const int k = i >> 2, r4 = (i & 3) * 4;
        u32x4 v = {wt[k * 17 + r4], wt[k * 17 + r4 + 1], wt[k * 17 + r4 + 2], wt[k * 17 + r4 + 3]};
        st16_sc1(v, rw, (k * S + 16 * t + r4) * 4, 0); }
    __syncthreads();
}

constexpr int CP = 2048 + 128;
DEV int cpad(int i) { return i + (i >> 4); }
DEV void fft2_twiddles(cf (&t1)[16], cf (&t2)[16]) {
    const int m = threadIdx.x & 127, n2 = m >> 4;
#pragma unroll
    for (int i = 1; i < 16; ++i) { t1[i] = twid(m * i, 2048); t2[i] = twid(n2 * i, 128); }
}
DEV void task_fft2(const Params& p, int t, char* lds, cf (&t1)[16], cf (&t2)[16]) {
    int tid = threadIdx.x;
    asm volatile("" : "+v"(tid));
#pragma unroll
    for (int i = 1; i < 16; ++i) asm volatile("" : "+v"(t1[i].x), "+v"(t1[i].y), "+v"(t2[i].x), "+v"(t2[i].y));
    const int col = tid >> 7, m = tid & 127;
    const int k = 4 * t + col;
    const bool act = k < DF;
    cf* buf = (cf*)lds + col * CP;
    const __amdgpu_buffer_rsrc_t rx = mkrsrc(p.ws + OFF_X1, (unsigned)(DF * S * 8)), ry = mkrsrc(p.ws + OFF_Y1, (unsigned)(DF * S * 8)), rw = mkrsrc(p.ws + OFF_WT, (unsigned)(DF * S * 4));
    cf v[16];
#pragma unroll
    for (int n0 = 0; n0 < 16; ++n0) { u32x2 u = ld8_sc1(rx, (k * S + 128 * n0 + m) * 8, 0); v[n0] = __builtin_bit_cast(cf, u); }
    const int f0b = m & 15, n2 = m >> 4;
    const int f1c = m >> 4;
    unsigned wv[16];
#pragma unroll
    for (int u = 0; u < 2; ++u)
#pragma unroll
        for (int f2 = 0; f2 < 8; ++f2) wv[u * 8 + f2] = __builtin_amdgcn_raw_buffer_load_b32(rw, (k * S + f0b + 16 * (f1c + 8 * u) + 256 * f2) * 4, 0, 16);
    dft16<-1>(v);
#pragma unroll
    for (int f0 = 1; f0 < 16; ++f0) v[f0] = cmul(v[f0], t1[f0]);
#pragma unroll
    for (int f0 = 0; f0 < 16; ++f0) buf[cpad(m * 16 + f0)] = v[f0];
    __syncthreads();
#pragma unroll
    for (int n1 = 0; n1 < 16; ++n1) v[n1] = buf[cpad((8 * n1 + n2) * 16 + f0b)];
    dft16<-1>(v);
#pragma unroll
    for (int f1 = 1; f1 < 16; ++f1) v[f1] = cmul(v[f1], t2[f1]);
    __syncthreads();
#pragma unroll
    for (int f1 = 0; f1 < 16; ++f1) buf[cpad((f1 * 8 + n2) * 16 + f0b)] = v[f1];
    __syncthreads();
#pragma unroll
    for (int u = 0; u < 2; ++u)
#pragma unroll
        for (int j = 0; j < 8; ++j) v[u * 8 + j] = buf[cpad(((f1c + 8 * u) * 8 + j) * 16 + f0b)];
    dft8<-1>(v); dft8<-1>(v + 8);
    constexpr float NRM = 1.0f / (2048.0f * 640.0f);
#pragma unroll
    for (int i = 0; i < 16; ++i) v[i] = cmul(v[i], cf{bf_lo(wv[i]) * NRM, bf_hi(wv[i]) * NRM});
    dft8<1>(v); dft8<1>(v + 8);
#pragma unroll
    for (int u = 0; u < 2; ++u)
#pragma unroll
        for (int j = 0; j < 8; ++j) buf[cpad(((f1c + 8 * u) * 8 + j) * 16 + f0b)] = v[u * 8 + j];
    __syncthreads();
#pragma unroll
    for (int f1 = 0; f1 < 16; ++f1) v[f1] = buf[cpad((f1 * 8 + n2) * 16 + f0b)];
#pragma unroll
    for (int f1 = 1; f1 < 16; ++f1) v[f1] = cmulc(v[f1], t2[f1]);
    dft16<1>(v);
#pragma unroll
    for (int n1 = 0; n1 < 16; ++n1) buf[cpad((8 * n1 + n2) * 16 + f0b)] = v[n1];
    __syncthreads();
#pragma unroll
    for (int f0 = 0; f0 < 16; ++f0) v[f0] = buf[cpad(m * 16 + f0)];
#pragma unroll
    for (int f0 = 1; f0 < 16; ++f0) v[f0] = cmulc(v[f0], t1[f0]);
    dft16<1>(v);
    if (act) {
#pragma unroll
        for (int n0 = 0; n0 < 16; ++n0) st8_sc1(__builtin_bit_cast(u32x2, v[n0]), ry, (k * S + 128 * n0 + m) * 8, 0);
    }
    __syncthreads();
}

DEV void task_fft3(const Params& p, int t, char* lds) {
    const int tid = threadIdx.x;
    cf* a = (cf*)lds; cf* b = a + 8 * FRP; cf* tab = b + 8 * FRP;
    make_tab640(tab, tid);
    const __amdgpu_buffer_rsrc_t ry = mkrsrc(p.ws + OFF_Y1, 6 * MB);
    for (int i = tid; i < DF * 8; i += 512) {
        const int k = i >> 3, q = i & 7;
        f32x4 o = __builtin_bit_cast(f32x4, ld16_sc1(ry, (k * S + 16 * t + 2 * q) * 8, 0));
        if (k == 0 || k == 320) { o[1] = 0.f; o[3] = 0.f; }
        a[q * FRP + k] = cf{o[0] - o[3], o[1] + o[2]};
        if (k > 0 && k < 320) a[q * FRP + 640 - k] = cf{o[0] + o[3], o[2] - o[1]};
    }
    __syncthreads();
    fft640x8<1>(a, b, tab, tid);
    for (int i = tid; i < 8 * 80; i += 512) {
        const int q = i / 80, ch = i % 80;
        const cf* d = b + q * FRP + ch * 8;
        u32x4 u = {pk_bf16(d[0].x, d[1].x), pk_bf16(d[2].x, d[3].x), pk_bf16(d[4].x, d[5].x), pk_bf16(d[6].x, d[7].x)};
        u32x4 w = {pk_bf16(d[0].y, d[1].y), pk_bf16(d[2].y, d[3].y), pk_bf16(d[4].y, d[5].y), pk_bf16(d[6].y, d[7].y)};
        *(u32x4*)(p.out + (size_t)(16 * t + 2 * q) * (2 * D) + ch * 8) = u;
        *(u32x4*)(p.out + (size_t)(16 * t + 2 * q + 1) * (2 * D) + ch * 8) = w;
    }
    __syncthreads();
}

constexpr int NT1 = 128, NT2 = 81, NT3 = 128, NTO = NSEG * 5, NTASK = NT1 + NT2 + NT3 + NTO;
__global__ void __launch_bounds__(512, 2) k_main(Params p) {
    extern __shared__ __attribute__((aligned(16))) char lds[];
#define TL(slot) do {} while (0)
    const int role = get_role(p, lds);
    TL(1);
    if (role < 32) { role_qk(p, role, lds); TL(7); return; }
    if (role < 64) { role_vo(p, role - 32, lds); TL(7); return; }
    if (role < NCHAIN) { role_comb(p, role - 64, lds); TL(7); return; }
    const int w = role - NCHAIN, tid = threadIdx.x, lane = tid & 63;
    if (w < 64) role_gemm_qk(p, w >> 1, w & 1, lds);
    else if (w < 128) role_gemm_vu(p, (w - 64) >> 1, w & 1, lds);
    else if (tid < 64 && w - 128 < 1 + NTPG) probe_page(p, w - 128, lane);
    TL(2);
    int cap = SPIN_CAP;
#define NEXT_TASK(Q) (__syncthreads(), bcast0(threadIdx.x == 0 ? (int)flag_add(p, Q) : 0, lds))
    if (w < 128) {
        int t = NEXT_TASK(FL_QUEUE);
        while (t < NT1) {
            task_fft1(p, t, lds);
            WAIT_VM(0); __syncthreads();
            if (tid == 0) flag_add(p, FL_F1);
            t = NEXT_TASK(FL_QUEUE);
        }
        TL(3);
        cf tw1[16], tw2[16];
        if (t < NT1 + NT2) fft2_twiddles(tw1, tw2);
        while (t < NT1 + NT2) {
            if (tid < 64) flags_wait(p, FL_F1, 0, 1, NT1, lane, cap);
            __syncthreads();
            task_fft2(p, t - NT1, lds, tw1, tw2);
            WAIT_VM(0); __syncthreads();
            if (tid == 0) flag_add(p, FL_F2);
            t = NEXT_TASK(FL_QUEUE);
        }
        TL(4);
        while (t < NT1 + NT2 + NT3) {
            if (tid < 64) flags_wait(p, FL_F2, 0, 1, NT2, lane, cap);
            __syncthreads();
            task_fft3(p, t - NT1 - NT2, lds);
            t = NEXT_TASK(FL_QUEUE);
        }
        TL(5);
    } else {
        int t = NEXT_TASK(FL_QOUT);
        while (t < NTO) {
            task_out(p, t / 5, t % 5, lds, cap);
            t = NEXT_TASK(FL_QOUT);
        }
    }
    TL(7);
}

extern "C" void kernel_launch(void* const* d_in, const int* in_sizes, int n_in, void* d_out, int out_size, void* d_ws, size_t ws_size, hipStream_t stream) {
    Params p;
    p.ax = (const unsigned short*)d_in[0]; p.wr = (const unsigned short*)d_in[1]; p.wi = (const unsigned short*)d_in[2];
    p.bx = (const unsigned short*)d_in[3]; p.mem0 = (const unsigned short*)d_in[4];
    p.wq = (const unsigned short*)d_in[5]; p.wk = (const unsigned short*)d_in[6]; p.wv = (const unsigned short*)d_in[7]; p.wo = (const unsigned short*)d_in[8];
    p.out = (unsigned short*)d_out;
    p.ws = (char*)(((size_t)d_ws + 2 * MB - 1) & ~(size_t)(2 * MB - 1));
    constexpr int LDS = 131072;
    (void)hipFuncSetAttribute((const void*)k_main, hipFuncAttributeMaxDynamicSharedMemorySize, LDS);
    (void)hipMemsetAsync(p.ws + OFF_CTRL, 0, FLAG_BYTES, stream);
    k_main<<<NWG, 512, LDS, stream>>>(p);
}
```

```cpp
#include <hip/hip_runtime.h>
#include <hip/hip_bf16.h>

typedef __bf16 bf16x8 __attribute__((ext_vector_type(8)));
typedef __bf16 bf16x16 __attribute__((ext_vector_type(16)));
typedef __bf16 bf16x2 __attribute__((ext_vector_type(2)));
typedef short s16x4 __attribute__((ext_vector_type(4)));
typedef float f32x2 __attribute__((ext_vector_type(2)));
typedef float f32x4 __attribute__((ext_vector_type(4)));
typedef float f32x16 __attribute__((ext_vector_type(16)));
typedef unsigned u32x2 __attribute__((ext_vector_type(2)));
typedef unsigned u32x4 __attribute__((ext_vector_type(4)));
typedef unsigned u32x8 __attribute__((ext_vector_type(8)));
#define LAS __attribute__((address_space(3)))
#define GAS __attribute__((address_space(1)))
#define DEV __device__ __forceinline__

constexpr int S = 2048, D = 640, DF = 321, H = 32, DH = 128, NSEG = 64, NCT = 40;
constexpr unsigned SENT = 0xFFFFFFFFu;
constexpr int SPIN_CAP = 1 << 15;

constexpr size_t MB = 1 << 20;
constexpr size_t OFF_CTRL = 0;
constexpr size_t OFF_COMM0 = 2 * MB;
constexpr size_t OFF_QX = 6 * MB;
constexpr size_t OFF_KX = OFF_QX + 16 * MB;
constexpr size_t OFF_U = OFF_KX + 16 * MB;
constexpr size_t OFF_T = OFF_U + 80 * MB;
constexpr int NTPG = 22;
constexpr size_t OFF_PX = OFF_T + 44 * MB;
constexpr size_t OFF_PXM = OFF_PX + 4 * MB;
constexpr size_t OFF_X1 = OFF_PXM + 1 * MB;
constexpr size_t OFF_Y1 = OFF_X1 + 6 * MB;
constexpr size_t OFF_WT = OFF_Y1 + 6 * MB;
constexpr size_t WS_NEED = OFF_WT + 3 * MB;

constexpr int RM = 4, RP = 2;
DEV unsigned mema_off(int sl, int r) { return (sl * 4 + r) * 8192; }
DEV unsigned memb_off(int sl, int r) { return (16 + sl) * 8192 + r * 2048; }
DEV unsigned mem_off(int sl, int r, int c) { return c < 32 ? mema_off(sl, r) + c * 256 : memb_off(sl, r) + (c - 32) * 256; }
DEV unsigned px_base(int sl, int r) { return (20 + (sl * 2 + r) * 2) * 8192; }
DEV unsigned pm_base(int sl, int r) { return (36 + sl) * 8192 + r * 4096; }
constexpr unsigned ANN_BASE = 40 * 8192, STA_BASE = 41 * 8192, STB_BASE = 42 * 8192, COMM_CHUNKS = 44;
DEV unsigned rowi(unsigned m, unsigned t) { return 4 * (m >> 1) + 2 * t + (m & 1); }
DEV unsigned physo(unsigned o) { return ((o >> 13) << 14) + (o & 8191u); }
DEV unsigned t_off(int s, unsigned long long tmask) { const int pg = s / 3; return pg * (2u << 20) + (s - 3 * pg) * (NCT * 16384) + (((unsigned)(tmask >> pg) & 1u) << 13); }
constexpr int FL_TNEAR = 0, FL_TFAR = 1, FL_QUEUE = 2, FL_READY = 3, FL_F1 = 4, FL_F2 = 5, FL_TMO = 6, FL_QOUT = 7, FL_SIDE = 8, FL_GQ = 32, FL_GU = 96, FL_PX = 160, FL_CB = 224, FL_END = 264;
constexpr int FLAG_BYTES = FL_END * 128;
constexpr int TUNE_W = 16384;
struct Params {
    const unsigned short* ax; const unsigned short* wr; const unsigned short* wi;
    const unsigned short* bx; const unsigned short* mem0;
    const unsigned short* wq; const unsigned short* wk; const unsigned short* wv; const unsigned short* wo;
    unsigned short* out; char* ws;
};

DEV __amdgpu_buffer_rsrc_t mkrsrc(const void* p, unsigned bytes) { return __builtin_amdgcn_make_buffer_rsrc((void*)p, 0, bytes, 0x00020000); }
DEV u32x4 ld16_sc1(__amdgpu_buffer_rsrc_t r, unsigned voff, unsigned soff) { return __builtin_amdgcn_raw_buffer_load_b128(r, voff, soff, 16); }
DEV u32x2 ld8_sc1(__amdgpu_buffer_rsrc_t r, unsigned voff, unsigned soff) { return __builtin_amdgcn_raw_buffer_load_b64(r, voff, soff, 16); }
DEV void st16_sc1(u32x4 v, __amdgpu_buffer_rsrc_t r, unsigned voff, unsigned soff) { __builtin_amdgcn_raw_buffer_store_b128(v, r, voff, soff, 16); asm volatile("s_nop 1" :: "v"(v) : "memory"); }
DEV void st8_sc1(u32x2 v, __amdgpu_buffer_rsrc_t r, unsigned voff, unsigned soff) { __builtin_amdgcn_raw_buffer_store_b64(v, r, voff, soff, 16); }
DEV void st4_sc1(unsigned v, __amdgpu_buffer_rsrc_t r, unsigned voff, unsigned soff) { __builtin_amdgcn_raw_buffer_store_b32(v, r, voff, soff, 16); }
DEV unsigned pk_bf16(float a, float b) { f32x2 f = {a, b}; bf16x2 p = __builtin_convertvector(f, bf16x2); return __builtin_bit_cast(unsigned, p); }
DEV float bf_lo(unsigned u) { return __builtin_bit_cast(float, u << 16); }
DEV float bf_hi(unsigned u) { return __builtin_bit_cast(float, u & 0xFFFF0000u); }
DEV unsigned nosent(unsigned u) { return u < 0xFFFFFFFEu ? u : 0xFFFFFFFEu; }
DEV unsigned mx4(u32x4 v) { return max(max(v.x, v.y), max(v.z, v.w)); }
DEV u32x4 mk4(u32x2 a, u32x2 b) { return u32x4{a.x, a.y, b.x, b.y}; }
DEV bf16x8 as_bf8(u32x4 v) { return __builtin_bit_cast(bf16x8, v); }
DEV u32x2 tr_read(const char* lds, unsigned off) { s16x4 v = __builtin_amdgcn_ds_read_tr16_b64_v4i16((LAS s16x4*)(lds + off)); return __builtin_bit_cast(u32x2, v); }
DEV u32x4 lds_ld16(const char* lds, unsigned off) { return *(const u32x4*)(lds + off); }
DEV void lds_st16(char* lds, unsigned off, u32x4 v) { *(u32x4*)(lds + off) = v; }
DEV void glds16(const void* g, char* lds) { __builtin_amdgcn_global_load_lds((const GAS unsigned*)g, (LAS unsigned*)lds, 16, 0, 0); }
#define WAIT_VM(n) asm volatile("s_waitcnt vmcnt(" #n ")" ::: "memory")
#define WAIT_LGKM0() asm volatile("s_waitcnt lgkmcnt(0)" ::: "memory")
DEV void raw_barrier() { asm volatile("" ::: "memory"); __builtin_amdgcn_s_waitcnt(0xC07F); __builtin_amdgcn_s_barrier(); asm volatile("" ::: "memory"); }
DEV float max3f(float a, float b, float c) { return __builtin_fmaxf(__builtin_fmaxf(a, b), c); }
template <class T> DEV T lds_vol_ld(const char* p) { return *(volatile LAS T*)p; }
template <class T> DEV void lds_vol_st(char* p, T v) { *(volatile LAS T*)p = v; }

DEV unsigned swz(unsigned row) { return ((row & 3u) << 2) | ((row >> 2) & 3u); }
DEV unsigned img_off(unsigned row, unsigned ch) { return 256u * row + 16u * (ch ^ swz(row)); }
DEV void bdma16(__amdgpu_buffer_rsrc_t r, char* lds, unsigned voff, unsigned soff) { __builtin_amdgcn_raw_ptr_buffer_load_lds(r, (LAS void*)lds, 16, voff, soff, 0, 0); }
template <int NROWS>
DEV void stage_img(__amdgpu_buffer_rsrc_t r, unsigned elem0, unsigned pitch, char* img, int wave, int lane) {
    const unsigned row0 = 4 * wave + (lane >> 4), voff = (row0 * pitch + (((lane & 15) ^ swz(row0)) << 3)) * 2;
#pragma unroll
    for (int it = 0; it < NROWS / 32; ++it) bdma16(r, img + (wave + 8 * it) * 1024, voff, (elem0 + it * 32 * pitch) * 2);
}
DEV unsigned lds_addr(const char* p) { return (unsigned)(size_t)(const LAS char*)p; }
DEV void tr8(u32x2 (&o)[8], const unsigned (&a)[8]) {
    asm volatile("ds_read_b64_tr_b16 %0, %8\n\tds_read_b64_tr_b16 %1, %9\n\tds_read_b64_tr_b16 %2, %10\n\tds_read_b64_tr_b16 %3, %11\n\t"
                 "ds_read_b64_tr_b16 %4, %12\n\tds_read_b64_tr_b16 %5, %13\n\tds_read_b64_tr_b16 %6, %14\n\tds_read_b64_tr_b16 %7, %15\n\ts_waitcnt lgkmcnt(0)"
                 : "=&v"(o[0]), "=&v"(o[1]), "=&v"(o[2]), "=&v"(o[3]), "=&v"(o[4]), "=&v"(o[5]), "=&v"(o[6]), "=&v"(o[7])
                 : "v"(a[0]), "v"(a[1]), "v"(a[2]), "v"(a[3]), "v"(a[4]), "v"(a[5]), "v"(a[6]), "v"(a[7]) : "memory");
}
DEV unsigned a16(const char* img, unsigned r0, unsigned c, int lane) {
    const unsigned q = (lane & 15) >> 2, p = lane & 3;
    return lds_addr(img) + img_off(r0 + q, 2 * c + (p >> 1)) + 8 * (p & 1);
}
DEV unsigned a32(const char* img, unsigned r0, unsigned c, int lane) {
    const unsigned blk = (lane >> 4) & 1, q = (lane & 15) >> 2, p = lane & 3;
    return lds_addr(img) + img_off(r0 + q, 4 * c + 2 * blk + (p >> 1)) + 8 * (p & 1);
}
DEV unsigned a16p(const char* img, unsigned k0, unsigned c, int lane) {
    const unsigned q = (lane & 15) >> 2, p = lane & 3;
    const unsigned row = (k0 & ~15u) + 8 * (q >> 1) + 2 * ((k0 & 15u) >> 2) + (q & 1);
    return lds_addr(img) + img_off(row, 2 * c + (p >> 1)) + 8 * (p & 1);
}
template <bool PERM>
DEV void load_bsp2(bf16x16& x, bf16x16& y, const char* imgx, unsigned cx, const char* imgy, unsigned cy, int lane) {
    const unsigned g = lane >> 4;
    unsigned a[8]; u32x2 o[8];
#pragma unroll
    for (int i = 0; i < 4; ++i) {
        const unsigned k0 = 32 * (i >> 1) + 8 * g + 4 * (i & 1);
        a[i] = PERM ? a16p(imgx, k0, cx, lane) : a16(imgx, k0, cx, lane); a[4 + i] = PERM ? a16p(imgy, k0, cy, lane) : a16(imgy, k0, cy, lane);
    }
    tr8(o, a);
    u32x8 r = {o[0].x, o[0].y, o[1].x, o[1].y, o[2].x, o[2].y, o[3].x, o[3].y}, t = {o[4].x, o[4].y, o[5].x, o[5].y, o[6].x, o[6].y, o[7].x, o[7].y};
    x = __builtin_bit_cast(bf16x16, r); y = __builtin_bit_cast(bf16x16, t);
}
DEV f32x4 smf(u32x4 a, bf16x16 b, f32x4 c, int idx) { return __builtin_amdgcn_smfmac_f32_16x16x64_bf16(as_bf8(a), b, c, idx, 0, 0); }
DEV f32x4 mf16(u32x4 a, u32x4 b, f32x4 c) { return __builtin_amdgcn_mfma_f32_16x16x32_bf16(as_bf8(a), as_bf8(b), c, 0, 0, 0); }
DEV f32x16 mf32(u32x4 a, u32x4 b, f32x16 c) { return __builtin_amdgcn_mfma_f32_32x32x16_bf16(as_bf8(a), as_bf8(b), c, 0, 0, 0); }
DEV float u2f(unsigned u) { return __builtin_bit_cast(float, u); }
DEV unsigned f2u(float f) { return __builtin_bit_cast(unsigned, f); }
DEV float xmax16_32(float x) {
    auto r = __builtin_amdgcn_permlane16_swap(f2u(x), f2u(x), false, false);
    x = fmaxf(u2f(r[0]), u2f(r[1]));
    auto r2 = __builtin_amdgcn_permlane32_swap(f2u(x), f2u(x), false, false);
    return fmaxf(u2f(r2[0]), u2f(r2[1]));
}
DEV float xsum16_32(float x) {
    auto r = __builtin_amdgcn_permlane16_swap(f2u(x), f2u(x), false, false);
    x = u2f(r[0]) + u2f(r[1]);
    auto r2 = __builtin_amdgcn_permlane32_swap(f2u(x), f2u(x), false, false);
    return u2f(r2[0]) + u2f(r2[1]);
}

#define ST(role, s, slot) do {} while (0)
#define MT(x) do {} while (0)
#define MTW() do {} while (0)
#define PACC(acc, a, b) do {} while (0)
#define PDUMP(role, x, y, z, w) do {} while (0)
#define WS(k) do {} while (0)
#define WSDECL do {} while (0)
#define WSDUMP() do {} while (0)
#define TLR(role, slot) do {} while (0)
DEV unsigned* flag_ptr(const Params& p, int line) { return (unsigned*)(p.ws + OFF_CTRL) + line * 32; }
DEV void flag_set(const Params& p, int line, unsigned v) { __hip_atomic_store(flag_ptr(p, line), v, __ATOMIC_RELAXED, __HIP_MEMORY_SCOPE_AGENT); }
DEV unsigned flag_add(const Params& p, int line) { return __hip_atomic_fetch_add(flag_ptr(p, line), 1u, __ATOMIC_RELAXED, __HIP_MEMORY_SCOPE_AGENT); }
DEV unsigned flag_get(const Params& p, int line) { return __hip_atomic_load(flag_ptr(p, line), __ATOMIC_RELAXED, __HIP_MEMORY_SCOPE_AGENT); }
DEV unsigned flags_wait(const Params& p, int line0, int stride, int n, unsigned want, int lane, int& cap) {
    unsigned v = 0;
    for (int sp = 0;; ++sp) {
        v = flag_get(p, line0 + stride * (lane < n ? lane : 0));
        if (__all(v >= want)) break;
        if (sp > cap) { flag_set(p, FL_TMO, 1u); cap = 0; break; }
        __builtin_amdgcn_s_sleep(4);
    }
    asm volatile("" ::: "memory");
    return v;
}
#define TUNEW(i, dflt) (dflt)
DEV void probe_page(const Params& p, int pg, int lane) {
    const unsigned xcc = __builtin_amdgcn_s_getreg(20 | (3 << 11)) & 0xf;
    unsigned best[2] = {1u << 30, 1u << 30};
    char* base = p.ws + (pg < 1 ? OFF_COMM0 : OFF_T + (pg - 1) * 2 * MB) + 2 * MB - 16384;
    const __amdgpu_buffer_rsrc_t r = mkrsrc(base, 16384);
    const u32x4 sv = {SENT, SENT, SENT, SENT};
    st16_sc1(sv, r, lane * 128, 0); st16_sc1(sv, r, lane * 128, 8192);
    WAIT_VM(0);
#pragma unroll 1
    for (int i = 0; i < 6; ++i) {
#pragma unroll
        for (int c = 0; c < 2; ++c) {
            const char* q = base + c * 8192 + i * 768;
            unsigned long long t0, t1; unsigned v;
            asm volatile("s_memtime %0\n\ts_waitcnt lgkmcnt(0)\n\tglobal_load_dword %2, %3, off sc1\n\ts_waitcnt vmcnt(0)\n\ts_memtime %1\n\ts_waitcnt lgkmcnt(0)"
                         : "=&s"(t0), "=&s"(t1), "=&v"(v) : "v"(q) : "memory");
            best[c] = min(best[c], (unsigned)(t1 - t0));
        }
    }
    unsigned side = best[0] < best[1] ? 0u : 1u;
    if (xcc >= 4) side ^= 1u;
    if (lane == 0) flag_set(p, FL_SIDE + pg, 1u + side);
}
DEV unsigned long long wait_sides(const Params& p, int lane, int& cap) {
    const unsigned v = flags_wait(p, FL_SIDE, 1, 1 + NTPG, 1u, lane, cap);
    return __ballot(lane < 1 + NTPG && v == 2u);
}

constexpr unsigned WBYTES = D * H * DH * 2;
#define STAGE_RING(RING, EXTRA, ISSUE, READ) do { \
        _Pragma("unroll") for (int bt = 0; bt < RING - 1; ++bt) { char* img = lds + bt * 32768; ISSUE; } \
        _Pragma("unroll") for (int bq = 0; bq < 10; ++bq) { \
            if (bq + RING - 1 <= 10) asm volatile("s_waitcnt vmcnt(%0)" :: "i"(4 * (RING - 2)) : "memory"); \
            else asm volatile("s_waitcnt vmcnt(%0)" :: "i"(4 * (9 - bq) + (EXTRA)) : "memory"); \
            raw_barrier(); \
            if (bq + RING - 1 < 10) { const int bt = bq + RING - 1; char* img = lds + (bt % RING) * 32768; ISSUE; } \
            else if (bq + RING - 1 == 10) { AFTER_W; } \
            { const int bt = bq; const char* img = lds + (bt % RING) * 32768; READ; } \
        } \
        raw_barrier(); \
    } while (0)
constexpr int XT_BYTES = 40960;
DEV void stage_x(__amdgpu_buffer_rsrc_t rx, int seg, char* img, int wave, int lane) {
    const int tok = 8 * (wave & 3) + (lane >> 3), c = (lane & 7) ^ ((tok >> 1) & 7);
    const unsigned voff = (tok * D + (wave >> 2) * 64 + c * 8) * 2;
#pragma unroll
    for (int it = 0; it < 5; ++it) bdma16(rx, img + (wave + 8 * it) * 1024, voff, (seg * 32 * D + it * 128) * 2);
}
DEV unsigned xfrag_off(int ks, int lane) {
    const int tok = lane & 31, hi = lane >> 5, c = 2 * (ks & 3) + hi;
    return (ks >> 2) * 4096 + tok * 128 + ((c ^ ((tok >> 1) & 7)) << 4);
}

DEV void role_gemm_qk(const Params& p, int h, int par, char* lds) {
    const int tid = threadIdx.x, lane = tid & 63, wave = __builtin_amdgcn_readfirstlane(tid >> 6);
    u32x4 w[40];
    const __amdgpu_buffer_rsrc_t rw0 = mkrsrc(p.wq, WBYTES), rw1 = mkrsrc(p.wk, WBYTES), rx = mkrsrc(p.bx, S * D * 2);
    for (int i = 0; i < par * TUNEW(7, 0); ++i) __builtin_amdgcn_s_sleep(2);
    char* xbuf = lds + 40960;
#define XSLOT(i) (lds + ((i) % 3 == 0 ? 3 * XT_BYTES : ((i) % 3 - 1) * XT_BYTES))
    (void)xbuf;
#define AFTER_W stage_x(rx, par, XSLOT(0), wave, lane)
    STAGE_RING(3, 5,
        do { stage_img<64>(rw0, bt * 64 * H * DH + h * DH, H * DH, img, wave, lane); stage_img<64>(rw1, bt * 64 * H * DH + h * DH, H * DH, img + 16384, wave, lane); } while (0),
        do { unsigned a[8]; u32x2 o[8];
             _Pragma("unroll") for (int i = 0; i < 8; ++i) a[i] = a32(img + (wave >> 2) * 16384, 16 * (i >> 1) + 8 * (lane >> 5) + 4 * (i & 1), wave & 3, lane);
             tr8(o, a);
             _Pragma("unroll") for (int ks = 0; ks < 4; ++ks) w[bt * 4 + ks] = mk4(o[2 * ks], o[2 * ks + 1]); } while (0));
#undef AFTER_W
    TLR(104 + 2 * h + par, 6);
    const __amdgpu_buffer_rsrc_t ro = mkrsrc(p.ws + (wave < 4 ? OFF_QX : OFF_KX), TUNEW(4, 0) ? 0 : 16 * MB);
    stage_x(rx, par + 2, XSLOT(1), wave, lane);
    for (int i = 0; i < 32; ++i) {
        const int seg = par + 2 * i;
        char* img = XSLOT(i);
        if (i == 0) WAIT_VM(5); else if (i <= 2) WAIT_VM(0); else if (i < 31) WAIT_VM(7); else WAIT_VM(2);
        raw_barrier();
        if (tid == 0 && i >= 1 && i != 3) flag_set(p, FL_GQ + 2 * h + par, i <= 2 ? i : i - 1);
        if (i + 2 < 32) stage_x(rx, seg + 4, XSLOT(i + 2), wave, lane);
        f32x16 acc = {};
        {
            u32x4 xf[2][4];
#pragma unroll
            for (int j = 0; j < 4; ++j) xf[0][j] = lds_ld16(img, xfrag_off(j, lane));
#pragma unroll
            for (int bq = 0; bq < 10; ++bq) {
                if (bq + 1 < 10) {
#pragma unroll
                    for (int j = 0; j < 4; ++j) xf[(bq + 1) & 1][j] = lds_ld16(img, xfrag_off(4 * (bq + 1) + j, lane));
                }
                __builtin_amdgcn_sched_barrier(0);
#pragma unroll
                for (int j = 0; j < 4; ++j) acc = mf32(w[4 * bq + j], xf[bq & 1][j], acc);
                __builtin_amdgcn_sched_barrier(0);
            }
        }
#pragma unroll
        for (int a = 0; a < 4; a += 2) {
            unsigned x0 = pk_bf16(acc[4 * a], acc[4 * a + 1]), x1 = pk_bf16(acc[4 * a + 2], acc[4 * a + 3]);
            unsigned y0 = pk_bf16(acc[4 * a + 4], acc[4 * a + 5]), y1 = pk_bf16(acc[4 * a + 6], acc[4 * a + 7]);
            auto r0 = __builtin_amdgcn_permlane32_swap(x0, y0, false, false);
            auto r1 = __builtin_amdgcn_permlane32_swap(x1, y1, false, false);
            u32x4 v = {r0[0], r1[0], r0[1], r1[1]};
            st16_sc1(v, ro, ((4 * (wave & 3) + a) * 32 + lane) * 16, (seg * H + h) * 8192);
        }
    }
    WAIT_VM(0);
    raw_barrier();
    if (tid == 0) flag_set(p, FL_GQ + 2 * h + par, 32);
}

DEV void role_gemm_vu(const Params& p, int h, int par, char* lds) {
    const int tid = threadIdx.x, lane = tid & 63, wave = __builtin_amdgcn_readfirstlane(tid >> 6);
    const int et = wave & 3, kh = wave >> 2, g = lane >> 4;
    u32x4 wv[20], wo[5][4];
    const __amdgpu_buffer_rsrc_t rw0 = mkrsrc(p.wv, WBYTES), rw1 = mkrsrc(p.wo, WBYTES), rx = mkrsrc(p.bx, S * D * 2);
    for (int i = 0; i < par * TUNEW(7, 0); ++i) __builtin_amdgcn_s_sleep(2);
#define XS2(i) (lds + (((i) & 1) ? 0 : 3 * XT_BYTES))
#define AFTER_W stage_x(rx, par, XS2(0), wave, lane)
    STAGE_RING(3, 5,
        do { stage_img<32>(rw0, bt * 32 * H * DH + h * DH, H * DH, img, wave, lane); stage_img<32>(rw0, (320 + bt * 32) * H * DH + h * DH, H * DH, img + 8192, wave, lane);
             stage_img<64>(rw1, (h * DH + (bt & 1) * 64) * D + (bt >> 1) * 128, D, img + 16384, wave, lane); } while (0),
        do { unsigned a[8]; u32x2 o[8];
             _Pragma("unroll") for (int i = 0; i < 4; ++i) { a[i] = a32(img + kh * 8192, 16 * (i >> 1) + 8 * (lane >> 5) + 4 * (i & 1), et, lane);
                 a[4 + i] = a16(img + 16384, 32 * (i >> 1) + 16 * (g >> 1) + 4 * (g & 1) + 8 * (i & 1), wave, lane); }
             tr8(o, a);
             _Pragma("unroll") for (int ks = 0; ks < 2; ++ks) { wv[bt * 2 + ks] = mk4(o[2 * ks], o[2 * ks + 1]); wo[bt >> 1][2 * (bt & 1) + ks] = mk4(o[4 + 2 * ks], o[5 + 2 * ks]); } } while (0));
#undef AFTER_W
    TLR(168 + 2 * h + par, 6);
    char* xch = lds + XT_BYTES;
    char* vimg = xch + 32768;
    const __amdgpu_buffer_rsrc_t ru = mkrsrc(p.ws + OFF_U, TUNEW(4, 0) ? 0 : 80 * MB);
    f32x4 k0, k1;
#define VU_P1(i) do { \
        const char* img = XS2(i) + kh * 20480; \
        f32x16 acc = {}; \
        u32x4 xf[2][4]; \
        _Pragma("unroll") for (int j = 0; j < 4; ++j) xf[0][j] = lds_ld16(img, xfrag_off(j, lane)); \
        _Pragma("unroll") for (int bq = 0; bq < 5; ++bq) { \
            if (bq + 1 < 5) { _Pragma("unroll") for (int j = 0; j < 4; ++j) xf[(bq + 1) & 1][j] = lds_ld16(img, xfrag_off(4 * (bq + 1) + j, lane)); } \
            __builtin_amdgcn_sched_barrier(0); \
            _Pragma("unroll") for (int j = 0; j < 4; ++j) acc = mf32(wv[4 * bq + j], xf[bq & 1][j], acc); \
            __builtin_amdgcn_sched_barrier(0); \
        } \
        const f32x4 lo0 = {acc[0], acc[1], acc[2], acc[3]}, lo1 = {acc[4], acc[5], acc[6], acc[7]}, hi0 = {acc[8], acc[9], acc[10], acc[11]}, hi1 = {acc[12], acc[13], acc[14], acc[15]}; \
        char* xo = xch + ((i) & 1) * 16384 + wave * 2048 + lane * 16; \
        *(f32x4*)xo = KH ? lo0 : hi0; *(f32x4*)(xo + 1024) = KH ? lo1 : hi1; \
        k0 = KH ? hi0 : lo0; k1 = KH ? hi1 : lo1; \
    } while (0)
#define VU_FIN(i) do { \
        const char* xi = xch + ((i) & 1) * 16384 + (wave ^ 4) * 2048 + lane * 16; \
        const f32x4 a = k0 + *(const f32x4*)xi, b = k1 + *(const f32x4*)(xi + 1024); \
        lds_st16(vimg + ((i) & 1) * 8192, ((et * 4 + 2 * kh + (lane >> 5)) * 32 + (lane & 31)) * 16, u32x4{pk_bf16(a[0], a[1]), pk_bf16(a[2], a[3]), pk_bf16(b[0], b[1]), pk_bf16(b[2], b[3])}); \
    } while (0)
#define VU_P2(i) do { \
        u32x4 vf[2][4]; \
        _Pragma("unroll") for (int tt = 0; tt < 2; ++tt) _Pragma("unroll") for (int ks = 0; ks < 4; ++ks) vf[tt][ks] = lds_ld16(vimg + ((i) & 1) * 8192, ((ks * 4 + g) * 32 + 16 * tt + (lane & 15)) * 16); \
        _Pragma("unroll") for (int bt = 0; bt < 5; ++bt) { \
            f32x4 d0 = {}, d1 = {}; \
            _Pragma("unroll") for (int ks = 0; ks < 4; ++ks) { d0 = mf16(vf[0][ks], wo[bt][ks], d0); d1 = mf16(vf[1][ks], wo[bt][ks], d1); } \
            st16_sc1(u32x4{pk_bf16(d0[0], d0[1]), pk_bf16(d0[2], d0[3]), pk_bf16(d1[0], d1[1]), pk_bf16(d1[2], d1[3])}, ru, lane * 16 + (8 * bt + wave) * 32768, ((par + 2 * (i)) * NCT * H + h) * 1024); \
        } \
    } while (0)
#define VU_LOOP() do { \
          \
        stage_x(rx, par + 2, XS2(1), wave, lane); \
        WAIT_VM(5); raw_barrier(); \
        VU_P1(0); raw_barrier(); \
        stage_x(rx, par + 4, XS2(0), wave, lane); \
        VU_FIN(0); raw_barrier(); \
        VU_P2(0); \
        WAIT_VM(10);   raw_barrier(); \
        VU_P1(1); \
        WAIT_VM(0); raw_barrier(); \
        if (tid == 0) flag_set(p, FL_GU + 2 * h + par, 1); \
          \
        for (int i = 2; i <= 33; ++i) { \
            if (i + 1 < 32) stage_x(rx, par + 2 * (i + 1), XS2(i + 1), wave, lane); \
            if (i <= 32) VU_FIN(i - 1); \
            if (KH) { if (i >= 3) VU_P2(i - 2); if (i < 32) VU_P1(i); } \
            else { if (i < 32) VU_P1(i); if (i >= 3) VU_P2(i - 2); } \
            if (i <= 4) WAIT_VM(0); else WAIT_VM(5);        \
            raw_barrier(); \
            if (tid == 0 && i >= 3 && i != 5) flag_set(p, FL_GU + 2 * h + par, i <= 4 ? i - 1 : i - 2); \
        } \
    } while (0)
    if (kh) { constexpr bool KH = true; VU_LOOP(); } else { constexpr bool KH = false; VU_LOOP(); }
    WAIT_VM(0);
    raw_barrier();
    if (tid == 0) flag_set(p, FL_GU + 2 * h + par, 32);
}

DEV unsigned now_rt() { return (unsigned)__builtin_amdgcn_s_memrealtime(); }
DEV unsigned clamp_rt(unsigned t) { const unsigned n = now_rt(); const int d = (int)(t - n); return (d > 400 || d < 0) ? n : t; }
DEV void wait_rt(unsigned t) {
    int d;
    while ((d = (int)(t - now_rt())) > 3) {}
    if (d > 0) asm volatile("s_nop 3");
    if (d > 1) asm volatile("s_nop 3");
    if (d > 2) asm volatile("s_nop 3");
    asm volatile("" ::: "memory");
}
#define PIN_S(x) do { x = __builtin_amdgcn_readfirstlane(x); asm volatile("" : "+s"(x)); } while (0)
#define SB() __builtin_amdgcn_sched_barrier(0)
DEV unsigned mx5(unsigned m, u32x4 v) { return max(max(max(m, v.x), v.y), max(v.z, v.w)); }
DEV unsigned read_ann(__amdgpu_buffer_rsrc_t r, unsigned soff, int cap) {
    unsigned v = 0;
    for (int sp = 0; sp <= cap; ++sp) {
        asm volatile("" ::: "memory");
        v = __builtin_amdgcn_readfirstlane(__builtin_amdgcn_raw_buffer_load_b32(r, 0, soff, 16));
        if (v != SENT) break;
    }
    return v;
}
DEV float wave_max(float x) {
    x = fmaxf(x, u2f(__builtin_amdgcn_update_dpp(0, f2u(x), 0x128, 0xf, 0xf, true)));
    x = fmaxf(x, u2f(__builtin_amdgcn_update_dpp(0, f2u(x), 0x124, 0xf, 0xf, true)));
    x = fmaxf(x, u2f(__builtin_amdgcn_update_dpp(0, f2u(x), 0x122, 0xf, 0xf, true)));
    x = fmaxf(x, u2f(__builtin_amdgcn_update_dpp(0, f2u(x), 0x121, 0xf, 0xf, true)));
    return xmax16_32(x);
}
DEV void dma_mem(__amdgpu_buffer_rsrc_t rc0, char* mimg, int lane, unsigned oa, unsigned ob) {
    const unsigned oa2 = oa + 4096;
    __builtin_amdgcn_raw_ptr_buffer_load_lds(rc0, (LAS void*)mimg, 16, lane * 16, oa, 0, 16);
    __builtin_amdgcn_raw_ptr_buffer_load_lds(rc0, (LAS void*)mimg, 16, lane * 16, oa, 1024, 16);
    __builtin_amdgcn_raw_ptr_buffer_load_lds(rc0, (LAS void*)mimg, 16, lane * 16, oa, 2048, 16);
    __builtin_amdgcn_raw_ptr_buffer_load_lds(rc0, (LAS void*)mimg, 16, lane * 16, oa, 3072, 16);
    __builtin_amdgcn_raw_ptr_buffer_load_lds(rc0, (LAS void*)(mimg + 4096), 16, lane * 16, oa2, 0, 16);
    __builtin_amdgcn_raw_ptr_buffer_load_lds(rc0, (LAS void*)(mimg + 4096), 16, lane * 16, oa2, 1024, 16);
    __builtin_amdgcn_raw_ptr_buffer_load_lds(rc0, (LAS void*)(mimg + 4096), 16, lane * 16, oa2, 2048, 16);
    __builtin_amdgcn_raw_ptr_buffer_load_lds(rc0, (LAS void*)(mimg + 4096), 16, lane * 16, oa2, 3072, 16);
    __builtin_amdgcn_raw_ptr_buffer_load_lds(rc0, (LAS void*)(mimg + 8192), 16, lane * 16, ob, 0, 16);
    __builtin_amdgcn_raw_ptr_buffer_load_lds(rc0, (LAS void*)(mimg + 8192), 16, lane * 16, ob, 1024, 16);
}
DEV void fill_mimg(char* mimg, int lane) {
    WAIT_VM(0);
#pragma unroll
    for (int i = 0; i < 10; ++i) lds_st16(mimg, i * 1024 + lane * 16, u32x4{SENT, SENT, SENT, SENT});
    __builtin_amdgcn_s_waitcnt(0xC07F);
}
DEV bf16x16 cat8(u32x4 a, u32x4 b) { u32x8 r = {a.x, a.y, a.z, a.w, b.x, b.y, b.z, b.w}; return __builtin_bit_cast(bf16x16, r); }
DEV f32x4 zero4() { return f32x4{0.f, 0.f, 0.f, 0.f}; }
#define RDF(i) af[i] = lds_ld16(mimg, (i) * 1024 + lane * 16)
DEV bool any_nan(float a, float b) { return __builtin_amdgcn_fcmpf(a, b, 8  ) != 0; }
#define PROJ_READ(WAITN, TICK) do { \
        WS(0); \
        WAITN(4); \
        for (int sp_ = 0;; ++sp_) { \
            asm volatile("" ::: "memory"); \
            const unsigned pr_ = lds_vol_ld<unsigned>(mimg + (lane < NCT ? lane : 0) * 256 + 128 + (lane & 31) * 4); \
            af[4] = lds_vol_ld<u32x4>(mimg + 4096 + lane * 16); af[5] = lds_vol_ld<u32x4>(mimg + 5120 + lane * 16); \
            if (__all(pr_ != SENT)) break; \
            asm volatile(""); \
            if (sp_ > cap) break; \
            TICK; \
        } \
        if (wave == 0 && ISQK) MT(tsp); \
        WS(1); \
    } while (0)
#define PROJ_MM(DO) do { RDF(0); RDF(1); SB(); DO(4); SB(); RDF(2); RDF(3); RDF(6); SB(); DO(5); SB(); RDF(7); RDF(8); RDF(9); SB(); \
        DO(0); DO(1); DO(2); DO(3); DO(6); DO(7); DO(8); DO(9); SB(); } while (0)
#define WAIT_VALID(TICK) do { \
        for (int sp_ = 0;; ++sp_) { \
            asm volatile("" ::: "memory"); \
            unsigned m_ = 0; \
            _Pragma("unroll") for (int st = 0; st < 10; ++st) m_ = mx5(m_, lds_ld16(mimg, st * 1024 + lane * 16)); \
            if (__all(m_ != SENT)) break; \
            if (sp_ > cap) { flag_set(p, FL_TMO, 1u); cap = 0; break; } \
            TICK; \
        } } while (0)
#define NOWAIT(n) (void)0
#define POLL_TICK do { if (tick == tnext) { tnext += 6; if (tick > 24) WAIT_VM(0); dma_mem(rc0, mimg, lane, oa, ob); } ++tick; } while (0)

constexpr float SCALE = 0.08838834764831845f;
constexpr float LOG2E = 1.4426950408889634f;
constexpr float MASKV = -999292928.0f;
DEV void round_scores(f32x4& d) {
    unsigned a = pk_bf16(d[0], d[1]), b = pk_bf16(d[2], d[3]);
    a = pk_bf16(bf_lo(a) * SCALE, bf_hi(a) * SCALE); b = pk_bf16(bf_lo(b) * SCALE, bf_hi(b) * SCALE);
    d = f32x4{bf_lo(a), bf_hi(a), bf_lo(b), bf_hi(b)};
}
DEV float max4(f32x4 d) { return fmaxf(fmaxf(d[0], d[1]), fmaxf(d[2], d[3])); }
DEV void exp4(f32x4& d, float mx) {
#pragma unroll
    for (int i = 0; i < 4; ++i) d[i] = __builtin_amdgcn_exp2f((d[i] - mx) * LOG2E);
}
DEV float sum4(f32x4 d) { return (d[0] + d[1]) + (d[2] + d[3]); }
DEV unsigned pk_pair(f32x4 d) { return pk_bf16(d[0] + d[2], d[1] + d[3]); }

DEV void role_qk(const Params& p, int h, char* lds) {
    constexpr bool ISQK = true; (void)ISQK;
    const int tid = threadIdx.x, lane = tid & 63, wave = __builtin_amdgcn_readfirstlane(tid >> 6);
    const int g = lane >> 4, l16 = lane & 15;
    unsigned* ctrl = (unsigned*)(p.ws + OFF_CTRL) + TUNE_W; (void)ctrl;
    bf16x16 wq[10], wk[10];
    const __amdgpu_buffer_rsrc_t rw0 = mkrsrc(p.wq, WBYTES), rw1 = mkrsrc(p.wk, WBYTES);
    for (int i = 0; i < TUNEW(8, 0); ++i) __builtin_amdgcn_s_sleep(2);
#define AFTER_W (void)0
    STAGE_RING(5, 0,
        do { stage_img<64>(rw0, bt * 64 * H * DH + h * DH, H * DH, img, wave, lane); stage_img<64>(rw1, bt * 64 * H * DH + h * DH, H * DH, img + 16384, wave, lane); } while (0),
        load_bsp2<true>(wq[bt], wk[bt], img, wave, img + 16384, wave, lane));
    TLR(h, 2);
    int cap = SPIN_CAP;
    const unsigned side0 = (flags_wait(p, FL_SIDE, 0, 1, 1u, lane, cap) == 2u ? 1u : 0u) << 13;
    flags_wait(p, FL_READY, 0, 1, NCT, lane, cap);
    const __amdgpu_buffer_rsrc_t rc0 = mkrsrc(p.ws + OFF_COMM0 + __builtin_amdgcn_readfirstlane(side0), 2 * MB - 8192);
    const __amdgpu_buffer_rsrc_t rq = mkrsrc(p.ws + OFF_QX, 32 * MB);
    const __amdgpu_buffer_rsrc_t rpx = mkrsrc(p.ws + OFF_PX, 5 * MB);
    char* mimg = lds;
    char* ximg = lds + 40960;
    char* misc = lds + 46080;
    unsigned* hist = (unsigned*)(misc + 32);
    char* qk = lds + 49152;
    const int idx = (lane & 2) ? 0xEEEE : 0x4444;
    unsigned long long tsp = 0; (void)tsp;
    WSDECL;
    {
        TLR(h, 3);
        flags_wait(p, FL_GQ + 2 * h, 0, 1, 1u, lane, cap);
        TLR(h, 4);
        u32x4 a = ld16_sc1(rq, tid * 16, h * 8192), b = ld16_sc1(rq, tid * 16, 16 * MB + h * 8192);
        lds_st16(qk, tid * 16, a); lds_st16(qk, 8192 + tid * 16, b);
        if (tid == 0) { lds_vol_st<int>(misc, 90); lds_vol_st<int>(misc + 4, 60); lds_vol_st<int>(misc + 8, 0); lds_vol_st<int>(misc + 12, 0); }
    }
    raw_barrier();
    u32x4 af[10];
    f32x4 aq, ak;
    char* xw = ximg + (16 * wave + l16) * 32 + g * 4;
#define Q_DO(st) aq = smf(af[st], wq[st], aq, idx)
#define K_DO(st) ak = smf(af[st], wk[st], ak, idx)
#define QK_DO(st) do { Q_DO(st); K_DO(st); } while (0)
#define QK_ALL() do { QK_DO(4); QK_DO(5); QK_DO(0); QK_DO(1); QK_DO(2); QK_DO(3); QK_DO(6); QK_DO(7); QK_DO(8); QK_DO(9); } while (0)
#define QK_PROJ(WAITN, TICK, HOOK, HOOK2) do { \
        float q0_, q1_; \
        for (int pass_ = 0;; ++pass_) { \
            aq = zero4(); ak = zero4(); asm volatile("" : "+v"(aq), "+v"(ak)); \
            PROJ_READ(WAITN, TICK); \
            HOOK; \
            PROJ_MM(QK_DO); \
            HOOK2; \
            q0_ = aq[0] + aq[2]; q1_ = aq[1] + aq[3]; \
            if (!any_nan(q0_, q1_)) break; \
            asm volatile(""); \
            if (pass_) break; \
            WAIT_VALID(TICK); \
        } \
        *(unsigned*)xw = pk_bf16(q0_, q1_); *(unsigned*)(xw + 16) = pk_pair(ak); \
        WS(4); } while (0)
#define PF_DECL const unsigned o = (tid - 64) * 16, so = (min(s + 1, NSEG - 1) * H + h) * 8192; u32x4 a0, a1, b0, b1
#define QK_PF do { a0 = ld16_sc1(rq, o, so); a1 = ld16_sc1(rq, o + 4096, so); b0 = ld16_sc1(rq, o, 16 * MB + so); b1 = ld16_sc1(rq, o + 4096, 16 * MB + so); SB(); } while (0)
#define PF_STORE do { char* nq = qk + ((s + 1) & 1) * 16384; lds_st16(nq, o, a0); lds_st16(nq, o + 4096, a1); lds_st16(nq, 8192 + o, b0); lds_st16(nq, 8192 + 4096 + o, b1); } while (0)
#define PF_CHECK do { if (s + 2 < NSEG && ((s & 1) ? gk1 : gk0) < (unsigned)((s + 2) >> 1) + 1u) { \
                const unsigned v = flags_wait(p, FL_GQ + 2 * h + (s & 1), 0, 1, ((s + 2) >> 1) + 1, lane, cap); if (s & 1) gk1 = v; else gk0 = v; } } while (0)
    if (wave == 0) {
        const unsigned gq = l16 >> 2, r = l16 & 3, key0 = 16 * (r >> 1) + 8 * (gq >> 1) + 2 * (gq & 1) + (r & 1);
        const unsigned koff = 8192 + (g * 32 + key0) * 16;
        const bool hi8 = l16 >= 8;
        const unsigned px_off = ((g >> 1) * 16 + rowi(l16 & 7, g & 1)) * 16 + (l16 >> 3) * 8;
        const unsigned pm_off = rowi(l16 & 7, g & 1) * 16 + (h & 1) * 8 + (g >> 1) * 4 + (l16 >> 3) * 4096;
        const unsigned foff = (8 * g + (l16 >> 2)) * 32 + (lane & 1) * 8;
        unsigned long long t1 = 0, t2 = 0, t3 = 0, t4 = 0; unsigned a12 = 0, a23 = 0, a34 = 0, a01 = 0; (void)t1; (void)t2; (void)t3; (void)t4; (void)a12; (void)a23; (void)a34; (void)a01;
        for (int s = 0; s < NSEG; ++s) {
            const char* kimg = qk + (s & 1) * 16384 + koff;
            unsigned opx = physo(px_base(s & 3, 0) + h * 512), opx1 = opx + 32768, opm = physo(pm_base(s & 3, 0) + (h >> 1) * 256);
            PIN_S(opx); PIN_S(opx1); PIN_S(opm);
            raw_barrier();
            MT(t1);
            u32x4 Fq[4], Fk[4], kf[8];
#define RD_KF do { _Pragma("unroll") for (int ks = 0; ks < 4; ++ks) { kf[2 * ks] = lds_ld16(kimg, ks * 2048); kf[2 * ks + 1] = lds_ld16(kimg, ks * 2048 + 64); } SB(); } while (0)
            QK_PROJ(NOWAIT, (void)0, (void)0, RD_KF);
            MT(t2);
            raw_barrier();
            MT(t3); WS(5);
            if (s + 1 < NSEG) {
#pragma unroll
                for (int ks = 0; ks < 4; ++ks) { u32x2 a = tr_read(ximg + foff, ks * 1024), b = tr_read(ximg + foff, ks * 1024 + 128); Fq[ks] = u32x4{a.x, a.y, b.x, b.y}; }
#pragma unroll
                for (int ks = 0; ks < 4; ++ks) { u32x2 c = tr_read(ximg + foff, ks * 1024 + 16), d = tr_read(ximg + foff, ks * 1024 + 128 + 16); Fk[ks] = u32x4{c.x, c.y, d.x, d.y}; }
                SB();
                f32x4 d0 = mf16(kf[0], Fq[0], zero4()), d1 = mf16(kf[1], Fq[0], zero4());
                d0 = mf16(kf[2], Fq[1], d0); d1 = mf16(kf[3], Fq[1], d1);
                d0 = mf16(kf[4], Fq[2], d0); d1 = mf16(kf[5], Fq[2], d1); d0 = mf16(kf[6], Fq[3], d0); d1 = mf16(kf[7], Fq[3], d1);
                SB();
                f32x4 dm = mf16(Fk[0], Fq[0], zero4());
                dm = mf16(Fk[1], Fq[1], dm); dm = mf16(Fk[2], Fq[2], dm); dm = mf16(Fk[3], Fq[3], dm);
                SB();
                float x0 = hi8 ? d1[0] : d0[0], x1 = hi8 ? d1[1] : d0[1], x2 = hi8 ? d1[2] : d0[2], x3 = hi8 ? d1[3] : d0[3];
                float mx = max3f(max3f(x0, x1, x2), x3, x3);
                mx = max3f(mx, mx, u2f(__builtin_amdgcn_update_dpp(0, f2u(mx), 0x128, 0xf, 0xf, true)));
                SB();
                float y0 = g >= 2 ? dm[2] : dm[0], y1 = g >= 2 ? dm[3] : dm[1];
                mx = xmax16_32(max3f(mx, y0, y1));
                constexpr float CS = SCALE * LOG2E;
                const float nm = -mx * CS;
                x0 = __builtin_amdgcn_exp2f(__builtin_fmaf(x0, CS, nm)); x1 = __builtin_amdgcn_exp2f(__builtin_fmaf(x1, CS, nm));
                x2 = __builtin_amdgcn_exp2f(__builtin_fmaf(x2, CS, nm)); x3 = __builtin_amdgcn_exp2f(__builtin_fmaf(x3, CS, nm));
                y0 = __builtin_amdgcn_exp2f(__builtin_fmaf(y0, CS, nm)); y1 = __builtin_amdgcn_exp2f(__builtin_fmaf(y1, CS, nm));
                float sm = (x0 + x1) + (x2 + x3);
                sm = sm + u2f(__builtin_amdgcn_update_dpp(0, f2u(sm), 0x128, 0xf, 0xf, true)) + (y0 + y1);
                const float inv = __builtin_amdgcn_rcpf(xsum16_32(sm));
                const u32x2 pxv = {nosent(pk_bf16(x0 * inv, x1 * inv)), nosent(pk_bf16(x2 * inv, x3 * inv))};
                st8_sc1(pxv, rc0, px_off, opx);
                st4_sc1(nosent(pk_bf16(y0 * inv, y1 * inv)), rc0, pm_off, opm);
                st8_sc1(pxv, rc0, px_off, opx1);
                MT(t4); WS(7); WSDUMP();
                { const unsigned ta = now_rt(); if (lane == 0) st4_sc1(nosent(ta), rc0, s * 4, physo(STA_BASE + h * 256)); }
                MTW(); PACC(a12, t1, t2); PACC(a23, t2, t3); PACC(a34, t3, t4); PACC(a01, t1, tsp); PDUMP(h, a12, a23, a34, a01);
            }
        }
    } else if (wave <= 2) {
        const int tt = wave - 1;
        const int tok = 16 * tt + l16;
        unsigned gk0 = 0u, gk1 = flags_wait(p, FL_GQ + 2 * h + 1, 0, 1, 1u, lane, cap);
        for (int s = 0; s < NSEG; ++s) {
            PF_DECL;
            raw_barrier();
            QK_PROJ(NOWAIT, (void)0, QK_PF, (void)0);
            raw_barrier();
            WS(7); WSDUMP();
            __builtin_amdgcn_s_sleep(2);
            WAIT_VM(4);
            if (lane == 0 && s > 0 && s <= NSEG - 4) flag_set(p, FL_PX + 2 * h + tt, s);
            const char* kimg = qk + (s & 1) * 16384 + 8192 + (g * 32 + l16) * 16;
            const char* qimg = qk + (s & 1) * 16384 + (g * 32 + tok) * 16;
            f32x4 d0 = {}, d1 = {}, dm = {};
#pragma unroll
            for (int ks = 0; ks < 4; ++ks) {
                const unsigned e0 = 32 * ks + 8 * g + (l16 >> 2);
                u32x2 a = tr_read(ximg, e0 * 32 + (lane & 3) * 8), b = tr_read(ximg, (e0 + 4) * 32 + (lane & 3) * 8);
                const u32x4 qf = lds_ld16(qimg, ks * 2048);
                d0 = mf16(lds_ld16(kimg, ks * 2048), qf, d0);
                d1 = mf16(lds_ld16(kimg, ks * 2048 + 256), qf, d1);
                dm = mf16(u32x4{a.x, a.y, b.x, b.y}, qf, dm);
            }
            round_scores(d0); round_scores(d1); round_scores(dm);
#pragma unroll
            for (int r = 0; r < 4; ++r) { if (4 * g + r > tok) d0[r] = MASKV; if (16 + 4 * g + r > tok) d1[r] = MASKV; }
            if (g < 2) dm = f32x4{-3.0e38f, -3.0e38f, -3.0e38f, -3.0e38f};
            const float mx = xmax16_32(fmaxf(fmaxf(max4(d0), max4(d1)), max4(dm)));
            exp4(d0, mx); exp4(d1, mx); exp4(dm, mx);
            const float inv = 1.0f / xsum16_32(sum4(d0) + sum4(d1) + sum4(dm));
            u32x4 v = {pk_bf16(d0[0] * inv, d0[1] * inv), pk_bf16(d0[2] * inv, d0[3] * inv), pk_bf16(d1[0] * inv, d1[1] * inv), pk_bf16(d1[2] * inv, d1[3] * inv)};
            st16_sc1(v, rpx, lane * 16, ((s * H + h) * 2 + tt) * 1024);
            if (g >= 2) {
                st4_sc1(pk_bf16(dm[0] * inv, dm[1] * inv), rpx, tok * 16 + (g - 2) * 4, 4 * MB + (s * H + h) * 512);
                st4_sc1(pk_bf16(dm[2] * inv, dm[3] * inv), rpx, tok * 16 + 8 + (g - 2) * 4, 4 * MB + (s * H + h) * 512);
            }
            PF_STORE;
            if (s >= NSEG - 4) { WAIT_VM(0); if (lane == 0) flag_set(p, FL_PX + 2 * h + tt, s + 1); }
            PF_CHECK;
        }
    } else if (wave <= 4) {
        unsigned gk0 = 0u, gk1 = flags_wait(p, FL_GQ + 2 * h + 1, 0, 1, 1u, lane, cap);
        const float mg1 = 0.25f * (float)TUNEW(0, 48), mg2 = 0.25f * (float)TUNEW(1, 16);
        float d1e = 90.0f, d2e = 60.0f;
        for (int s = 0; s < NSEG; ++s) {
            PF_DECL;
            raw_barrier();
            QK_PROJ(NOWAIT, (void)0, QK_PF, (void)0);
            raw_barrier();
            WS(7); WSDUMP();
            __builtin_amdgcn_s_sleep(6);
            WAIT_VM(0);
            PF_STORE;
            if (h == 0 && wave == 4 && s >= 2) {
                const unsigned tb = hist[2 * (s - 1)], tx = hist[2 * (s - 1) + 1];
                const unsigned va = __builtin_amdgcn_raw_buffer_load_b32(rc0, (lane & 31) * 256, physo(STA_BASE) + (s - 1) * 4, 16);
                const unsigned vb = __builtin_amdgcn_raw_buffer_load_b32(rc0, physo((lane < NCT ? lane : 0) * 256), physo(STB_BASE) + (s - 1) * 4, 16);
                if (__all(va != SENT && vb != SENT)) {
                    const float a = wave_max((float)(int)(va - tb)), b = wave_max((float)(int)(vb - tx));
                    if (tb != SENT) d1e += 0.5f * (fminf(a, d1e + 2.0f) - d1e);
                    d2e += 0.5f * (fminf(b, d2e + 2.0f) - d2e);
                    if (lane == 0) { lds_vol_st<int>(misc, (int)(d1e + mg1)); lds_vol_st<int>(misc + 4, (int)(d2e + mg2)); }
                }
            }
            if (wave == 3 && s >= 1) {
                st16_sc1(u32x4{SENT, SENT, SENT, SENT}, rc0, (lane & 31) * 16 + (lane >> 5) * 32768, physo(px_base((s - 1) & 3, 0) + h * 512));
                if (lane < 32) st8_sc1(u32x2{SENT, SENT}, rc0, l16 * 16 + (h & 1) * 8 + (g & 1) * 4096, physo(pm_base((s - 1) & 3, 0) + (h >> 1) * 256));
            }
            PF_CHECK;
        }
    } else if (wave < 7) {
        const int tfirst = wave == 6 ? TUNEW(5, 0) : TUNEW(5, 0) + 2;
        for (int s = 0; s < NSEG; ++s) {
            unsigned oa = physo(mema_off(s & 3, 0)), ob = physo(memb_off(s & 3, 0));
            PIN_S(oa); PIN_S(ob);
            int tick = 0, tnext = tfirst;
            raw_barrier();
            QK_PROJ(NOWAIT, POLL_TICK, (void)0, (void)0);
            raw_barrier();
            WS(7); WSDUMP();
            WAIT_VM(0);
            if (lane == 0) lds_vol_st<int>(misc + 8 + (wave - 5) * 4, s + 1);
        }
    } else {
        int nfail = 0;
        const int tfirst = TUNEW(5, 0) + 4, gap2 = TUNEW(6, 3);
        for (int s = 0; s < NSEG; ++s) {
            const unsigned tm0 = s > 0 ? read_ann(rc0, physo(ANN_BASE) + (s - 1) * 128 + 4, cap) : 0u;
            while (lds_vol_ld<int>(misc + 8) < s || lds_vol_ld<int>(misc + 12) < s) {}
            fill_mimg(mimg, lane);
            unsigned oa = physo(mema_off(s & 3, 0)), ob = physo(memb_off(s & 3, 0)), oann = physo(ANN_BASE + s * 128);
            PIN_S(oa); PIN_S(ob); PIN_S(oann);
            const unsigned tm = s > 0 ? clamp_rt(tm0) : now_rt();
            const int dd1 = __builtin_amdgcn_readfirstlane(lds_vol_ld<int>(misc)), dd2 = __builtin_amdgcn_readfirstlane(lds_vol_ld<int>(misc + 4));
            int tick = 0, tnext = tfirst;
            wait_rt(tm);
            dma_mem(rc0, mimg, lane, oa, ob);
            const unsigned c0 = (unsigned)__builtin_amdgcn_s_memtime();
            for (int i = 0; i < gap2; ++i) asm volatile("s_nop 7");
            dma_mem(rc0, mimg, lane, oa, ob);
            WAIT_VM(12);
            raw_barrier();
#define QK_ANN do { if (h == 0) { \
                const unsigned te = tm + ((((unsigned)__builtin_amdgcn_s_memtime() - c0) * 2731u) >> 16), x1 = nosent(te + dd1), x2 = nosent(x1 + dd2); \
                if (lane == 0) { st8_sc1(u32x2{x1, x2}, rc0, 0, oann); hist[2 * s] = pass_ ? SENT : te; hist[2 * s + 1] = x1; } } } while (0)
            QK_PROJ(NOWAIT, POLL_TICK, QK_ANN, (void)0);
            raw_barrier();
            WS(7); WSDUMP();
            PDUMP(32 + h, c1 - c0, c2 - c1, (c0 - cpre) - (tm - rpre) * 24, tm - rpre);
            if (s >= 4) nfail += tick;
        }
    }
}

DEV void role_vo(const Params& p, int h, char* lds) {
    constexpr bool ISQK = false; (void)ISQK;
    const int tid = threadIdx.x, lane = tid & 63, wave = __builtin_amdgcn_readfirstlane(tid >> 6);
    const int g = lane >> 4, l16 = lane & 15, j4 = wave & 3;
    bf16x16 w[20];
    const __amdgpu_buffer_rsrc_t rw0 = mkrsrc(p.wv, WBYTES), rw1 = mkrsrc(p.wo, WBYTES);
    for (int i = 0; i < TUNEW(8, 0); ++i) __builtin_amdgcn_s_sleep(2);
    STAGE_RING(5, 0,
        do { stage_img<64>(rw0, bt * 64 * H * DH + h * DH, H * DH, img, wave, lane); stage_img<64>(rw1, (h * DH + (bt & 1) * 64) * D + (bt >> 1) * 128, D, img + 16384, wave, lane); } while (0),
        do { if (wave < 4) load_bsp2<true>(w[4 * (bt >> 1) + (bt & 1)], w[4 * (bt >> 1) + 2 + (bt & 1)], img, 2 * j4, img, 2 * j4 + 1, lane);
             else load_bsp2<false>(w[4 * (bt >> 1) + (bt & 1)], w[4 * (bt >> 1) + 2 + (bt & 1)], img + 16384, 2 * j4, img + 16384, 2 * j4 + 1, lane); } while (0));
#undef AFTER_W
    TLR(32 + h, 2);
    int cap = SPIN_CAP;
    const unsigned long long sides = wait_sides(p, lane, cap), tmask = sides >> 1;
    flags_wait(p, FL_READY, 0, 1, NCT, lane, cap);
    const __amdgpu_buffer_rsrc_t rc0 = mkrsrc(p.ws + OFF_COMM0 + (((unsigned)sides & 1u) << 13), 2 * MB - 8192);
    const __amdgpu_buffer_rsrc_t rt = mkrsrc(p.ws + OFF_T, 44 * MB);
    char* mimg = lds;
    char* vimg = lds + 40960;
    const int idx = (lane & 2) ? 0xEEEE : 0x4444;
    unsigned long long tsp = 0; (void)tsp;
    WSDECL;
    if (wave < 4) {
        u32x4 af[10];
        f32x4 a0, a1;
        const unsigned e_ = 32 * j4 + l16;
        char* vw = vimg + (2 * (e_ >> 2) + (e_ & 1)) * 32 + (4 * g + 2 * ((e_ >> 1) & 1)) * 2;
#define VW(st, tl) w[4 * ((st) >> 1) + 2 * (tl) + ((st) & 1)]
#define VO_DO(st) do { a0 = smf(af[st], VW(st, 0), a0, idx); a1 = smf(af[st], VW(st, 1), a1, idx); } while (0)
        for (int s = 0; s < NSEG; ++s) {
            raw_barrier();
            float v0_, v1_;
            for (int pass_ = 0;; ++pass_) {
                a0 = zero4(); a1 = zero4(); asm volatile("" : "+v"(a0), "+v"(a1));
                PROJ_READ(NOWAIT, (void)0);
                PROJ_MM(VO_DO);
                v0_ = a0[0] + a0[2]; v1_ = a0[1] + a0[3];
                if (!any_nan(v0_, v1_)) break;
                asm volatile("");
                if (pass_) break;
                WAIT_VALID((void)0);
            }
            *(unsigned*)vw = pk_bf16(v0_, v1_); *(unsigned*)(vw + 256) = pk_pair(a1);
            WS(4);
            raw_barrier();
            WS(5); WSDUMP();
        }
    } else {
        const int gap2 = TUNEW(6, 3);
        const unsigned vr_off = (8 * g + (l16 >> 2)) * 32 + (lane & 3) * 8;
        const unsigned t_voff = (8 * (g >> 1) + 2 * j4 + (g & 1)) * 16384 + l16 * 16, t_voff2 = g < 2 ? (32 + 2 * j4 + g) * 16384 + l16 * 16 : 0x7FFFFFF0u;
        for (int s = 0; s < NSEG; ++s) {
            if (wave == 7) fill_mimg(mimg, lane);
            unsigned ot = t_off(s, tmask) + h * 256, ot2 = ot + 16 * 16384;
            unsigned oa = physo(mema_off(s & 3, 0)), ob = physo(memb_off(s & 3, 0));
            PIN_S(oa); PIN_S(ob); PIN_S(ot); PIN_S(ot2);
            if (wave == 7) {
                if (s > 0) wait_rt(clamp_rt(read_ann(rc0, physo(ANN_BASE + (s - 1) * 128 + 4), cap)));
                dma_mem(rc0, mimg, lane, oa, ob);
                for (int i = 0; i < gap2; ++i) asm volatile("s_nop 7");
                asm volatile("s_nop 7");
                dma_mem(rc0, mimg, lane, oa, ob);
                WAIT_VM(12);
            }
            raw_barrier();
            WS(0);
            {
                int tick = 0, tnext = 3 * (j4 ^ 3);
                for (int sp = 0; sp <= cap; ++sp) {
                    const unsigned pr = lds_vol_ld<unsigned>(mimg + (lane < NCT ? lane : 0) * 256 + 128 + (lane & 31) * 4);
                    if (__all(pr != SENT)) break;
                    if (tick == tnext) { tnext += 12; WAIT_VM(0); dma_mem(rc0, mimg, lane, oa, ob); }
                    ++tick;
                }
                if (wave != 7) WAIT_VM(0);
            }
            if (wave == 7) {
                for (int sp = 0;; ++sp) {
                    WAIT_VM(0);
                    unsigned m = 0;
#pragma unroll
                    for (int st = 0; st < 10; ++st) m = mx5(m, lds_ld16(mimg, st * 1024 + lane * 16));
                    if (__all(m != SENT)) break;
                    if (sp > cap) { flag_set(p, FL_TMO, 1u); cap = 0; break; }
                    dma_mem(rc0, mimg, lane, oa, ob);
                }
            }
            f32x4 d[10];
#pragma unroll
            for (int i = 0; i < 10; ++i) { d[i] = zero4(); asm volatile("" : "+v"(d[i])); }
            raw_barrier();
            WS(5);
            u32x4 A[2];
#pragma unroll
            for (int ks = 0; ks < 2; ++ks) { u32x2 a = tr_read(vimg + vr_off, ks * 1024), b = tr_read(vimg + vr_off, ks * 1024 + 128); A[ks] = u32x4{a.x, a.y, b.x, b.y}; }
            unsigned tw[10];
#define T_MM(i) do { d[i] = smf(A[0], w[2 * (i)], d[i], idx); d[i] = smf(A[1], w[2 * (i) + 1], d[i], idx); } while (0)
#define T_PK(i) tw[i] = nosent(pk_bf16(d[i][0] + d[i][2], d[i][1] + d[i][3]))
#define T_ST4(i, so) do { \
                auto a = __builtin_amdgcn_permlane16_swap(tw[i], tw[(i) + 1], false, false); \
                auto b = __builtin_amdgcn_permlane16_swap(tw[(i) + 2], tw[(i) + 3], false, false); \
                auto c = __builtin_amdgcn_permlane32_swap(a[0], b[0], false, false); \
                auto e = __builtin_amdgcn_permlane32_swap(a[1], b[1], false, false); \
                st16_sc1(u32x4{c[0], c[1], e[0], e[1]}, rt, t_voff, so); } while (0)
            T_MM(0); T_MM(1); SB();
            T_MM(2); T_PK(0); SB(); T_MM(3); T_PK(1); SB(); T_MM(4); T_PK(2); SB(); T_MM(5); T_PK(3); SB();
            T_MM(6); T_ST4(0, ot); SB(); T_MM(7); T_PK(4); T_PK(5); SB(); T_MM(8); T_PK(6); SB(); T_MM(9); T_PK(7); SB();
            T_ST4(4, ot2); T_PK(8); T_PK(9);
            {
                auto a = __builtin_amdgcn_permlane16_swap(tw[8], tw[9], false, false);
                auto c = __builtin_amdgcn_permlane32_swap(a[0], a[0], false, false);
                auto e = __builtin_amdgcn_permlane32_swap(a[1], a[1], false, false);
                st16_sc1(u32x4{c[0], c[1], e[0], e[1]}, rt, t_voff2, ot);
            }
            WS(7); WSDUMP();
        }
    }
}

DEV void role_comb(const Params& p, int c, char* lds) {
    const int tid = threadIdx.x, lane = tid & 63, wave = __builtin_amdgcn_readfirstlane(tid >> 6);
    const int g = lane >> 4, l16 = lane & 15;
    unsigned* ctrl = (unsigned*)(p.ws + OFF_CTRL) + TUNE_W; (void)ctrl;
    const __amdgpu_buffer_rsrc_t rt = mkrsrc(p.ws + OFF_T, 44 * MB), ru = mkrsrc(p.ws + OFF_U, 80 * MB);
    const int idx = (lane & 2) ? 0xEEEE : 0x4444;
    int cap = SPIN_CAP;
    const u32x4 sv = {SENT, SENT, SENT, SENT};
    {
        const __amdgpu_buffer_rsrc_t r = mkrsrc(p.ws + OFF_COMM0, COMM_CHUNKS * 16384);
        for (unsigned i = (c * 512 + tid) * 16; i < COMM_CHUNKS * 16384; i += NCT * 8192) st16_sc1(sv, r, i, 0);
        WAIT_VM(0);
        raw_barrier();
        if (tid == 0) flag_add(p, FL_READY);
    }
    const unsigned long long sides = wait_sides(p, lane, cap), tmask = sides >> 1;
    const __amdgpu_buffer_rsrc_t rc0 = mkrsrc(p.ws + OFF_COMM0 + (((unsigned)sides & 1u) << 13), 2 * MB - 8192);
    st16_sc1(sv, rt, tid * 16, t_off(0, tmask) + c * 16384);
    st16_sc1(sv, rt, tid * 16, t_off(1, tmask) + c * 16384);
    flags_wait(p, FL_READY, 0, 1, NCT, lane, cap);
    WAIT_VM(0);
    raw_barrier();
    TLR(64 + c, 2);
    const unsigned rstr = c < 32 ? 16384u : 2048u;
    if (tid < 64) {
        const unsigned m = 2 * (l16 >> 2) + (l16 & 1), t = (l16 >> 1) & 1;
        const u32x4 src = *(const u32x4*)(p.mem0 + m * D + 16 * c + 8 * t);
        st16_sc1(u32x4{nosent(src.x), nosent(src.y), nosent(src.z), nosent(src.w)}, rc0, l16 * 16 + g * rstr, physo(mem_off(0, 0, c)));
    }
    flags_wait(p, FL_GU, 2, H, 1u, lane, cap);
#pragma unroll
    for (int j = 0; j < 4; ++j) lds_st16(lds, tid * 16 + j * 8192, ld16_sc1(ru, tid * 16 + j * 8192, c * 32768));
    raw_barrier();
    TLR(64 + c, 3);
    if (wave == 0) {
        int nfail = 0;
        constexpr bool second = true;
        int nft = 0, nfm = 0, nfx = 0; (void)nft; (void)nfm; (void)nfx;
        const unsigned st_voff = (l16 & 6) == 0 ? rowi(2 * g + (l16 & 1), l16 >> 3) * 16 : 0x7FFFFFF0u;
        unsigned long long c0 = 0, c1 = 0, c2 = 0; unsigned k01 = 0, k12 = 0; (void)c0; (void)c1; (void)c2; (void)k01; (void)k12;
        for (int s = 0; s + 1 < NSEG; ++s) {
            const char* ub = lds + (s & 1) * 32768;
            u32x4 uf[24];
#pragma unroll
            for (int j = 0; j < 24; ++j) uf[j] = lds_ld16(ub, j * 1024 + lane * 16);
            unsigned ot = t_off(s, tmask) + c * 16384;
            unsigned ox0 = physo(px_base(s & 3, 0)), ox1 = ox0 + 16384, om = physo(pm_base(s & 3, 0));
            unsigned ost = physo(mem_off((s + 1) & 3, 0, c));
            const unsigned opx_x = ox0 ^ (ox0 + 32768), opx_y = ox1 ^ (ox1 + 32768);
            PIN_S(ot); PIN_S(ox0); PIN_S(ox1); PIN_S(om); PIN_S(ost);
            unsigned tp = clamp_rt(read_ann(rc0, physo(ANN_BASE + s * 128), cap));
            int sp = 0;
            float sa, sb;
            bool chk = false;
            unsigned oy0 = ox0 + 32768, oy1 = ox1 + 32768, on = om + 4096;
            PIN_S(oy0); PIN_S(oy1); PIN_S(on);
#define C_LDX(j, o0, o1) px[j] = ld16_sc1(rc0, lane * 16 + ((j) & 7) * 1024, (j) < 8 ? (o0) : (o1))
#define C_PASS2() do { \
                f32x4 ax = zero4(); \
                _Pragma("unroll") for (int hb = 0; hb < 2; ++hb) { \
                    u32x4 uu[16]; \
                    _Pragma("unroll") for (int j = 0; j < 16; ++j) uu[j] = lds_ld16(ub, (16 * hb + j) * 1024 + lane * 16); \
                    SB(); \
                    _Pragma("unroll") for (int j = 0; j < 8; ++j) ax = smf(px[8 * hb + j], cat8(uu[2 * j], uu[2 * j + 1]), ax, idx); \
                } \
                _Pragma("unroll") for (int j = 0; j < 4; ++j) ax = smf(pm[j], cat8(tf[2 * j], tf[2 * j + 1]), ax, idx); \
                sa = ax[0] + ax[2]; sb = ax[1] + ax[3]; \
            } while (0)
#define C_PUBLISH() do { \
                MT(c1); \
                const float give = (lane & 1) ? sa : sb; \
                const float got = u2f(__builtin_amdgcn_update_dpp(0, f2u(give), 0xB1, 0xf, 0xf, true)); \
                const unsigned pk = nosent(pk_bf16((lane & 1) ? got : sa, (lane & 1) ? sb : got)); \
                u32x4 v; \
                v.x = pk; \
                v.y = __builtin_amdgcn_update_dpp(0, pk, 0x102, 0xf, 0xf, true); \
                v.z = __builtin_amdgcn_update_dpp(0, pk, 0x104, 0xf, 0xf, true); \
                v.w = __builtin_amdgcn_update_dpp(0, pk, 0x106, 0xf, 0xf, true); \
                st16_sc1(v, rc0, st_voff, ost); \
                tb = now_rt(); \
            } while (0)
            bool done = false, fast = false;
            unsigned tb = 0;
            {
                u32x4 tf[8], pm[4], px[16];
                f32x4 ax = zero4();
                asm volatile("" : "+v"(ax));
                wait_rt(tp);
                MT(c0);
#pragma unroll
                for (int j = 0; j < 16; ++j) { C_LDX(j, ox0, ox1); SB(); }
#pragma unroll
                for (int j = 0; j < 4; ++j) { pm[j] = ld16_sc1(rc0, lane * 16 + j * 1024, om); SB(); tf[2 * j] = ld16_sc1(rt, lane * 16 + 2 * j * 1024, ot); SB(); tf[2 * j + 1] = ld16_sc1(rt, lane * 16 + (2 * j + 1) * 1024, ot); SB(); }
                u32x4 ul[8];
#pragma unroll
                for (int j = 0; j < 16; ++j) {
                    const u32x4 u0 = j < 12 ? uf[j < 12 ? 2 * j : 0] : ul[j < 12 ? 0 : 2 * j - 24], u1 = j < 12 ? uf[j < 12 ? 2 * j + 1 : 0] : ul[j < 12 ? 0 : 2 * j - 23];
                    ax = smf(px[j], cat8(u0, u1), ax, idx);
                    SB();
                    if (j < 8) ul[j] = lds_ld16(ub, (24 + j) * 1024 + lane * 16);
                    if (second) C_LDX(j, oy0, oy1);
                    SB();
                }
#pragma unroll
                for (int j = 0; j < 4; ++j) { ax = smf(pm[j], cat8(tf[2 * j], tf[2 * j + 1]), ax, idx); SB(); }
                sa = ax[0] + ax[2]; sb = ax[1] + ax[3];
                done = fast = !any_nan(sa, sb);
                if (done) {
                    C_PUBLISH();
                    asm volatile("" :: "v"(px[0]), "v"(px[1]), "v"(px[2]), "v"(px[3]), "v"(px[4]), "v"(px[5]), "v"(px[6]), "v"(px[7]), "v"(px[8]), "v"(px[9]), "v"(px[10]), "v"(px[11]), "v"(px[12]),
                                 "v"(px[13]), "v"(px[14]), "v"(px[15]) : "memory");
                } else {
                    ++sp;
#pragma unroll
                    for (int j = 0; j < 4; ++j) { pm[j] = ld16_sc1(rc0, lane * 16 + j * 1024, on); tf[2 * j] = ld16_sc1(rt, lane * 16 + 2 * j * 1024, ot); tf[2 * j + 1] = ld16_sc1(rt, lane * 16 + (2 * j + 1) * 1024, ot); }
                    SB();
                    C_PASS2();
                    done = !any_nan(sa, sb);
                }
            }
            while (!done) {
                ++sp;
                u32x4 tf[8], pm[4], px[16];
#pragma unroll
                for (int j = 0; j < 16; ++j) { C_LDX(j, ox0, ox1); SB(); }
#pragma unroll
                for (int j = 0; j < 4; ++j) { pm[j] = ld16_sc1(rc0, lane * 16 + j * 1024, om); SB(); tf[2 * j] = ld16_sc1(rt, lane * 16 + 2 * j * 1024, ot); SB(); tf[2 * j + 1] = ld16_sc1(rt, lane * 16 + (2 * j + 1) * 1024, ot); SB(); }
                C_PASS2();
                if (!any_nan(sa, sb) || chk) break;
                ox0 ^= opx_x; ox1 ^= opx_y; om ^= 4096;
                if (sp >= 3) {
                    for (int k = 0;; ++k) {
                        asm volatile("" ::: "memory");
                        unsigned m = 0;
#pragma unroll
                        for (int j = 0; j < 8; ++j) m = mx5(mx5(mx5(m, ld16_sc1(rc0, lane * 16 + j * 1024, ox0)), ld16_sc1(rc0, lane * 16 + j * 1024, ox1)), ld16_sc1(rt, lane * 16 + j * 1024, ot));
#pragma unroll
                        for (int j = 0; j < 4; ++j) m = mx5(m, ld16_sc1(rc0, lane * 16 + j * 1024, om));
                        if (__all(m != SENT)) break;
                        if (k > cap) { flag_set(p, FL_TMO, 1u); cap = 0; break; }
                    }
                    chk = true;
                }
            }
            if (!fast) C_PUBLISH();
            MT(c2); MTW(); PACC(k01, c0, c1); PACC(k12, c1, c2); PDUMP(64 + c, k01, k12, (unsigned)sp, 0u);
            if (lane == 0) st4_sc1(nosent(tb), rc0, s * 4, physo(STB_BASE + c * 256));
            if (lane == 0) flag_set(p, FL_CB + c, s + 1);
            if (s >= 4) nfail += sp;
            raw_barrier();
        }
    } else {
        const int udly = TUNEW(3, 0);
        unsigned gk0 = 0, gk1 = 0;
        for (int s = 0; s + 1 < NSEG; ++s) {
            if (wave >= 4 && s + 2 < NSEG) {
                char* nb = lds + ((s + 1) & 1) * 32768;
                for (int i = 0; i < udly; ++i) __builtin_amdgcn_s_sleep(2);
                const unsigned want = ((s + 1) >> 1) + 1;
                if ((((s + 1) & 1) ? gk1 : gk0) < want) {
                    unsigned v = flags_wait(p, FL_GU + ((s + 1) & 1), 2, H, want, lane, cap);
#pragma unroll
                    for (int o = 32; o >= 1; o >>= 1) v = min(v, (unsigned)__shfl_xor((int)v, o));
                    v = __builtin_amdgcn_readfirstlane(v);
                    if ((s + 1) & 1) gk1 = v; else gk0 = v;
                }
                u32x4 t[8];
#pragma unroll
                for (int j = 0; j < 8; ++j) t[j] = ld16_sc1(ru, (tid - 256) * 16 + j * 4096, ((s + 1) * NCT + c) * 32768);
#pragma unroll
                for (int j = 0; j < 8; ++j) lds_st16(nb, (tid - 256) * 16 + j * 4096, t[j]);
            } else if (wave <= 2 && s + 2 < NSEG) {
                for (int i = 0; i < udly; ++i) __builtin_amdgcn_s_sleep(2);
#pragma unroll
                for (int j = 0; j < 4; ++j) st16_sc1(sv, rt, (tid - 64) * 16 + j * 2048, t_off(s + 2, tmask) + c * 16384);
                WAIT_VM(0);
            } else if (wave == 3 && s >= 1) {
                for (int i = 0; i < udly; ++i) __builtin_amdgcn_s_sleep(2);
                st16_sc1(sv, rc0, l16 * 16 + g * rstr, physo(mem_off((s - 1) & 3, 0, c)));
                WAIT_VM(0);
            }
            raw_barrier();
        }
    }
}

constexpr int NCHAIN = 104, NWG = 256;
DEV int bcast0(int v, char* lds) {
    if (threadIdx.x == 0) *(int*)lds = v;
    __syncthreads();
    const int r = __builtin_amdgcn_readfirstlane(*(int*)lds);
    __syncthreads();
    return r;
}
DEV int near_role(int t) { return t < 40 ? 64 + t : t < 64 ? 104 + 2 * (t - 40) + 1 : t - 64; }
DEV int far_role(int u) { return u < 24 ? 232 + u : u < 56 ? 168 + 2 * (u - 24) : u < 88 ? 104 + 2 * (u - 56) : u < 120 ? 168 + 2 * (u - 88) + 1 : 104 + 2 * (24 + u - 120) + 1; }
DEV int get_role(const Params& p, char* lds) {
    int role = 0;
    if (threadIdx.x == 0) {
        const unsigned xcc = __builtin_amdgcn_s_getreg(20 | (3 << 11)) & 0xf;
        if (xcc < 4) { const int t = (int)flag_add(p, FL_TNEAR); role = t < 128 ? near_role(t) : far_role(255 - t); }
        else { const int u = (int)flag_add(p, FL_TFAR); role = u < 128 ? far_role(u) : near_role(255 - u); }
    }
    return bcast0(role, lds);
}

DEV void task_out(const Params& p, int s, int cg, char* lds, int& cap) {
    const int tid = threadIdx.x, lane = tid & 63, wave = __builtin_amdgcn_readfirstlane(tid >> 6);
    const int g = lane >> 4, l16 = lane & 15;
    const __amdgpu_buffer_rsrc_t rpx = mkrsrc(p.ws + OFF_PX, 5 * MB);
    const __amdgpu_buffer_rsrc_t rt = mkrsrc(p.ws + OFF_T, 44 * MB), ru = mkrsrc(p.ws + OFF_U, 80 * MB);
    const unsigned long long tmask = wait_sides(p, lane, cap) >> 1;
    const int c = cg * 8 + wave;
    flags_wait(p, FL_GU + (s & 1), 2, H, (s >> 1) + 1, lane, cap);
    u32x4 uf[32];
#pragma unroll
    for (int j = 0; j < 32; ++j) uf[j] = ld16_sc1(ru, lane * 16 + j * 1024, (s * NCT + c) * 32768);
    if (wave == 0) { flags_wait(p, FL_CB + cg * 8, 1, 8, s + 1, lane, cap); flags_wait(p, FL_PX, 1, 64, s + 1, lane, cap); }
    __syncthreads();
    u32x4 tf[8];
    {
        u32x4 v[10];
#pragma unroll
        for (int i = 0; i < 8; ++i) v[i] = ld16_sc1(rpx, tid * 16 + i * 8192, s * 65536);
#pragma unroll
        for (int i = 0; i < 2; ++i) v[8 + i] = ld16_sc1(rpx, tid * 16 + i * 8192, 4 * MB + s * 16384);
#pragma unroll
        for (int j = 0; j < 8; ++j) tf[j] = ld16_sc1(rt, lane * 16 + j * 1024, t_off(s, tmask) + c * 16384);
#pragma unroll
        for (int i = 0; i < 10; ++i) lds_st16(lds, tid * 16 + i * 8192, v[i]);
    }
    raw_barrier();
    f32x4 d0 = {}, d1 = {};
#pragma unroll
    for (int hh = 0; hh < 32; ++hh) {
        d0 = mf16(uf[hh], lds_ld16(lds, hh * 2048 + lane * 16), d0);
        d1 = mf16(uf[hh], lds_ld16(lds, hh * 2048 + 1024 + lane * 16), d1);
    }
#pragma unroll
    for (int j = 0; j < 8; ++j) {
        d0 = mf16(tf[j], lds_ld16(lds, 65536 + (4 * j + g) * 512 + l16 * 16), d0);
        d1 = mf16(tf[j], lds_ld16(lds, 65536 + (4 * j + g) * 512 + 256 + l16 * 16), d1);
    }
    unsigned short* o = p.out + (size_t)(s * 32 + l16) * (2 * D) + D + c * 16 + 4 * g;
    *(u32x2*)o = u32x2{pk_bf16(d0[0], d0[1]), pk_bf16(d0[2], d0[3])};
    *(u32x2*)(o + 16 * 2 * D) = u32x2{pk_bf16(d1[0], d1[1]), pk_bf16(d1[2], d1[3])};
    __syncthreads();
}
DEV void task_out_fine(const Params& p, int s, int c, char* lds, int& cap) {
    const int tid = threadIdx.x, lane = tid & 63, wave = __builtin_amdgcn_readfirstlane(tid >> 6);
    const int g = lane >> 4, l16 = lane & 15;
    const __amdgpu_buffer_rsrc_t rpx = mkrsrc(p.ws + OFF_PX, 5 * MB);
    const __amdgpu_buffer_rsrc_t rt = mkrsrc(p.ws + OFF_T, 44 * MB), ru = mkrsrc(p.ws + OFF_U, 80 * MB);
    const unsigned long long tmask = wait_sides(p, lane, cap) >> 1;
    flags_wait(p, FL_GU + (s & 1), 2, H, (s >> 1) + 1, lane, cap);
    u32x4 uf[4], tf;
#pragma unroll
    for (int j = 0; j < 4; ++j) uf[j] = ld16_sc1(ru, lane * 16 + (4 * wave + j) * 1024, (s * NCT + c) * 32768);
    if (wave == 0) flags_wait(p, FL_CB + c, 0, 1, s, lane, cap);
    __syncthreads();
    flags_wait(p, FL_PX + 8 * wave, 1, 8, s + 1, lane, cap);
    u32x4 pf[4][2], pm[2];
#pragma unroll
    for (int j = 0; j < 4; ++j) { pf[j][0] = ld16_sc1(rpx, lane * 16 + (4 * wave + j) * 2048, s * 65536); pf[j][1] = ld16_sc1(rpx, lane * 16 + (4 * wave + j) * 2048 + 1024, s * 65536); }
    pm[0] = ld16_sc1(rpx, (4 * wave + g) * 512 + l16 * 16, 4 * MB + s * 16384); pm[1] = ld16_sc1(rpx, (4 * wave + g) * 512 + 256 + l16 * 16, 4 * MB + s * 16384);
    for (int sp = 0;; ++sp) {
        asm volatile("" ::: "memory");
        tf = ld16_sc1(rt, lane * 16 + wave * 1024, t_off(s, tmask) + c * 16384);
        if (__all(mx4(tf) != SENT)) break;
        if (sp > cap) { flag_set(p, FL_TMO, 1u); cap = 0; break; }
    }
    f32x4 d0 = mf16(tf, pm[0], zero4()), d1 = mf16(tf, pm[1], zero4());
#pragma unroll
    for (int j = 0; j < 4; ++j) { d0 = mf16(uf[j], pf[j][0], d0); d1 = mf16(uf[j], pf[j][1], d1); }
    *(f32x4*)(lds + wave * 2048 + lane * 16) = d0; *(f32x4*)(lds + wave * 2048 + 1024 + lane * 16) = d1;
    __syncthreads();
    if (wave < 2) {
        f32x4 a = {};
#pragma unroll
        for (int w = 0; w < 8; ++w) a += *(const f32x4*)(lds + w * 2048 + wave * 1024 + lane * 16);
        *(u32x2*)(p.out + (size_t)(s * 32 + 16 * wave + l16) * (2 * D) + D + c * 16 + 4 * g) = u32x2{pk_bf16(a[0], a[1]), pk_bf16(a[2], a[3])};
    }
}

struct cf { float x, y; };
DEV cf cmul(cf a, cf b) { return cf{a.x * b.x - a.y * b.y, a.x * b.y + a.y * b.x}; }
DEV cf cmulc(cf a, cf b) { return cf{a.x * b.x + a.y * b.y, a.y * b.x - a.x * b.y}; }
DEV cf cadd(cf a, cf b) { return cf{a.x + b.x, a.y + b.y}; }
DEV cf csub(cf a, cf b) { return cf{a.x - b.x, a.y - b.y}; }
template <int SG> DEV cf mulmi(cf a) { return SG < 0 ? cf{a.y, -a.x} : cf{-a.y, a.x}; }
template <int SG> DEV void dft4(cf& a, cf& b, cf& c, cf& d) {
    cf t0 = cadd(a, c), t1 = csub(a, c), t2 = cadd(b, d), t3 = mulmi<SG>(csub(b, d));
    a = cadd(t0, t2); c = csub(t0, t2); b = cadd(t1, t3); d = csub(t1, t3);
}
template <int SG> DEV void dft8(cf* v) {
    constexpr float R = 0.70710678118654752f;
    dft4<SG>(v[0], v[2], v[4], v[6]);
    dft4<SG>(v[1], v[3], v[5], v[7]);
    cf o1 = cf{(v[3].x - SG * v[3].y) * R, (v[3].y + SG * v[3].x) * R};
    cf o2 = mulmi<SG>(v[5]);
    cf o3 = cf{(-v[7].x - SG * v[7].y) * R, (-v[7].y + SG * v[7].x) * R};
    cf e0 = v[0], e1 = v[2], e2 = v[4], e3 = v[6], o0 = v[1];
    v[0] = cadd(e0, o0); v[4] = csub(e0, o0);
    v[1] = cadd(e1, o1); v[5] = csub(e1, o1);
    v[2] = cadd(e2, o2); v[6] = csub(e2, o2);
    v[3] = cadd(e3, o3); v[7] = csub(e3, o3);
}
template <int SG> DEV void dft16(cf* v) {
    constexpr float C1 = 0.92387953251128674f, S1 = 0.38268343236508977f, R = 0.70710678118654752f;
#pragma unroll
    for (int b = 0; b < 4; ++b) dft4<SG>(v[b], v[4 + b], v[8 + b], v[12 + b]);
    const cf w1 = {C1, SG * S1}, w2 = {R, SG * R}, w3 = {S1, SG * C1}, w6 = {-R, SG * R}, w9 = {-C1, -SG * S1};
    v[5] = cmul(v[5], w1); v[6] = cmul(v[6], w2); v[7] = cmul(v[7], w3);
    v[9] = cmul(v[9], w2); v[10] = mulmi<SG>(v[10]); v[11] = cmul(v[11], w6);
    v[13] = cmul(v[13], w3); v[14] = cmul(v[14], w6); v[15] = cmul(v[15], w9);
#pragma unroll
    for (int c = 0; c < 4; ++c) dft4<SG>(v[4 * c], v[4 * c + 1], v[4 * c + 2], v[4 * c + 3]);
    cf t;
    t = v[1]; v[1] = v[4]; v[4] = t;  t = v[2]; v[2] = v[8]; v[8] = t;  t = v[3]; v[3] = v[12]; v[12] = t;
    t = v[6]; v[6] = v[9]; v[9] = t;  t = v[7]; v[7] = v[13]; v[13] = t; t = v[11]; v[11] = v[14]; v[14] = t;
}
template <int SG> DEV void dft5(cf* v) {
    constexpr float C1 = 0.30901699437494742f, C2 = -0.80901699437494742f, S1 = 0.95105651629515357f, S2 = 0.58778525229247313f;
    cf a1 = cadd(v[1], v[4]), b1 = csub(v[1], v[4]), a2 = cadd(v[2], v[3]), b2 = csub(v[2], v[3]);
    cf r1 = {v[0].x + C1 * a1.x + C2 * a2.x, v[0].y + C1 * a1.y + C2 * a2.y};
    cf r2 = {v[0].x + C2 * a1.x + C1 * a2.x, v[0].y + C2 * a1.y + C1 * a2.y};
    cf i1 = mulmi<SG>(cf{S1 * b1.x + S2 * b2.x, S1 * b1.y + S2 * b2.y});
    cf i2 = mulmi<SG>(cf{S2 * b1.x - S1 * b2.x, S2 * b1.y - S1 * b2.y});
    v[0] = cadd(v[0], cadd(a1, a2));
    v[1] = cadd(r1, i1); v[4] = csub(r1, i1); v[2] = cadd(r2, i2); v[3] = csub(r2, i2);
}
DEV cf twid(int num, int den) { float s, c; sincospif(-2.0f * (float)num / (float)den, &s, &c); return cf{c, s}; }

constexpr int FRP = 648;
template <int SG> DEV void fft640x8(cf* a, cf* b, const cf* tab, int tid) {
    if (tid < 320) {
        const int q = tid / 40, j = tid % 40; cf v[16];
#pragma unroll
        for (int r = 0; r < 16; ++r) v[r] = a[q * FRP + j + 40 * r];
        dft16<SG>(v);
#pragma unroll
        for (int r = 0; r < 16; ++r) b[q * FRP + 16 * j + (r ^ (j & 15))] = v[r];
    }
    __syncthreads();
    for (int t = tid; t < 640; t += 512) {
        const int q = t / 80, j = t % 80, k = j & 15; cf v[8];
#pragma unroll
        for (int r = 0; r < 8; ++r) { const int n = j + 80 * r; v[r] = b[q * FRP + (n & ~15) + ((n & 15) ^ ((n >> 4) & 15))]; }
#pragma unroll
        for (int r = 1; r < 8; ++r) { const cf w = tab[5 * k * r]; v[r] = SG < 0 ? cmul(v[r], w) : cmulc(v[r], w); }
        dft8<SG>(v);
#pragma unroll
        for (int r = 0; r < 8; ++r) a[q * FRP + (j >> 4) * 128 + k + 16 * r] = v[r];
    }
    __syncthreads();
    for (int t = tid; t < 1024; t += 512) {
        const int q = t >> 7, j = t & 127; cf v[5];
#pragma unroll
        for (int r = 0; r < 5; ++r) v[r] = a[q * FRP + j + 128 * r];
#pragma unroll
        for (int r = 1; r < 5; ++r) { const cf w = tab[j * r]; v[r] = SG < 0 ? cmul(v[r], w) : cmulc(v[r], w); }
        dft5<SG>(v);
#pragma unroll
        for (int r = 0; r < 5; ++r) b[q * FRP + j + 128 * r] = v[r];
    }
    __syncthreads();
}
DEV void make_tab640(cf* tab, int tid) { for (int i = tid; i < 640; i += 512) tab[i] = twid(i, 640); }

DEV void task_fft1(const Params& p, int t, char* lds) {
    const int tid = threadIdx.x;
    cf* a = (cf*)lds; cf* b = a + 8 * FRP; cf* tab = b + 8 * FRP;
    make_tab640(tab, tid);
    for (int i = tid; i < 8 * 80; i += 512) {
        const int q = i / 80, ch = i % 80;
        const u32x4 u = *(const u32x4*)(p.ax + (size_t)(16 * t + 2 * q) * D + ch * 8), w = *(const u32x4*)(p.ax + (size_t)(16 * t + 2 * q + 1) * D + ch * 8);
        cf* d = a + q * FRP + ch * 8;
        d[0] = cf{bf_lo(u.x), bf_lo(w.x)}; d[1] = cf{bf_hi(u.x), bf_hi(w.x)}; d[2] = cf{bf_lo(u.y), bf_lo(w.y)}; d[3] = cf{bf_hi(u.y), bf_hi(w.y)};
        d[4] = cf{bf_lo(u.z), bf_lo(w.z)}; d[5] = cf{bf_hi(u.z), bf_hi(w.z)}; d[6] = cf{bf_lo(u.w), bf_lo(w.w)}; d[7] = cf{bf_hi(u.w), bf_hi(w.w)};
    }
    __syncthreads();
    fft640x8<-1>(a, b, tab, tid);
    const __amdgpu_buffer_rsrc_t rx = mkrsrc(p.ws + OFF_X1, 6 * MB);
    for (int i = tid; i < DF * 8; i += 512) {
        const int k = i >> 3, q = i & 7;
        const cf z = b[q * FRP + k], y = b[q * FRP + (k ? 640 - k : 0)];
        f32x4 o = {0.5f * (z.x + y.x), 0.5f * (z.y - y.y), 0.5f * (z.y + y.y), 0.5f * (y.x - z.x)};
        st16_sc1(__builtin_bit_cast(u32x4, o), rx, (k * S + 16 * t + 2 * q) * 8, 0);
    }
    unsigned* wt = (unsigned*)a;
    for (int i = tid; i < 16 * DF; i += 512) { const int r = i / DF, k = i % DF; wt[k * 17 + r] = (unsigned)p.wr[(size_t)(16 * t + r) * DF + k] | ((unsigned)p.wi[(size_t)(16 * t + r) * DF + k] << 16); }
    __syncthreads();
    const __amdgpu_buffer_rsrc_t rw = mkrsrc(p.ws + OFF_WT, 3 * MB);
    for (int i = tid; i < DF * 4; i += 512) { const int k = i >> 2, r4 = (i & 3) * 4;
        u32x4 v = {wt[k * 17 + r4], wt[k * 17 + r4 + 1], wt[k * 17 + r4 + 2], wt[k * 17 + r4 + 3]};
        st16_sc1(v, rw, (k * S + 16 * t + r4) * 4, 0); }
    __syncthreads();
}

constexpr int CP = 2048 + 128;
DEV int cpad(int i) { return i + (i >> 4); }
DEV void fft2_twiddles(cf (&t1)[16], cf (&t2)[16]) {
    const int m = threadIdx.x & 127, n2 = m >> 4;
#pragma unroll
    for (int i = 1; i < 16; ++i) { t1[i] = twid(m * i, 2048); t2[i] = twid(n2 * i, 128); }
}
DEV void task_fft2(const Params& p, int t, char* lds, cf (&t1)[16], cf (&t2)[16]) {
    int tid = threadIdx.x;
    asm volatile("" : "+v"(tid));
#pragma unroll
    for (int i = 1; i < 16; ++i) asm volatile("" : "+v"(t1[i].x), "+v"(t1[i].y), "+v"(t2[i].x), "+v"(t2[i].y));
    const int col = tid >> 7, m = tid & 127;
    const int k = 4 * t + col;
    const bool act = k < DF;
    cf* buf = (cf*)lds + col * CP;
    const __amdgpu_buffer_rsrc_t rx = mkrsrc(p.ws + OFF_X1, (unsigned)(DF * S * 8)), ry = mkrsrc(p.ws + OFF_Y1, (unsigned)(DF * S * 8)), rw = mkrsrc(p.ws + OFF_WT, (unsigned)(DF * S * 4));
    cf v[16];
#pragma unroll
    for (int n0 = 0; n0 < 16; ++n0) { u32x2 u = ld8_sc1(rx, (k * S + 128 * n0 + m) * 8, 0); v[n0] = __builtin_bit_cast(cf, u); }
    const int f0b = m & 15, n2 = m >> 4;
    const int f1c = m >> 4;
    unsigned wv[16];
#pragma unroll
    for (int u = 0; u < 2; ++u)
#pragma unroll
        for (int f2 = 0; f2 < 8; ++f2) wv[u * 8 + f2] = __builtin_amdgcn_raw_buffer_load_b32(rw, (k * S + f0b + 16 * (f1c + 8 * u) + 256 * f2) * 4, 0, 16);
    dft16<-1>(v);
#pragma unroll
    for (int f0 = 1; f0 < 16; ++f0) v[f0] = cmul(v[f0], t1[f0]);
#pragma unroll
    for (int f0 = 0; f0 < 16; ++f0) buf[cpad(m * 16 + f0)] = v[f0];
    __syncthreads();
#pragma unroll
    for (int n1 = 0; n1 < 16; ++n1) v[n1] = buf[cpad((8 * n1 + n2) * 16 + f0b)];
    dft16<-1>(v);
#pragma unroll
    for (int f1 = 1; f1 < 16; ++f1) v[f1] = cmul(v[f1], t2[f1]);
    __syncthreads();
#pragma unroll
    for (int f1 = 0; f1 < 16; ++f1) buf[cpad((f1 * 8 + n2) * 16 + f0b)] = v[f1];
    __syncthreads();
#pragma unroll
    for (int u = 0; u < 2; ++u)
#pragma unroll
        for (int j = 0; j < 8; ++j) v[u * 8 + j] = buf[cpad(((f1c + 8 * u) * 8 + j) * 16 + f0b)];
    dft8<-1>(v); dft8<-1>(v + 8);
    constexpr float NRM = 1.0f / (2048.0f * 640.0f);
#pragma unroll
    for (int i = 0; i < 16; ++i) v[i] = cmul(v[i], cf{bf_lo(wv[i]) * NRM, bf_hi(wv[i]) * NRM});
    dft8<1>(v); dft8<1>(v + 8);
#pragma unroll
    for (int u = 0; u < 2; ++u)
#pragma unroll
        for (int j = 0; j < 8; ++j) buf[cpad(((f1c + 8 * u) * 8 + j) * 16 + f0b)] = v[u * 8 + j];
    __syncthreads();
#pragma unroll
    for (int f1 = 0; f1 < 16; ++f1) v[f1] = buf[cpad((f1 * 8 + n2) * 16 + f0b)];
#pragma unroll
    for (int f1 = 1; f1 < 16; ++f1) v[f1] = cmulc(v[f1], t2[f1]);
    dft16<1>(v);
#pragma unroll
    for (int n1 = 0; n1 < 16; ++n1) buf[cpad((8 * n1 + n2) * 16 + f0b)] = v[n1];
    __syncthreads();
#pragma unroll
    for (int f0 = 0; f0 < 16; ++f0) v[f0] = buf[cpad(m * 16 + f0)];
#pragma unroll
    for (int f0 = 1; f0 < 16; ++f0) v[f0] = cmulc(v[f0], t1[f0]);
    dft16<1>(v);
    if (act) {
#pragma unroll
        for (int n0 = 0; n0 < 16; ++n0) st8_sc1(__builtin_bit_cast(u32x2, v[n0]), ry, (k * S + 128 * n0 + m) * 8, 0);
    }
    __syncthreads();
}

DEV void task_fft3(const Params& p, int t, char* lds) {
    const int tid = threadIdx.x;
    cf* a = (cf*)lds; cf* b = a + 8 * FRP; cf* tab = b + 8 * FRP;
    make_tab640(tab, tid);
    const __amdgpu_buffer_rsrc_t ry = mkrsrc(p.ws + OFF_Y1, 6 * MB);
    for (int i = tid; i < DF * 8; i += 512) {
        const int k = i >> 3, q = i & 7;
        f32x4 o = __builtin_bit_cast(f32x4, ld16_sc1(ry, (k * S + 16 * t + 2 * q) * 8, 0));
        if (k == 0 || k == 320) { o[1] = 0.f; o[3] = 0.f; }
        a[q * FRP + k] = cf{o[0] - o[3], o[1] + o[2]};
        if (k > 0 && k < 320) a[q * FRP + 640 - k] = cf{o[0] + o[3], o[2] - o[1]};
    }
    __syncthreads();
    fft640x8<1>(a, b, tab, tid);
    for (int i = tid; i < 8 * 80; i += 512) {
        const int q = i / 80, ch = i % 80;
        const cf* d = b + q * FRP + ch * 8;
        u32x4 u = {pk_bf16(d[0].x, d[1].x), pk_bf16(d[2].x, d[3].x), pk_bf16(d[4].x, d[5].x), pk_bf16(d[6].x, d[7].x)};
        u32x4 w = {pk_bf16(d[0].y, d[1].y), pk_bf16(d[2].y, d[3].y), pk_bf16(d[4].y, d[5].y), pk_bf16(d[6].y, d[7].y)};
        *(u32x4*)(p.out + (size_t)(16 * t + 2 * q) * (2 * D) + ch * 8) = u;
        *(u32x4*)(p.out + (size_t)(16 * t + 2 * q + 1) * (2 * D) + ch * 8) = w;
    }
    __syncthreads();
}

constexpr int NT1 = 128, NT2 = 81, NT3 = 128, NFINE = 2, NTO = (NSEG - NFINE) * 5;
__global__ void __launch_bounds__(512, 2) k_main(Params p) {
    extern __shared__ __attribute__((aligned(16))) char lds[];
#define TL(slot) do {} while (0)
    const int role = get_role(p, lds);
    TL(1);
    if (role < 32) { role_qk(p, role, lds); TL(7); return; }
    if (role < 64) { role_vo(p, role - 32, lds); TL(7); return; }
    if (role < NCHAIN) { role_comb(p, role - 64, lds); TL(7); return; }
    const int w = role - NCHAIN, tid = threadIdx.x, lane = tid & 63;
    if (w < 64) role_gemm_qk(p, w >> 1, w & 1, lds);
    else if (w < 128) role_gemm_vu(p, (w - 64) >> 1, w & 1, lds);
    else if (tid < 64 && w - 128 < 1 + NTPG) probe_page(p, w - 128, lane);
    TL(2);
    int cap = SPIN_CAP;
#define NEXT_TASK(Q) (__syncthreads(), bcast0(threadIdx.x == 0 ? (int)flag_add(p, Q) : 0, lds))
    if (w < 48 && (w & 1)) { TL(3); TL(4); TL(5); TL(7); return; }
    if (w < 128) {
        int t = NEXT_TASK(FL_QUEUE);
        while (t < NT1) {
            task_fft1(p, t, lds);
            WAIT_VM(0); __syncthreads();
            if (tid == 0) flag_add(p, FL_F1);
            t = NEXT_TASK(FL_QUEUE);
        }
        TL(3);
        cf tw1[16], tw2[16];
        if (t < NT1 + NT2) fft2_twiddles(tw1, tw2);
        while (t < NT1 + NT2) {
            if (tid < 64) flags_wait(p, FL_F1, 0, 1, NT1, lane, cap);
            __syncthreads();
            task_fft2(p, t - NT1, lds, tw1, tw2);
            WAIT_VM(0); __syncthreads();
            if (tid == 0) flag_add(p, FL_F2);
            t = NEXT_TASK(FL_QUEUE);
        }
        TL(4);
        while (t < NT1 + NT2 + NT3) {
            if (tid < 64) flags_wait(p, FL_F2, 0, 1, NT2, lane, cap);
            __syncthreads();
            task_fft3(p, t - NT1 - NT2, lds);
            t = NEXT_TASK(FL_QUEUE);
        }
        TL(5);
        t -= NT1 + NT2 + NT3;
        if (t < NFINE * NCT) task_out_fine(p, NSEG - NFINE + t / NCT, t % NCT, lds, cap);
    } else {
        int t = NEXT_TASK(FL_QOUT);
        while (t < NTO) {
            task_out(p, t / 5, t % 5, lds, cap);
            t = NEXT_TASK(FL_QOUT);
        }
    }
    TL(7);
}

extern "C" void kernel_launch(void* const* d_in, const int* in_sizes, int n_in, void* d_out, int out_size, void* d_ws, size_t ws_size, hipStream_t stream) {
    Params p;
    p.ax = (const unsigned short*)d_in[0]; p.wr = (const unsigned short*)d_in[1]; p.wi = (const unsigned short*)d_in[2];
    p.bx = (const unsigned short*)d_in[3]; p.mem0 = (const unsigned short*)d_in[4];
    p.wq = (const unsigned short*)d_in[5]; p.wk = (const unsigned short*)d_in[6]; p.wv = (const unsigned short*)d_in[7]; p.wo = (const unsigned short*)d_in[8];
    p.out = (unsigned short*)d_out;
    p.ws = (char*)(((size_t)d_ws + 2 * MB - 1) & ~(size_t)(2 * MB - 1));
    constexpr int LDS = 163840;
    (void)hipFuncSetAttribute((const void*)k_main, hipFuncAttributeMaxDynamicSharedMemorySize, LDS);
    (void)hipMemsetAsync(p.ws + OFF_CTRL, 0, FLAG_BYTES, stream);
    k_main<<<NWG, 512, LDS, stream>>>(p);
}
```

```cpp
#include <hip/hip_runtime.h>
#include <hip/hip_bf16.h>

typedef __bf16 bf16x8 __attribute__((ext_vector_type(8)));
typedef __bf16 bf16x16 __attribute__((ext_vector_type(16)));
typedef __bf16 bf16x2 __attribute__((ext_vector_type(2)));
typedef short s16x4 __attribute__((ext_vector_type(4)));
typedef float f32x2 __attribute__((ext_vector_type(2)));
typedef float f32x4 __attribute__((ext_vector_type(4)));
typedef float f32x16 __attribute__((ext_vector_type(16)));
typedef unsigned u32x2 __attribute__((ext_vector_type(2)));
typedef unsigned u32x4 __attribute__((ext_vector_type(4)));
typedef unsigned u32x8 __attribute__((ext_vector_type(8)));
#define LAS __attribute__((address_space(3)))
#define GAS __attribute__((address_space(1)))
#define DEV __device__ __forceinline__

constexpr int S = 2048, D = 640, DF = 321, H = 32, DH = 128, NSEG = 64, NCT = 40;
constexpr unsigned SENT = 0xFFFFFFFFu;
constexpr int SPIN_CAP = 1 << 15;

constexpr size_t MB = 1 << 20;
constexpr size_t OFF_CTRL = 0;
constexpr size_t OFF_COMM0 = 2 * MB;
constexpr size_t OFF_QX = 6 * MB;
constexpr size_t OFF_KX = OFF_QX + 16 * MB;
constexpr size_t OFF_U = OFF_KX + 16 * MB;
constexpr size_t OFF_T = OFF_U + 80 * MB;
constexpr int NTPG = 22;
constexpr size_t OFF_PX = OFF_T + 44 * MB;
constexpr size_t OFF_PXM = OFF_PX + 4 * MB;
constexpr size_t OFF_X1 = OFF_PXM + 1 * MB;
constexpr size_t OFF_Y1 = OFF_X1 + 6 * MB;
constexpr size_t OFF_WT = OFF_Y1 + 6 * MB;
constexpr size_t WS_NEED = OFF_WT + 3 * MB;

constexpr int RM = 4, RP = 2;
DEV unsigned mema_off(int sl, int r) { return (sl * 4 + r) * 8192; }
DEV unsigned memb_off(int sl, int r) { return (16 + sl) * 8192 + r * 2048; }
DEV unsigned mem_off(int sl, int r, int c) { return c < 32 ? mema_off(sl, r) + c * 256 : memb_off(sl, r) + (c - 32) * 256; }
DEV unsigned px_base(int sl, int r) { return (20 + (sl * 2 + r) * 2) * 8192; }
DEV unsigned pm_base(int sl, int r) { return (36 + sl) * 8192 + r * 4096; }
constexpr unsigned ANN_BASE = 40 * 8192, STA_BASE = 41 * 8192, STB_BASE = 42 * 8192, COMM_CHUNKS = 44;
DEV unsigned rowi(unsigned m, unsigned t) { return 4 * (m >> 1) + 2 * t + (m & 1); }
DEV unsigned physo(unsigned o) { return ((o >> 13) << 14) + (o & 8191u); }
DEV unsigned t_off(int s, unsigned long long tmask) { const int pg = s / 3; return pg * (2u << 20) + (s - 3 * pg) * (NCT * 16384) + (((unsigned)(tmask >> pg) & 1u) << 13); }
constexpr int FL_TNEAR = 0, FL_TFAR = 1, FL_QUEUE = 2, FL_READY = 3, FL_F1 = 4, FL_F2 = 5, FL_TMO = 6, FL_QOUT = 7, FL_SIDE = 8, FL_GQ = 32, FL_GU = 96, FL_PX = 160, FL_CB = 224, FL_END = 264;
constexpr int FLAG_BYTES = FL_END * 128;
constexpr int TUNE_W = 16384;
struct Params {
    const unsigned short* ax; const unsigned short* wr; const unsigned short* wi;
    const unsigned short* bx; const unsigned short* mem0;
    const unsigned short* wq; const unsigned short* wk; const unsigned short* wv; const unsigned short* wo;
    unsigned short* out; char* ws;
};

DEV __amdgpu_buffer_rsrc_t mkrsrc(const void* p, unsigned bytes) { return __builtin_amdgcn_make_buffer_rsrc((void*)p, 0, bytes, 0x00020000); }
DEV u32x4 ld16_sc1(__amdgpu_buffer_rsrc_t r, unsigned voff, unsigned soff) { return __builtin_amdgcn_raw_buffer_load_b128(r, voff, soff, 16); }
DEV u32x2 ld8_sc1(__amdgpu_buffer_rsrc_t r, unsigned voff, unsigned soff) { return __builtin_amdgcn_raw_buffer_load_b64(r, voff, soff, 16); }
DEV void st16_sc1(u32x4 v, __amdgpu_buffer_rsrc_t r, unsigned voff, unsigned soff) { __builtin_amdgcn_raw_buffer_store_b128(v, r, voff, soff, 16); asm volatile("s_nop 1" :: "v"(v) : "memory"); }
DEV void st8_sc1(u32x2 v, __amdgpu_buffer_rsrc_t r, unsigned voff, unsigned soff) { __builtin_amdgcn_raw_buffer_store_b64(v, r, voff, soff, 16); }
DEV void st4_sc1(unsigned v, __amdgpu_buffer_rsrc_t r, unsigned voff, unsigned soff) { __builtin_amdgcn_raw_buffer_store_b32(v, r, voff, soff, 16); }
DEV unsigned pk_bf16(float a, float b) { f32x2 f = {a, b}; bf16x2 p = __builtin_convertvector(f, bf16x2); return __builtin_bit_cast(unsigned, p); }
DEV float bf_lo(unsigned u) { return __builtin_bit_cast(float, u << 16); }
DEV float bf_hi(unsigned u) { return __builtin_bit_cast(float, u & 0xFFFF0000u); }
DEV unsigned nosent(unsigned u) { return u < 0xFFFFFFFEu ? u : 0xFFFFFFFEu; }
DEV unsigned mx4(u32x4 v) { return max(max(v.x, v.y), max(v.z, v.w)); }
DEV f32x4 zero4() { return f32x4{0.f, 0.f, 0.f, 0.f}; }
DEV u32x4 mk4(u32x2 a, u32x2 b) { return u32x4{a.x, a.y, b.x, b.y}; }
DEV bf16x8 as_bf8(u32x4 v) { return __builtin_bit_cast(bf16x8, v); }
DEV u32x2 tr_read(const char* lds, unsigned off) { s16x4 v = __builtin_amdgcn_ds_read_tr16_b64_v4i16((LAS s16x4*)(lds + off)); return __builtin_bit_cast(u32x2, v); }
DEV u32x4 lds_ld16(const char* lds, unsigned off) { return *(const u32x4*)(lds + off); }
DEV void lds_st16(char* lds, unsigned off, u32x4 v) { *(u32x4*)(lds + off) = v; }
DEV void glds16(const void* g, char* lds) { __builtin_amdgcn_global_load_lds((const GAS unsigned*)g, (LAS unsigned*)lds, 16, 0, 0); }
#define WAIT_VM(n) asm volatile("s_waitcnt vmcnt(" #n ")" ::: "memory")
#define WAIT_LGKM0() asm volatile("s_waitcnt lgkmcnt(0)" ::: "memory")
DEV void raw_barrier() { asm volatile("" ::: "memory"); __builtin_amdgcn_s_waitcnt(0xC07F); __builtin_amdgcn_s_barrier(); asm volatile("" ::: "memory"); }
DEV float max3f(float a, float b, float c) { return __builtin_fmaxf(__builtin_fmaxf(a, b), c); }
template <class T> DEV T lds_vol_ld(const char* p) { return *(volatile LAS T*)p; }
template <class T> DEV void lds_vol_st(char* p, T v) { *(volatile LAS T*)p = v; }

DEV unsigned swz(unsigned row) { return ((row & 3u) << 2) | ((row >> 2) & 3u); }
DEV unsigned img_off(unsigned row, unsigned ch) { return 256u * row + 16u * (ch ^ swz(row)); }
DEV void bdma16(__amdgpu_buffer_rsrc_t r, char* lds, unsigned voff, unsigned soff) { __builtin_amdgcn_raw_ptr_buffer_load_lds(r, (LAS void*)lds, 16, voff, soff, 0, 0); }
template <int NROWS>
DEV void stage_img(__amdgpu_buffer_rsrc_t r, unsigned elem0, unsigned pitch, char* img, int wave, int lane) {
    const unsigned row0 = 4 * wave + (lane >> 4), voff = (row0 * pitch + (((lane & 15) ^ swz(row0)) << 3)) * 2;
#pragma unroll
    for (int it = 0; it < NROWS / 32; ++it) bdma16(r, img + (wave + 8 * it) * 1024, voff, (elem0 + it * 32 * pitch) * 2);
}
DEV unsigned lds_addr(const char* p) { return (unsigned)(size_t)(const LAS char*)p; }
DEV void tr8(u32x2 (&o)[8], const unsigned (&a)[8]) {
    asm volatile("ds_read_b64_tr_b16 %0, %8\n\tds_read_b64_tr_b16 %1, %9\n\tds_read_b64_tr_b16 %2, %10\n\tds_read_b64_tr_b16 %3, %11\n\t"
                 "ds_read_b64_tr_b16 %4, %12\n\tds_read_b64_tr_b16 %5, %13\n\tds_read_b64_tr_b16 %6, %14\n\tds_read_b64_tr_b16 %7, %15\n\ts_waitcnt lgkmcnt(0)"
                 : "=&v"(o[0]), "=&v"(o[1]), "=&v"(o[2]), "=&v"(o[3]), "=&v"(o[4]), "=&v"(o[5]), "=&v"(o[6]), "=&v"(o[7])
                 : "v"(a[0]), "v"(a[1]), "v"(a[2]), "v"(a[3]), "v"(a[4]), "v"(a[5]), "v"(a[6]), "v"(a[7]) : "memory");
}
DEV unsigned a16(const char* img, unsigned r0, unsigned c, int lane) {
    const unsigned q = (lane & 15) >> 2, p = lane & 3;
    return lds_addr(img) + img_off(r0 + q, 2 * c + (p >> 1)) + 8 * (p & 1);
}
DEV unsigned a32(const char* img, unsigned r0, unsigned c, int lane) {
    const unsigned blk = (lane >> 4) & 1, q = (lane & 15) >> 2, p = lane & 3;
    return lds_addr(img) + img_off(r0 + q, 4 * c + 2 * blk + (p >> 1)) + 8 * (p & 1);
}
DEV unsigned a16p(const char* img, unsigned k0, unsigned c, int lane) {
    const unsigned q = (lane & 15) >> 2, p = lane & 3;
    const unsigned row = (k0 & ~15u) + 8 * (q >> 1) + 2 * ((k0 & 15u) >> 2) + (q & 1);
    return lds_addr(img) + img_off(row, 2 * c + (p >> 1)) + 8 * (p & 1);
}
template <bool PERM>
DEV void load_bsp2(bf16x16& x, bf16x16& y, const char* imgx, unsigned cx, const char* imgy, unsigned cy, int lane) {
    const unsigned g = lane >> 4;
    unsigned a[8]; u32x2 o[8];
#pragma unroll
    for (int i = 0; i < 4; ++i) {
        const unsigned k0 = 32 * (i >> 1) + 8 * g + 4 * (i & 1);
        a[i] = PERM ? a16p(imgx, k0, cx, lane) : a16(imgx, k0, cx, lane); a[4 + i] = PERM ? a16p(imgy, k0, cy, lane) : a16(imgy, k0, cy, lane);
    }
    tr8(o, a);
    u32x8 r = {o[0].x, o[0].y, o[1].x, o[1].y, o[2].x, o[2].y, o[3].x, o[3].y}, t = {o[4].x, o[4].y, o[5].x, o[5].y, o[6].x, o[6].y, o[7].x, o[7].y};
    x = __builtin_bit_cast(bf16x16, r); y = __builtin_bit_cast(bf16x16, t);
}
DEV f32x4 smf(u32x4 a, bf16x16 b, f32x4 c, int idx) { return __builtin_amdgcn_smfmac_f32_16x16x64_bf16(as_bf8(a), b, c, idx, 0, 0); }
DEV f32x4 mf16(u32x4 a, u32x4 b, f32x4 c) { return __builtin_amdgcn_mfma_f32_16x16x32_bf16(as_bf8(a), as_bf8(b), c, 0, 0, 0); }
DEV f32x16 mf32(u32x4 a, u32x4 b, f32x16 c) { return __builtin_amdgcn_mfma_f32_32x32x16_bf16(as_bf8(a), as_bf8(b), c, 0, 0, 0); }
DEV float u2f(unsigned u) { return __builtin_bit_cast(float, u); }
DEV unsigned f2u(float f) { return __builtin_bit_cast(unsigned, f); }
DEV float xmax16_32(float x) {
    auto r = __builtin_amdgcn_permlane16_swap(f2u(x), f2u(x), false, false);
    x = fmaxf(u2f(r[0]), u2f(r[1]));
    auto r2 = __builtin_amdgcn_permlane32_swap(f2u(x), f2u(x), false, false);
    return fmaxf(u2f(r2[0]), u2f(r2[1]));
}
DEV float xsum16_32(float x) {
    auto r = __builtin_amdgcn_permlane16_swap(f2u(x), f2u(x), false, false);
    x = u2f(r[0]) + u2f(r[1]);
    auto r2 = __builtin_amdgcn_permlane32_swap(f2u(x), f2u(x), false, false);
    return u2f(r2[0]) + u2f(r2[1]);
}

#define ST(role, s, slot) do {} while (0)
#define MT(x) do {} while (0)
#define MTW() do {} while (0)
#define PACC(acc, a, b) do {} while (0)
#define PDUMP(role, x, y, z, w) do {} while (0)
#define WS(k) do {} while (0)
#define WSDECL do {} while (0)
#define WSDUMP() do {} while (0)
#define TLR(role, slot) do {} while (0)
DEV unsigned* flag_ptr(const Params& p, int line) { return (unsigned*)(p.ws + OFF_CTRL) + line * 32; }
DEV void flag_set(const Params& p, int line, unsigned v) { __hip_atomic_store(flag_ptr(p, line), v, __ATOMIC_RELAXED, __HIP_MEMORY_SCOPE_AGENT); }
DEV unsigned flag_add(const Params& p, int line) { return __hip_atomic_fetch_add(flag_ptr(p, line), 1u, __ATOMIC_RELAXED, __HIP_MEMORY_SCOPE_AGENT); }
DEV unsigned flag_get(const Params& p, int line) { return __hip_atomic_load(flag_ptr(p, line), __ATOMIC_RELAXED, __HIP_MEMORY_SCOPE_AGENT); }
DEV unsigned flags_wait(const Params& p, int line0, int stride, int n, unsigned want, int lane, int& cap) {
    unsigned v = 0;
    for (int sp = 0;; ++sp) {
        v = flag_get(p, line0 + stride * (lane < n ? lane : 0));
        if (__all(v >= want)) break;
        if (sp > cap) { flag_set(p, FL_TMO, 1u); cap = 0; break; }
        __builtin_amdgcn_s_sleep(4);
    }
    asm volatile("" ::: "memory");
    return v;
}
#define TUNEW(i, dflt) (dflt)
DEV void probe_page(const Params& p, int pg, int lane) {
    const unsigned xcc = __builtin_amdgcn_s_getreg(20 | (3 << 11)) & 0xf;
    unsigned best[2] = {1u << 30, 1u << 30};
    char* base = p.ws + (pg < 1 ? OFF_COMM0 : OFF_T + (pg - 1) * 2 * MB) + 2 * MB - 16384;
    const __amdgpu_buffer_rsrc_t r = mkrsrc(base, 16384);
    const u32x4 sv = {SENT, SENT, SENT, SENT};
    st16_sc1(sv, r, lane * 128, 0); st16_sc1(sv, r, lane * 128, 8192);
    WAIT_VM(0);
#pragma unroll 1
    for (int i = 0; i < (pg < 3 ? TUNEW(11, 7) : TUNEW(10, 12)); ++i) {
#pragma unroll
        for (int c = 0; c < 2; ++c) {
            const char* q = base + c * 8192 + i * 512;
            unsigned long long t0, t1; unsigned v;
            asm volatile("s_memtime %0\n\ts_waitcnt lgkmcnt(0)\n\tglobal_load_dword %2, %3, off sc1\n\ts_waitcnt vmcnt(0)\n\ts_memtime %1\n\ts_waitcnt lgkmcnt(0)"
                         : "=&s"(t0), "=&s"(t1), "=&v"(v) : "v"(q) : "memory");
            best[c] = min(best[c], (unsigned)(t1 - t0));
        }
    }
    unsigned side = best[0] < best[1] ? 0u : 1u;
    if (xcc >= 4) side ^= 1u;
    if (lane == 0) flag_set(p, FL_SIDE + pg, 1u + side);
}
DEV unsigned long long wait_sides(const Params& p, int lane, int& cap, int n = 1 + NTPG) {
    const unsigned v = flags_wait(p, FL_SIDE, 1, n, 1u, lane, cap);
    return __ballot(lane < n && v == 2u);
}

constexpr unsigned WBYTES = D * H * DH * 2;
#define STAGE_RING(RING, EXTRA, ISSUE, READ) do { \
        _Pragma("unroll") for (int bt = 0; bt < RING - 1; ++bt) { char* img = lds + bt * 32768; ISSUE; } \
        _Pragma("unroll") for (int bq = 0; bq < 10; ++bq) { \
            if (bq + RING - 1 <= 10) asm volatile("s_waitcnt vmcnt(%0)" :: "i"(4 * (RING - 2)) : "memory"); \
            else asm volatile("s_waitcnt vmcnt(%0)" :: "i"(4 * (9 - bq) + (EXTRA)) : "memory"); \
            raw_barrier(); \
            if (bq + RING - 1 < 10) { const int bt = bq + RING - 1; char* img = lds + (bt % RING) * 32768; ISSUE; } \
            else if (bq + RING - 1 == 10) { AFTER_W; } \
            { const int bt = bq; const char* img = lds + (bt % RING) * 32768; READ; } \
        } \
        raw_barrier(); \
    } while (0)
constexpr int NSPLIT = 16;
DEV int vu_seg(int par, int i) { return i < NSPLIT / 2 ? par + 2 * i : min(i + NSPLIT / 2, NSEG - 1); }
DEV int vu_par(int seg) { return seg < NSPLIT ? seg & 1 : 0; }
DEV unsigned vu_cnt(int seg) { return seg < NSPLIT ? (seg >> 1) + 1 : seg - NSPLIT / 2 + 1; }
constexpr int XT_BYTES = 40960;
DEV void stage_x(__amdgpu_buffer_rsrc_t rx, int seg, char* img, int wave, int lane) {
    const int tok = 8 * (wave & 3) + (lane >> 3), c = (lane & 7) ^ ((tok >> 1) & 7);
    const unsigned voff = (tok * D + (wave >> 2) * 64 + c * 8) * 2;
#pragma unroll
    for (int it = 0; it < 5; ++it) bdma16(rx, img + (wave + 8 * it) * 1024, voff, (seg * 32 * D + it * 128) * 2);
}
DEV void stage_x1(__amdgpu_buffer_rsrc_t rx, int seg, char* img, int wave, int lane, int it) {
    const int tok = 8 * (wave & 3) + (lane >> 3), c = (lane & 7) ^ ((tok >> 1) & 7);
    bdma16(rx, img + (wave + 8 * it) * 1024, (tok * D + (wave >> 2) * 64 + c * 8) * 2, (seg * 32 * D + it * 128) * 2);
}
DEV unsigned xfrag_off(int ks, int lane) {
    const int tok = lane & 31, hi = lane >> 5, c = 2 * (ks & 3) + hi;
    return (ks >> 2) * 4096 + tok * 128 + ((c ^ ((tok >> 1) & 7)) << 4);
}

DEV void role_gemm_qk(const Params& p, int h, char* lds) {
    constexpr int par = 0;
    const int tid = threadIdx.x, lane = tid & 63, wave = __builtin_amdgcn_readfirstlane(tid >> 6);
    u32x4 w[40];
    const __amdgpu_buffer_rsrc_t rw0 = mkrsrc(p.wq, WBYTES), rw1 = mkrsrc(p.wk, WBYTES), rx = mkrsrc(p.bx, S * D * 2);
    for (int i = 0; i < par * TUNEW(7, 0); ++i) __builtin_amdgcn_s_sleep(2);
    char* xbuf = lds + 40960;
#define XSLOT(i) (lds + ((i) % 3 == 0 ? 3 * XT_BYTES : ((i) % 3 - 1) * XT_BYTES))
    (void)xbuf;
#define AFTER_W stage_x(rx, par, XSLOT(0), wave, lane)
    STAGE_RING(3, 5,
        do { stage_img<64>(rw0, bt * 64 * H * DH + h * DH, H * DH, img, wave, lane); stage_img<64>(rw1, bt * 64 * H * DH + h * DH, H * DH, img + 16384, wave, lane); } while (0),
        do { unsigned a[8]; u32x2 o[8];
             _Pragma("unroll") for (int i = 0; i < 8; ++i) a[i] = a32(img + (wave >> 2) * 16384, 16 * (i >> 1) + 8 * (lane >> 5) + 4 * (i & 1), wave & 3, lane);
             tr8(o, a);
             _Pragma("unroll") for (int ks = 0; ks < 4; ++ks) w[bt * 4 + ks] = mk4(o[2 * ks], o[2 * ks + 1]); } while (0));
#undef AFTER_W
    TLR(104 + h, 6);
    const __amdgpu_buffer_rsrc_t ro = mkrsrc(p.ws + (wave < 4 ? OFF_QX : OFF_KX), TUNEW(4, 0) ? 0 : 16 * MB);
    stage_x(rx, 1, XSLOT(1), wave, lane);
    const int nearly = TUNEW(12, 10);
    for (int i = 0; i < NSEG; ++i) {
        const int seg = i;
        char* img = XSLOT(i);
        if (i == 0) WAIT_VM(5); else if (i == 1) WAIT_VM(0); else WAIT_VM(7);
        raw_barrier();
        if (tid == 0 && (i == 1 || i > nearly + 1)) flag_set(p, FL_GQ + 2 * h + par, i == 1 ? 1 : i - 1);
        constexpr bool various = true;
        f32x16 acc = {};
        {
            u32x4 xf[2][4];
#pragma unroll
            for (int j = 0; j < 4; ++j) xf[0][j] = lds_ld16(img, xfrag_off(j, lane));
#pragma unroll
            for (int bq = 0; bq < 10; ++bq) {
                if (bq + 1 < 10) {
#pragma unroll
                    for (int j = 0; j < 4; ++j) xf[(bq + 1) & 1][j] = lds_ld16(img, xfrag_off(4 * (bq + 1) + j, lane));
                }
                __builtin_amdgcn_sched_barrier(0);
#pragma unroll
                for (int j = 0; j < 4; ++j) acc = mf32(w[4 * bq + j], xf[bq & 1][j], acc);
                __builtin_amdgcn_sched_barrier(0);
                if (bq == 4 && i >= 2 && i <= nearly) { WAIT_VM(2); __builtin_amdgcn_s_barrier(); if (tid == 0) flag_set(p, FL_GQ + 2 * h + par, i); }
                if ((bq & 1) && various) stage_x1(rx, min(seg + 2, NSEG - 1), XSLOT(i + 2), wave, lane, bq >> 1);
            }
        }
#pragma unroll
        for (int a = 0; a < 4; a += 2) {
            unsigned x0 = pk_bf16(acc[4 * a], acc[4 * a + 1]), x1 = pk_bf16(acc[4 * a + 2], acc[4 * a + 3]);
            unsigned y0 = pk_bf16(acc[4 * a + 4], acc[4 * a + 5]), y1 = pk_bf16(acc[4 * a + 6], acc[4 * a + 7]);
            auto r0 = __builtin_amdgcn_permlane32_swap(x0, y0, false, false);
            auto r1 = __builtin_amdgcn_permlane32_swap(x1, y1, false, false);
            u32x4 v = {r0[0], r1[0], r0[1], r1[1]};
            st16_sc1(v, ro, ((4 * (wave & 3) + a) * 32 + lane) * 16, (seg * H + h) * 8192);
        }
    }
    WAIT_VM(0);
    raw_barrier();
    if (tid == 0) flag_set(p, FL_GQ + 2 * h + par, NSEG);
}

DEV void role_gemm_vu(const Params& p, int h, int par, char* lds) {
    const int tid = threadIdx.x, lane = tid & 63, wave = __builtin_amdgcn_readfirstlane(tid >> 6);
    const int et = wave & 3, kh = wave >> 2, g = lane >> 4;
    u32x4 wv[20], wo[5][4];
    const __amdgpu_buffer_rsrc_t rw0 = mkrsrc(p.wv, WBYTES), rw1 = mkrsrc(p.wo, WBYTES), rx = mkrsrc(p.bx, S * D * 2);
    for (int i = 0; i < par * TUNEW(7, 0); ++i) __builtin_amdgcn_s_sleep(2);
#define XS2(i) (lds + (((i) & 1) ? 0 : 3 * XT_BYTES))
#define AFTER_W stage_x(rx, par, XS2(0), wave, lane)
    STAGE_RING(3, 5,
        do { stage_img<32>(rw0, bt * 32 * H * DH + h * DH, H * DH, img, wave, lane); stage_img<32>(rw0, (320 + bt * 32) * H * DH + h * DH, H * DH, img + 8192, wave, lane);
             stage_img<64>(rw1, (h * DH + (bt & 1) * 64) * D + (bt >> 1) * 128, D, img + 16384, wave, lane); } while (0),
        do { unsigned a[8]; u32x2 o[8];
             _Pragma("unroll") for (int i = 0; i < 4; ++i) { a[i] = a32(img + kh * 8192, 16 * (i >> 1) + 8 * (lane >> 5) + 4 * (i & 1), et, lane);
                 a[4 + i] = a16(img + 16384, 32 * (i >> 1) + 16 * (g >> 1) + 4 * (g & 1) + 8 * (i & 1), wave, lane); }
             tr8(o, a);
             _Pragma("unroll") for (int ks = 0; ks < 2; ++ks) { wv[bt * 2 + ks] = mk4(o[2 * ks], o[2 * ks + 1]); wo[bt >> 1][2 * (bt & 1) + ks] = mk4(o[4 + 2 * ks], o[5 + 2 * ks]); } } while (0));
#undef AFTER_W
    TLR(168 + 2 * h + par, 6);
    char* xch = lds + XT_BYTES;
    char* vimg = xch + 32768;
    const __amdgpu_buffer_rsrc_t ru = mkrsrc(p.ws + OFF_U, TUNEW(4, 0) ? 0 : 80 * MB);
    f32x4 k0, k1;
    const int nit = par ? NSPLIT / 2 : NSEG - NSPLIT / 2;
#define VU_P1(i, MID) do { \
        const char* img = XS2(i) + kh * 20480; \
        f32x16 acc = {}; \
        u32x4 xf[2][4]; \
        _Pragma("unroll") for (int j = 0; j < 4; ++j) xf[0][j] = lds_ld16(img, xfrag_off(j, lane)); \
        _Pragma("unroll") for (int bq = 0; bq < 5; ++bq) { \
            if (bq + 1 < 5) { _Pragma("unroll") for (int j = 0; j < 4; ++j) xf[(bq + 1) & 1][j] = lds_ld16(img, xfrag_off(4 * (bq + 1) + j, lane)); } \
            __builtin_amdgcn_sched_barrier(0); \
            _Pragma("unroll") for (int j = 0; j < 4; ++j) acc = mf32(wv[4 * bq + j], xf[bq & 1][j], acc); \
            __builtin_amdgcn_sched_barrier(0); \
            MID; \
        } \
        const f32x4 lo0 = {acc[0], acc[1], acc[2], acc[3]}, lo1 = {acc[4], acc[5], acc[6], acc[7]}, hi0 = {acc[8], acc[9], acc[10], acc[11]}, hi1 = {acc[12], acc[13], acc[14], acc[15]}; \
        char* xo = xch + ((i) & 1) * 16384 + wave * 2048 + lane * 16; \
        *(f32x4*)xo = KH ? lo0 : hi0; *(f32x4*)(xo + 1024) = KH ? lo1 : hi1; \
        k0 = KH ? hi0 : lo0; k1 = KH ? hi1 : lo1; \
    } while (0)
#define VU_FIN(i) do { \
        const char* xi = xch + ((i) & 1) * 16384 + (wave ^ 4) * 2048 + lane * 16; \
        const f32x4 a = k0 + *(const f32x4*)xi, b = k1 + *(const f32x4*)(xi + 1024); \
        lds_st16(vimg + ((i) & 1) * 8192, ((et * 4 + 2 * kh + (lane >> 5)) * 32 + (lane & 31)) * 16, u32x4{pk_bf16(a[0], a[1]), pk_bf16(a[2], a[3]), pk_bf16(b[0], b[1]), pk_bf16(b[2], b[3])}); \
    } while (0)
#define VU_P2(i, MID) do { \
        u32x4 vf[2][4]; \
        unsigned vb_ = XT_BYTES + 32768 + ((i) & 1) * 8192 + (g * 32 + (lane & 15)) * 16; asm volatile("" : "+v"(vb_)); \
        _Pragma("unroll") for (int tt = 0; tt < 2; ++tt) _Pragma("unroll") for (int ks = 0; ks < 4; ++ks) vf[tt][ks] = lds_ld16(lds + vb_, ks * 2048 + tt * 256); \
        f32x4 d0[2], d1[2]; \
        _Pragma("unroll") for (int bq = 0; bq <= 5; ++bq) { \
            if (bq < 5) { \
                d0[bq & 1] = zero4(); d1[bq & 1] = zero4(); \
                _Pragma("unroll") for (int ks = 0; ks < 4; ++ks) { d0[bq & 1] = mf16(vf[0][ks], wo[bq < 5 ? bq : 0][ks], d0[bq & 1]); d1[bq & 1] = mf16(vf[1][ks], wo[bq < 5 ? bq : 0][ks], d1[bq & 1]); } \
            } \
            __builtin_amdgcn_sched_barrier(0); \
            if (bq > 0) { const f32x4 e0 = d0[(bq - 1) & 1], e1 = d1[(bq - 1) & 1]; \
                st16_sc1(u32x4{pk_bf16(e0[0], e0[1]), pk_bf16(e0[2], e0[3]), pk_bf16(e1[0], e1[1]), pk_bf16(e1[2], e1[3])}, ru, lane * 16, (vu_seg(par, i) * NCT * H + h) * 1024 + (8 * (bq - 1) + wave) * 32768); } \
            if (bq < 5) { MID; } \
            __builtin_amdgcn_sched_barrier(0); \
        } \
    } while (0)
#define VU_XP stage_x1(rx, vu_seg(par, i + 1), XS2(i + 1), wave, lane, bq)
#define VU_LOOP() do { \
          \
        stage_x(rx, par + 2, XS2(1), wave, lane); \
        WAIT_VM(5); raw_barrier(); \
        VU_P1(0, (void)0); raw_barrier(); \
        stage_x(rx, par + 4, XS2(0), wave, lane); \
        VU_FIN(0); raw_barrier(); \
        VU_P2(0, (void)0); \
        WAIT_VM(10);   raw_barrier(); \
        VU_P1(1, (void)0); \
        WAIT_VM(0); raw_barrier(); \
        if (tid == 0) flag_set(p, FL_GU + 2 * h + par, 1); \
        { const int i = 2; VU_FIN(1); VU_P1(2, VU_XP); WAIT_VM(0); raw_barrier(); } \
          \
        for (int i = 3; i < nit; ++i) { \
            if (KH) { VU_FIN(i - 1); VU_P2(i - 2, VU_XP); VU_P1(i, (void)0); if (i <= 4) WAIT_VM(0); else WAIT_VM(1); } \
            else { VU_P1(i, do { VU_XP; if (bq == 1) VU_FIN(i - 1); } while (0)); VU_P2(i - 2, (void)0); if (i <= 4) WAIT_VM(0); else WAIT_VM(5); } \
            raw_barrier(); \
            if (tid == 0 && i != 5) flag_set(p, FL_GU + 2 * h + par, i <= 4 ? i - 1 : i - 2); \
        } \
        VU_FIN(nit - 1); \
        for (int i = nit - 2; i < nit; ++i) { VU_P2(i, (void)0); raw_barrier(); } \
    } while (0)
    if (kh) { constexpr bool KH = true; VU_LOOP(); } else { constexpr bool KH = false; VU_LOOP(); }
    WAIT_VM(0);
    raw_barrier();
    if (tid == 0) flag_set(p, FL_GU + 2 * h + par, nit);
}

DEV unsigned now_rt() { return (unsigned)__builtin_amdgcn_s_memrealtime(); }
DEV unsigned clamp_rt(unsigned t) { const unsigned n = now_rt(); const int d = (int)(t - n); return (d > 400 || d < 0) ? n : t; }
DEV void wait_rt(unsigned t) {
    int d;
    while ((d = (int)(t - now_rt())) > 3) {}
    if (d > 0) asm volatile("s_nop 3");
    if (d > 1) asm volatile("s_nop 3");
    if (d > 2) asm volatile("s_nop 3");
    asm volatile("" ::: "memory");
}
#define PIN_S(x) do { x = __builtin_amdgcn_readfirstlane(x); asm volatile("" : "+s"(x)); } while (0)
#define SB() __builtin_amdgcn_sched_barrier(0)
DEV unsigned mx5(unsigned m, u32x4 v) { return max(max(max(m, v.x), v.y), max(v.z, v.w)); }
DEV unsigned read_ann(__amdgpu_buffer_rsrc_t r, unsigned soff, int cap) {
    unsigned v = 0;
    for (int sp = 0; sp <= cap; ++sp) {
        asm volatile("" ::: "memory");
        v = __builtin_amdgcn_readfirstlane(__builtin_amdgcn_raw_buffer_load_b32(r, 0, soff, 16));
        if (v != SENT) break;
    }
    return v;
}
DEV float wave_max(float x) {
    x = fmaxf(x, u2f(__builtin_amdgcn_update_dpp(0, f2u(x), 0x128, 0xf, 0xf, true)));
    x = fmaxf(x, u2f(__builtin_amdgcn_update_dpp(0, f2u(x), 0x124, 0xf, 0xf, true)));
    x = fmaxf(x, u2f(__builtin_amdgcn_update_dpp(0, f2u(x), 0x122, 0xf, 0xf, true)));
    x = fmaxf(x, u2f(__builtin_amdgcn_update_dpp(0, f2u(x), 0x121, 0xf, 0xf, true)));
    return xmax16_32(x);
}
DEV void dma_mem(__amdgpu_buffer_rsrc_t rc0, char* mimg, int lane, unsigned oa, unsigned ob) {
    const unsigned oa2 = oa + 4096;
    __builtin_amdgcn_raw_ptr_buffer_load_lds(rc0, (LAS void*)mimg, 16, lane * 16, oa, 0, 16);
    __builtin_amdgcn_raw_ptr_buffer_load_lds(rc0, (LAS void*)mimg, 16, lane * 16, oa, 1024, 16);
    __builtin_amdgcn_raw_ptr_buffer_load_lds(rc0, (LAS void*)mimg, 16, lane * 16, oa, 2048, 16);
    __builtin_amdgcn_raw_ptr_buffer_load_lds(rc0, (LAS void*)mimg, 16, lane * 16, oa, 3072, 16);
    __builtin_amdgcn_raw_ptr_buffer_load_lds(rc0, (LAS void*)(mimg + 4096), 16, lane * 16, oa2, 0, 16);
    __builtin_amdgcn_raw_ptr_buffer_load_lds(rc0, (LAS void*)(mimg + 4096), 16, lane * 16, oa2, 1024, 16);
    __builtin_amdgcn_raw_ptr_buffer_load_lds(rc0, (LAS void*)(mimg + 4096), 16, lane * 16, oa2, 2048, 16);
    __builtin_amdgcn_raw_ptr_buffer_load_lds(rc0, (LAS void*)(mimg + 4096), 16, lane * 16, oa2, 3072, 16);
    __builtin_amdgcn_raw_ptr_buffer_load_lds(rc0, (LAS void*)(mimg + 8192), 16, lane * 16, ob, 0, 16);
    __builtin_amdgcn_raw_ptr_buffer_load_lds(rc0, (LAS void*)(mimg + 8192), 16, lane * 16, ob, 1024, 16);
}
DEV void fill_mimg(char* mimg, int lane) {
    WAIT_VM(0);
#pragma unroll
    for (int i = 0; i < 10; ++i) lds_st16(mimg, i * 1024 + lane * 16, u32x4{SENT, SENT, SENT, SENT});
    __builtin_amdgcn_s_waitcnt(0xC07F);
}
DEV bf16x16 cat8(u32x4 a, u32x4 b) { u32x8 r = {a.x, a.y, a.z, a.w, b.x, b.y, b.z, b.w}; return __builtin_bit_cast(bf16x16, r); }
#define RDF(i) af[i] = lds_ld16(mimg, (i) * 1024 + lane * 16)
DEV bool any_nan(float a, float b) { return __builtin_amdgcn_fcmpf(a, b, 8  ) != 0; }
#define RD_PROBE() lds_vol_ld<unsigned>(mimg + (lane < NCT ? lane : 0) * 256 + 128 + (lane & 31) * 4)
#define RDV(i) af[i] = lds_vol_ld<u32x4>(mimg + (i) * 1024 + lane * 16)
#define RD_ALL() do { RDV(0); RDV(1); RDV(2); RDV(3); RDV(4); RDV(5); RDV(6); RDV(7); RDV(8); RDV(9); } while (0)
#define PROJ_READ(WAITN, TICK) do { \
        WS(0); \
        const unsigned pr0_ = RD_PROBE(); \
        RD_ALL(); \
        if (!__all(pr0_ != SENT)) { \
            for (int sp_ = 0; sp_ <= cap; ++sp_) { \
                TICK; \
                if (__all(RD_PROBE() != SENT)) break; \
            } \
            RD_ALL(); \
        } \
        if (wave == 0 && ISQK) MT(tsp); \
        WS(1); \
    } while (0)
#define PROJ_MM(DO) do { DO(0); DO(1); DO(2); DO(3); DO(4); DO(5); DO(6); DO(7); DO(8); DO(9); SB(); } while (0)
#define WAIT_VALID(TICK) do { \
        for (int sp_ = 0;; ++sp_) { \
            asm volatile("" ::: "memory"); \
            unsigned m_ = 0; \
            _Pragma("unroll") for (int st = 0; st < 10; ++st) m_ = mx5(m_, lds_ld16(mimg, st * 1024 + lane * 16)); \
            if (__all(m_ != SENT)) break; \
            if (sp_ > cap) { flag_set(p, FL_TMO, 1u); cap = 0; break; } \
            TICK; \
        } } while (0)
#define NOWAIT(n) (void)0
#define POLL_TICK do { if (tick == tnext) { tnext += 9; if (tick > 36) WAIT_VM(0); dma_mem(rc0, mimg, lane, oa, ob); } ++tick; } while (0)

constexpr float SCALE = 0.08838834764831845f;
constexpr float LOG2E = 1.4426950408889634f;
constexpr float MASKV = -999292928.0f;
DEV void round_scores(f32x4& d) {
    unsigned a = pk_bf16(d[0], d[1]), b = pk_bf16(d[2], d[3]);
    a = pk_bf16(bf_lo(a) * SCALE, bf_hi(a) * SCALE); b = pk_bf16(bf_lo(b) * SCALE, bf_hi(b) * SCALE);
    d = f32x4{bf_lo(a), bf_hi(a), bf_lo(b), bf_hi(b)};
}
DEV float fmx(float a, float b) { float r; asm("v_max_f32 %0, %1, %2" : "=v"(r) : "v"(a), "v"(b)); return r; }
DEV float max4(f32x4 d) { return fmaxf(fmaxf(d[0], d[1]), fmaxf(d[2], d[3])); }
DEV void exp4(f32x4& d, float mx) {
#pragma unroll
    for (int i = 0; i < 4; ++i) d[i] = __builtin_amdgcn_exp2f((d[i] - mx) * LOG2E);
}
DEV float sum4(f32x4 d) { return (d[0] + d[1]) + (d[2] + d[3]); }
DEV unsigned pk_pair(f32x4 d) { return pk_bf16(d[0] + d[2], d[1] + d[3]); }

DEV void role_qk(const Params& p, int h, char* lds) {
    constexpr bool ISQK = true; (void)ISQK;
    const int tid = threadIdx.x, lane = tid & 63, wave = __builtin_amdgcn_readfirstlane(tid >> 6);
    const int g = lane >> 4, l16 = lane & 15;
    unsigned* ctrl = (unsigned*)(p.ws + OFF_CTRL) + TUNE_W; (void)ctrl;
    bf16x16 wq[10], wk[10];
    const __amdgpu_buffer_rsrc_t rw0 = mkrsrc(p.wq, WBYTES), rw1 = mkrsrc(p.wk, WBYTES);
    for (int i = 0; i < TUNEW(8, 0); ++i) __builtin_amdgcn_s_sleep(2);
#define AFTER_W (void)0
    STAGE_RING(5, 0,
        do { stage_img<64>(rw0, bt * 64 * H * DH + h * DH, H * DH, img, wave, lane); stage_img<64>(rw1, bt * 64 * H * DH + h * DH, H * DH, img + 16384, wave, lane); } while (0),
        load_bsp2<true>(wq[bt], wk[bt], img, wave, img + 16384, wave, lane));
    TLR(h, 2);
    int cap = SPIN_CAP;
    const unsigned side0 = (flags_wait(p, FL_SIDE, 0, 1, 1u, lane, cap) == 2u ? 1u : 0u) << 13;
    flags_wait(p, FL_READY, 0, 1, NCT, lane, cap);
    const __amdgpu_buffer_rsrc_t rc0 = mkrsrc(p.ws + OFF_COMM0 + __builtin_amdgcn_readfirstlane(side0), 2 * MB - 8192);
    const __amdgpu_buffer_rsrc_t rq = mkrsrc(p.ws + OFF_QX, 32 * MB);
    const __amdgpu_buffer_rsrc_t rpx = mkrsrc(p.ws + OFF_PX, 5 * MB);
    char* mimg = lds;
    char* ximg = lds + 40960;
    char* misc = lds + 46080;
    unsigned* hist = (unsigned*)(misc + 32);
    char* qk = lds + 49152;
    const int idx = (lane & 2) ? 0xEEEE : 0x4444;
    unsigned long long tsp = 0; (void)tsp;
    WSDECL;
    {
        TLR(h, 3);
        flags_wait(p, FL_GQ + 2 * h, 0, 1, 1u, lane, cap);
        TLR(h, 4);
        u32x4 a = ld16_sc1(rq, tid * 16, h * 8192), b = ld16_sc1(rq, tid * 16, 16 * MB + h * 8192);
        lds_st16(qk, tid * 16, a); lds_st16(qk, 8192 + tid * 16, b);
        if (tid == 0) { lds_vol_st<int>(misc, TUNEW(15, 92)); lds_vol_st<int>(misc + 4, 64); lds_vol_st<int>(misc + 8, 0); lds_vol_st<int>(misc + 12, 0); }
    }
    raw_barrier();
    u32x4 af[10];
    f32x4 aq, ak;
    char* xw = ximg + (16 * wave + l16) * 32 + g * 4;
#define Q_DO(st) aq = smf(af[st], wq[st], aq, idx)
#define K_DO(st) ak = smf(af[st], wk[st], ak, idx)
#define QK_DO(st) do { Q_DO(st); K_DO(st); } while (0)
#define QK_ALL() do { QK_DO(4); QK_DO(5); QK_DO(0); QK_DO(1); QK_DO(2); QK_DO(3); QK_DO(6); QK_DO(7); QK_DO(8); QK_DO(9); } while (0)
#define QK_PROJ(WAITN, TICK, HOOK, HOOK2) do { \
        float q0_, q1_; \
        for (int pass_ = 0;; ++pass_) { \
            aq = zero4(); ak = zero4(); asm volatile("" : "+v"(aq), "+v"(ak)); \
            PROJ_READ(WAITN, TICK); \
            HOOK; \
            PROJ_MM(QK_DO); \
            HOOK2; \
            q0_ = aq[0] + aq[2]; q1_ = aq[1] + aq[3]; \
            if (!any_nan(q0_, q1_)) break; \
            asm volatile(""); \
            if (pass_) break; \
            WAIT_VALID(TICK); \
        } \
        *(unsigned*)xw = pk_bf16(q0_, q1_); *(unsigned*)(xw + 16) = pk_pair(ak); \
        WS(4); } while (0)
#define PF_DECL const unsigned o = (tid - 64) * 16, so = (min(s + 1, NSEG - 1) * H + h) * 8192; u32x4 a0, a1, b0, b1; const bool early = __all(gv >= (unsigned)min(s + 2, NSEG))
#define QK_PF do { a0 = ld16_sc1(rq, o, so); a1 = ld16_sc1(rq, o + 4096, so); b0 = ld16_sc1(rq, o, 16 * MB + so); b1 = ld16_sc1(rq, o + 4096, 16 * MB + so); gv = flag_get(p, FL_GQ + 2 * h); SB(); } while (0)
#define PF_STORE do { if (!early) { flags_wait(p, FL_GQ + 2 * h, 0, 1, min(s + 2, NSEG), lane, cap); QK_PF; } \
            char* nq = qk + ((s + 1) & 1) * 16384; lds_st16(nq, o, a0); lds_st16(nq, o + 4096, a1); lds_st16(nq, 8192 + o, b0); lds_st16(nq, 8192 + 4096 + o, b1); } while (0)
    if (wave == 0) {
        const unsigned gq = l16 >> 2, r = l16 & 3, key0 = 16 * (r >> 1) + 8 * (gq >> 1) + 2 * (gq & 1) + (r & 1);
        const unsigned koff = 8192 + (g * 32 + key0) * 16;
        const bool hi8 = l16 >= 8;
        const unsigned px_off = ((g >> 1) * 16 + rowi(l16 & 7, g & 1)) * 16 + (l16 >> 3) * 8;
        const unsigned pm_off = rowi(l16 & 7, g & 1) * 16 + (h & 1) * 8 + (g >> 1) * 4 + (l16 >> 3) * 4096;
        const unsigned foff = (8 * g + (l16 >> 2)) * 32 + (lane & 1) * 8;
        unsigned long long t1 = 0, t2 = 0, t3 = 0, t4 = 0; unsigned a12 = 0, a23 = 0, a34 = 0, a01 = 0; (void)t1; (void)t2; (void)t3; (void)t4; (void)a12; (void)a23; (void)a34; (void)a01;
        for (int s = 0; s < NSEG; ++s) {
            const char* kimg = qk + (s & 1) * 16384 + koff;
            unsigned opx = physo(px_base(s & 3, 0) + h * 512), opx1 = opx + 32768, opm = physo(pm_base(s & 3, 0) + (h >> 1) * 256);
            PIN_S(opx); PIN_S(opx1); PIN_S(opm);
            raw_barrier();
            MT(t1);
            u32x4 Fq[4], Fk[4], kf[8];
#define RD_KF do { _Pragma("unroll") for (int ks = 0; ks < 4; ++ks) { kf[2 * ks] = lds_ld16(kimg, ks * 2048); kf[2 * ks + 1] = lds_ld16(kimg, ks * 2048 + 64); } SB(); } while (0)
            QK_PROJ(NOWAIT, (void)0, (void)0, RD_KF);
            MT(t2);
            raw_barrier();
            MT(t3); WS(5);
            if (s + 1 < NSEG) {
#pragma unroll
                for (int ks = 0; ks < 4; ++ks) { u32x2 a = tr_read(ximg + foff, ks * 1024), b = tr_read(ximg + foff, ks * 1024 + 128); Fq[ks] = u32x4{a.x, a.y, b.x, b.y}; }
#pragma unroll
                for (int ks = 0; ks < 4; ++ks) { u32x2 c = tr_read(ximg + foff, ks * 1024 + 16), d = tr_read(ximg + foff, ks * 1024 + 128 + 16); Fk[ks] = u32x4{c.x, c.y, d.x, d.y}; }
                SB();
                f32x4 d0 = mf16(kf[0], Fq[0], zero4()), d1 = mf16(kf[1], Fq[0], zero4());
                d0 = mf16(kf[2], Fq[1], d0); d1 = mf16(kf[3], Fq[1], d1);
                d0 = mf16(kf[4], Fq[2], d0); d1 = mf16(kf[5], Fq[2], d1); d0 = mf16(kf[6], Fq[3], d0); d1 = mf16(kf[7], Fq[3], d1);
                SB();
                f32x4 dm = mf16(Fk[0], Fq[0], zero4());
                dm = mf16(Fk[1], Fq[1], dm); dm = mf16(Fk[2], Fq[2], dm); dm = mf16(Fk[3], Fq[3], dm);
                SB();
                constexpr float CS = SCALE * LOG2E;
                float x0 = (hi8 ? d1[0] : d0[0]) * CS, x1 = (hi8 ? d1[1] : d0[1]) * CS, x2 = (hi8 ? d1[2] : d0[2]) * CS, x3 = (hi8 ? d1[3] : d0[3]) * CS;
                float mx = max3f(max3f(x0, x1, x2), x3, x3);
                const float mxr = u2f(__builtin_amdgcn_update_dpp(0, f2u(mx), 0x128, 0xf, 0xf, true));
                asm volatile("" : "+v"(x0), "+v"(x1), "+v"(x2), "+v"(x3));
                SB();
                float y0 = (g >= 2 ? dm[2] : dm[0]) * CS, y1 = (g >= 2 ? dm[3] : dm[1]) * CS;
                mx = max3f(mx, mxr, fmaxf(y0, y1));
                asm volatile("" : "+v"(y0), "+v"(y1));
                { auto r = __builtin_amdgcn_permlane16_swap(f2u(mx), f2u(mx), false, false); mx = fmx(u2f(r[0]), u2f(r[1])); }
                { auto r = __builtin_amdgcn_permlane32_swap(f2u(mx), f2u(mx), false, false); mx = fmx(u2f(r[0]), u2f(r[1])); }
                x0 = __builtin_amdgcn_exp2f(x0 - mx); x1 = __builtin_amdgcn_exp2f(x1 - mx); x2 = __builtin_amdgcn_exp2f(x2 - mx); x3 = __builtin_amdgcn_exp2f(x3 - mx);
                y0 = __builtin_amdgcn_exp2f(y0 - mx); y1 = __builtin_amdgcn_exp2f(y1 - mx);
                float sm = (x0 + x1) + (x2 + x3);
                sm = sm + u2f(__builtin_amdgcn_update_dpp(0, f2u(sm), 0x128, 0xf, 0xf, true)) + (y0 + y1);
                const float inv = __builtin_amdgcn_rcpf(xsum16_32(sm));
                const u32x2 pxv = {nosent(pk_bf16(x0 * inv, x1 * inv)), nosent(pk_bf16(x2 * inv, x3 * inv))};
                st8_sc1(pxv, rc0, px_off, opx);
                st4_sc1(nosent(pk_bf16(y0 * inv, y1 * inv)), rc0, pm_off, opm);
                st8_sc1(pxv, rc0, px_off, opx1);
                MT(t4); WS(7); WSDUMP();
                { const unsigned ta = now_rt(); if (lane == 0) st4_sc1(nosent(ta), rc0, s * 4, physo(STA_BASE + h * 256)); }
                MTW(); PACC(a12, t1, t2); PACC(a23, t2, t3); PACC(a34, t3, t4); PACC(a01, t1, tsp); PDUMP(h, a12, a23, a34, a01);
            }
        }
    } else if (wave <= 2) {
        const int tt = wave - 1;
        const int tok = 16 * tt + l16;
        unsigned gv = 0;
        for (int s = 0; s < NSEG; ++s) {
            PF_DECL;
            raw_barrier();
            QK_PROJ(NOWAIT, (void)0, QK_PF, (void)0);
            raw_barrier();
            WS(7); WSDUMP();
            __builtin_amdgcn_s_sleep(2);
            WAIT_VM(5);
            if (lane == 0 && s > 0 && s <= NSEG - 4) flag_set(p, FL_PX + 2 * h + tt, s);
            const char* kimg = qk + (s & 1) * 16384 + 8192 + (g * 32 + l16) * 16;
            const char* qimg = qk + (s & 1) * 16384 + (g * 32 + tok) * 16;
            f32x4 d0 = {}, d1 = {}, dm = {};
#pragma unroll
            for (int ks = 0; ks < 4; ++ks) {
                const unsigned e0 = 32 * ks + 8 * g + (l16 >> 2);
                u32x2 a = tr_read(ximg, e0 * 32 + (lane & 3) * 8), b = tr_read(ximg, (e0 + 4) * 32 + (lane & 3) * 8);
                const u32x4 qf = lds_ld16(qimg, ks * 2048);
                d0 = mf16(lds_ld16(kimg, ks * 2048), qf, d0);
                d1 = mf16(lds_ld16(kimg, ks * 2048 + 256), qf, d1);
                dm = mf16(u32x4{a.x, a.y, b.x, b.y}, qf, dm);
            }
            round_scores(d0); round_scores(d1); round_scores(dm);
#pragma unroll
            for (int r = 0; r < 4; ++r) { if (4 * g + r > tok) d0[r] = MASKV; if (16 + 4 * g + r > tok) d1[r] = MASKV; }
            if (g < 2) dm = f32x4{-3.0e38f, -3.0e38f, -3.0e38f, -3.0e38f};
            const float mx = xmax16_32(fmaxf(fmaxf(max4(d0), max4(d1)), max4(dm)));
            exp4(d0, mx); exp4(d1, mx); exp4(dm, mx);
            const float inv = 1.0f / xsum16_32(sum4(d0) + sum4(d1) + sum4(dm));
            u32x4 v = {pk_bf16(d0[0] * inv, d0[1] * inv), pk_bf16(d0[2] * inv, d0[3] * inv), pk_bf16(d1[0] * inv, d1[1] * inv), pk_bf16(d1[2] * inv, d1[3] * inv)};
            st16_sc1(v, rpx, lane * 16, ((s * H + h) * 2 + tt) * 1024);
            if (g >= 2) {
                st4_sc1(pk_bf16(dm[0] * inv, dm[1] * inv), rpx, tok * 16 + (g - 2) * 4, 4 * MB + (s * H + h) * 512);
                st4_sc1(pk_bf16(dm[2] * inv, dm[3] * inv), rpx, tok * 16 + 8 + (g - 2) * 4, 4 * MB + (s * H + h) * 512);
            }
            PF_STORE;
            if (s >= NSEG - 4) { WAIT_VM(0); if (lane == 0) flag_set(p, FL_PX + 2 * h + tt, s + 1); }
        }
    } else if (wave <= 4) {
        unsigned gv = 0;
        const float mg1 = 0.25f * (float)TUNEW(0, 40), mg2 = 0.25f * (float)TUNEW(1, 24);
        float d1e = (float)TUNEW(15, 92) - 10.0f, d2e = 58.0f;
        for (int s = 0; s < NSEG; ++s) {
            PF_DECL;
            raw_barrier();
            QK_PROJ(NOWAIT, (void)0, QK_PF, (void)0);
            raw_barrier();
            WS(7); WSDUMP();
            __builtin_amdgcn_s_sleep(6);
            WAIT_VM(0);
            PF_STORE;
            if (h == 0 && wave == 4 && s >= 2) {
                const unsigned tb = hist[2 * (s - 1)], tx = hist[2 * (s - 1) + 1];
                const unsigned va = __builtin_amdgcn_raw_buffer_load_b32(rc0, (lane & 31) * 256, physo(STA_BASE) + (s - 1) * 4, 16);
                const unsigned vb = __builtin_amdgcn_raw_buffer_load_b32(rc0, physo((lane < NCT ? lane : 0) * 256), physo(STB_BASE) + (s - 1) * 4, 16);
                if (__all(va != SENT && vb != SENT)) {
                    const float a = wave_max((float)(int)(va - tb)), b = wave_max((float)(int)(vb - tx));
                    if (tb != SENT) d1e += 0.5f * (fminf(a, d1e + 2.0f) - d1e);
                    d2e += 0.5f * (fminf(b, d2e + 2.0f) - d2e);
                    if (lane == 0) { lds_vol_st<int>(misc, (int)(d1e + mg1)); lds_vol_st<int>(misc + 4, (int)(d2e + mg2)); }
                }
            }
            if (wave == 3 && s >= 1) {
                st16_sc1(u32x4{SENT, SENT, SENT, SENT}, rc0, (lane & 31) * 16 + (lane >> 5) * 32768, physo(px_base((s - 1) & 3, 0) + h * 512));
                if (lane < 32) st8_sc1(u32x2{SENT, SENT}, rc0, l16 * 16 + (h & 1) * 8 + (g & 1) * 4096, physo(pm_base((s - 1) & 3, 0) + (h >> 1) * 256));
            }
        }
    } else if (wave < 7) {
        const int tfirst = wave == 6 ? TUNEW(5, 0) : TUNEW(5, 0) + 3;
        for (int s = 0; s < NSEG; ++s) {
            unsigned oa = physo(mema_off(s & 3, 0)), ob = physo(memb_off(s & 3, 0));
            PIN_S(oa); PIN_S(ob);
            int tick = 0, tnext = tfirst;
            raw_barrier();
            QK_PROJ(NOWAIT, POLL_TICK, (void)0, (void)0);
            raw_barrier();
            WS(7); WSDUMP();
            WAIT_VM(0);
            if (lane == 0) lds_vol_st<int>(misc + 8 + (wave - 5) * 4, s + 1);
        }
    } else {
        int nfail = 0;
        const int extra = TUNEW(2, 14);
        const int tfirst = TUNEW(5, 0) + 6, gap2 = TUNEW(6, 3);
        for (int s = 0; s < NSEG; ++s) {
            const unsigned tm0 = s > 0 ? read_ann(rc0, physo(ANN_BASE) + (s - 1) * 128 + 4, cap) : 0u;
            while (lds_vol_ld<int>(misc + 8) < s || lds_vol_ld<int>(misc + 12) < s) {}
            fill_mimg(mimg, lane);
            unsigned oa = physo(mema_off(s & 3, 0)), ob = physo(memb_off(s & 3, 0)), oann = physo(ANN_BASE + s * 128);
            PIN_S(oa); PIN_S(ob); PIN_S(oann);
            const unsigned tm = s > 0 ? clamp_rt(tm0) : now_rt();
            const int dd1 = __builtin_amdgcn_readfirstlane(lds_vol_ld<int>(misc)), dd2 = __builtin_amdgcn_readfirstlane(lds_vol_ld<int>(misc + 4));
            int tick = 0, tnext = tfirst;
            wait_rt(tm);
            dma_mem(rc0, mimg, lane, oa, ob);
            const unsigned c0 = (unsigned)__builtin_amdgcn_s_memtime();
            if (gap2 >= 0) {
                for (int i = 0; i < gap2; ++i) asm volatile("s_nop 7");
                dma_mem(rc0, mimg, lane, oa, ob);
                WAIT_VM(12);
            } else WAIT_VM(2);
            raw_barrier();
#define QK_ANN do { if (h == 0) { \
                const unsigned te = tm + ((((unsigned)__builtin_amdgcn_s_memtime() - c0) * 2731u) >> 16), x1 = nosent(te + dd1 + (tick ? extra : 0)), x2 = nosent(x1 + dd2); \
                if (lane == 0) { st8_sc1(u32x2{x1, x2}, rc0, 0, oann); hist[2 * s] = (pass_ || tick) ? SENT : te; hist[2 * s + 1] = x1; } } } while (0)
            QK_PROJ(NOWAIT, POLL_TICK, QK_ANN, (void)0);
            raw_barrier();
            WS(7); WSDUMP();
            PDUMP(32 + h, c1 - c0, c2 - c1, (c0 - cpre) - (tm - rpre) * 24, tm - rpre);
            if (s >= 4) nfail += tick;
        }
    }
}

DEV void role_vo(const Params& p, int h, char* lds) {
    constexpr bool ISQK = false; (void)ISQK;
    const int tid = threadIdx.x, lane = tid & 63, wave = __builtin_amdgcn_readfirstlane(tid >> 6);
    const int g = lane >> 4, l16 = lane & 15, j4 = wave & 3;
    bf16x16 w[20];
    const __amdgpu_buffer_rsrc_t rw0 = mkrsrc(p.wv, WBYTES), rw1 = mkrsrc(p.wo, WBYTES);
    for (int i = 0; i < TUNEW(8, 0); ++i) __builtin_amdgcn_s_sleep(2);
    STAGE_RING(5, 0,
        do { stage_img<64>(rw0, bt * 64 * H * DH + h * DH, H * DH, img, wave, lane); stage_img<64>(rw1, (h * DH + (bt & 1) * 64) * D + (bt >> 1) * 128, D, img + 16384, wave, lane); } while (0),
        do { if (wave < 4) load_bsp2<true>(w[4 * (bt >> 1) + (bt & 1)], w[4 * (bt >> 1) + 2 + (bt & 1)], img, 2 * j4, img, 2 * j4 + 1, lane);
             else load_bsp2<false>(w[4 * (bt >> 1) + (bt & 1)], w[4 * (bt >> 1) + 2 + (bt & 1)], img + 16384, 2 * j4, img + 16384, 2 * j4 + 1, lane); } while (0));
#undef AFTER_W
    TLR(32 + h, 2);
    int cap = SPIN_CAP;
    const unsigned long long sides = wait_sides(p, lane, cap, 3);
    unsigned long long tmask = sides >> 1;
    flags_wait(p, FL_READY, 0, 1, NCT, lane, cap);
    const __amdgpu_buffer_rsrc_t rc0 = mkrsrc(p.ws + OFF_COMM0 + (((unsigned)sides & 1u) << 13), 2 * MB - 8192);
    const __amdgpu_buffer_rsrc_t rt = mkrsrc(p.ws + OFF_T, 44 * MB);
    char* mimg = lds;
    char* vimg = lds + 40960;
    const int idx = (lane & 2) ? 0xEEEE : 0x4444;
    unsigned long long tsp = 0; (void)tsp;
    WSDECL;
    if (wave < 4) {
        u32x4 af[10];
        f32x4 a0, a1;
        const unsigned e_ = 32 * j4 + l16;
        char* vw = vimg + (2 * (e_ >> 2) + (e_ & 1)) * 32 + (4 * g + 2 * ((e_ >> 1) & 1)) * 2;
#define VW(st, tl) w[4 * ((st) >> 1) + 2 * (tl) + ((st) & 1)]
#define VO_DO(st) do { a0 = smf(af[st], VW(st, 0), a0, idx); a1 = smf(af[st], VW(st, 1), a1, idx); } while (0)
        for (int s = 0; s < NSEG; ++s) {
            raw_barrier();
            float v0_, v1_;
            for (int pass_ = 0;; ++pass_) {
                a0 = zero4(); a1 = zero4(); asm volatile("" : "+v"(a0), "+v"(a1));
                PROJ_READ(NOWAIT, (void)0);
                PROJ_MM(VO_DO);
                v0_ = a0[0] + a0[2]; v1_ = a0[1] + a0[3];
                if (!any_nan(v0_, v1_)) break;
                asm volatile("");
                if (pass_) break;
                WAIT_VALID((void)0);
            }
            *(unsigned*)vw = pk_bf16(v0_, v1_); *(unsigned*)(vw + 256) = pk_pair(a1);
            WS(4);
            raw_barrier();
            WS(5); WSDUMP();
        }
    } else {
        const int gap2 = TUNEW(6, 3);
        const unsigned vr_off = (8 * g + (l16 >> 2)) * 32 + (lane & 3) * 8;
        const unsigned t_voff = (8 * (g >> 1) + 2 * j4 + (g & 1)) * 16384 + l16 * 16, t_voff2 = g < 2 ? (32 + 2 * j4 + g) * 16384 + l16 * 16 : 0x7FFFFFF0u;
        for (int s = 0; s < NSEG; ++s) {
            if (wave == 7) fill_mimg(mimg, lane);
            unsigned ot = t_off(s, tmask) + h * 256, ot2 = ot + 16 * 16384;
            unsigned oa = physo(mema_off(s & 3, 0)), ob = physo(memb_off(s & 3, 0));
            PIN_S(oa); PIN_S(ob); PIN_S(ot); PIN_S(ot2);
            if (wave == 7) {
                if (s > 0) wait_rt(clamp_rt(read_ann(rc0, physo(ANN_BASE + (s - 1) * 128 + 4), cap)));
                dma_mem(rc0, mimg, lane, oa, ob);
                if (gap2 >= 0) {
                    for (int i = 0; i < gap2; ++i) asm volatile("s_nop 7");
                    asm volatile("s_nop 7");
                    dma_mem(rc0, mimg, lane, oa, ob);
                    WAIT_VM(12);
                } else WAIT_VM(2);
            }
            raw_barrier();
            WS(0);
            {
                int tick = 0, tnext = 3 * (j4 ^ 3);
                for (int sp = 0; sp <= cap; ++sp) {
                    const unsigned pr = lds_vol_ld<unsigned>(mimg + (lane < NCT ? lane : 0) * 256 + 128 + (lane & 31) * 4);
                    if (__all(pr != SENT)) break;
                    if (tick == tnext) { tnext += 12; WAIT_VM(0); dma_mem(rc0, mimg, lane, oa, ob); }
                    ++tick;
                }
                if (wave != 7) WAIT_VM(0);
            }
            if (wave == 7) {
                for (int sp = 0;; ++sp) {
                    WAIT_VM(0);
                    unsigned m = 0;
#pragma unroll
                    for (int st = 0; st < 10; ++st) m = mx5(m, lds_ld16(mimg, st * 1024 + lane * 16));
                    if (__all(m != SENT)) break;
                    if (sp > cap) { flag_set(p, FL_TMO, 1u); cap = 0; break; }
                    dma_mem(rc0, mimg, lane, oa, ob);
                }
            }
            f32x4 d[10];
#pragma unroll
            for (int i = 0; i < 10; ++i) { d[i] = zero4(); asm volatile("" : "+v"(d[i])); }
            raw_barrier();
            WS(5);
            u32x4 A[2];
#pragma unroll
            for (int ks = 0; ks < 2; ++ks) { u32x2 a = tr_read(vimg + vr_off, ks * 1024), b = tr_read(vimg + vr_off, ks * 1024 + 128); A[ks] = u32x4{a.x, a.y, b.x, b.y}; }
            unsigned tw[10];
#define T_MM(i) do { d[i] = smf(A[0], w[2 * (i)], d[i], idx); d[i] = smf(A[1], w[2 * (i) + 1], d[i], idx); } while (0)
#define T_PK(i) tw[i] = nosent(pk_bf16(d[i][0] + d[i][2], d[i][1] + d[i][3]))
#define T_ST4(i, so) do { \
                auto a = __builtin_amdgcn_permlane16_swap(tw[i], tw[(i) + 1], false, false); \
                auto b = __builtin_amdgcn_permlane16_swap(tw[(i) + 2], tw[(i) + 3], false, false); \
                auto c = __builtin_amdgcn_permlane32_swap(a[0], b[0], false, false); \
                auto e = __builtin_amdgcn_permlane32_swap(a[1], b[1], false, false); \
                st16_sc1(u32x4{c[0], c[1], e[0], e[1]}, rt, t_voff, so); } while (0)
            T_MM(0); T_MM(1); SB();
            T_MM(2); T_PK(0); SB(); T_MM(3); T_PK(1); SB(); T_MM(4); T_PK(2); SB(); T_MM(5); T_PK(3); SB();
            T_MM(6); T_ST4(0, ot); SB(); T_MM(7); T_PK(4); T_PK(5); SB(); T_MM(8); T_PK(6); SB(); T_MM(9); T_PK(7); SB();
            T_ST4(4, ot2); T_PK(8); T_PK(9);
            {
                auto a = __builtin_amdgcn_permlane16_swap(tw[8], tw[9], false, false);
                auto c = __builtin_amdgcn_permlane32_swap(a[0], a[0], false, false);
                auto e = __builtin_amdgcn_permlane32_swap(a[1], a[1], false, false);
                st16_sc1(u32x4{c[0], c[1], e[0], e[1]}, rt, t_voff2, ot);
            }
            if (s == 2) tmask = wait_sides(p, lane, cap) >> 1;
            WS(7); WSDUMP();
        }
    }
}

DEV void role_comb(const Params& p, int c, char* lds) {
    const int tid = threadIdx.x, lane = tid & 63, wave = __builtin_amdgcn_readfirstlane(tid >> 6);
    const int g = lane >> 4, l16 = lane & 15;
    unsigned* ctrl = (unsigned*)(p.ws + OFF_CTRL) + TUNE_W; (void)ctrl;
    const __amdgpu_buffer_rsrc_t rt = mkrsrc(p.ws + OFF_T, 44 * MB), ru = mkrsrc(p.ws + OFF_U, 80 * MB);
    const int idx = (lane & 2) ? 0xEEEE : 0x4444;
    int cap = SPIN_CAP;
    const u32x4 sv = {SENT, SENT, SENT, SENT};
    if (TUNEW(13, 0)) {
        const unsigned* wsrc[4] = {(const unsigned*)p.wk, (const unsigned*)p.wv, (const unsigned*)p.wq, (const unsigned*)p.wo};
        unsigned acc = 0;
#pragma unroll
        for (int r = 0; r < 2; ++r)
#pragma unroll
            for (int k = 0; k < 4; ++k) acc ^= wsrc[k][(size_t)(r * 20480 + c * 512 + tid) * 32];
        asm volatile("" :: "v"(acc));
    }
    {
        const __amdgpu_buffer_rsrc_t r = mkrsrc(p.ws + OFF_COMM0, COMM_CHUNKS * 16384);
        for (unsigned i = (c * 512 + tid) * 16; i < COMM_CHUNKS * 16384; i += NCT * 8192) st16_sc1(sv, r, i, 0);
        WAIT_VM(0);
        raw_barrier();
        if (tid == 0) flag_add(p, FL_READY);
    }
    const unsigned long long sides = wait_sides(p, lane, cap, 3);
    unsigned long long tmask = sides >> 1;
    const __amdgpu_buffer_rsrc_t rc0 = mkrsrc(p.ws + OFF_COMM0 + (((unsigned)sides & 1u) << 13), 2 * MB - 8192);
    st16_sc1(sv, rt, tid * 16, t_off(0, tmask) + c * 16384);
    st16_sc1(sv, rt, tid * 16, t_off(1, tmask) + c * 16384);
    flags_wait(p, FL_READY, 0, 1, NCT, lane, cap);
    WAIT_VM(0);
    raw_barrier();
    TLR(64 + c, 2);
    const unsigned rstr = c < 32 ? 16384u : 2048u;
    if (tid < 64) {
        const unsigned m = 2 * (l16 >> 2) + (l16 & 1), t = (l16 >> 1) & 1;
        const u32x4 src = *(const u32x4*)(p.mem0 + m * D + 16 * c + 8 * t);
        st16_sc1(u32x4{nosent(src.x), nosent(src.y), nosent(src.z), nosent(src.w)}, rc0, l16 * 16 + g * rstr, physo(mem_off(0, 0, c)));
    }
    if (tid < 2) lds_vol_st<unsigned>(lds + 65536 + tid * 4, 0u);
    flags_wait(p, FL_GU, 2, H, 1u, lane, cap);
#pragma unroll
    for (int j = 0; j < 4; ++j) lds_st16(lds, tid * 16 + j * 8192, ld16_sc1(ru, tid * 16 + j * 8192, c * 32768));
    raw_barrier();
    TLR(64 + c, 3);
    if (wave == 0) {
        int nfail = 0;
        constexpr bool second = true;
        int nft = 0, nfm = 0, nfx = 0; (void)nft; (void)nfm; (void)nfx;
        const unsigned st_voff = (l16 & 6) == 0 ? rowi(2 * g + (l16 & 1), l16 >> 3) * 16 : 0x7FFFFFF0u;
        unsigned long long c0 = 0, c1 = 0, c2 = 0; unsigned k01 = 0, k12 = 0; (void)c0; (void)c1; (void)c2; (void)k01; (void)k12;
        for (int s = 0; s + 1 < NSEG; ++s) {
            const char* ub = lds + (s & 1) * 32768;
            u32x4 uf[24];
#pragma unroll
            for (int j = 0; j < 24; ++j) uf[j] = lds_ld16(ub, j * 1024 + lane * 16);
            unsigned ot = t_off(s, tmask) + c * 16384;
            unsigned ox0 = physo(px_base(s & 3, 0)), ox1 = ox0 + 16384, om = physo(pm_base(s & 3, 0));
            unsigned ost = physo(mem_off((s + 1) & 3, 0, c));
            const unsigned opx_x = ox0 ^ (ox0 + 32768), opx_y = ox1 ^ (ox1 + 32768);
            PIN_S(ot); PIN_S(ox0); PIN_S(ox1); PIN_S(om); PIN_S(ost);
            unsigned tp = clamp_rt(read_ann(rc0, physo(ANN_BASE + s * 128), cap));
            int sp = 0;
            float sa, sb;
            bool chk = false;
            unsigned oy0 = ox0 + 32768, oy1 = ox1 + 32768, on = om + 4096;
            PIN_S(oy0); PIN_S(oy1); PIN_S(on);
#define C_LDX(j, o0, o1) px[j] = ld16_sc1(rc0, lane * 16 + ((j) & 7) * 1024, (j) < 8 ? (o0) : (o1))
#define C_PASS2() do { \
                f32x4 ax = zero4(); \
                _Pragma("unroll") for (int hb = 0; hb < 2; ++hb) { \
                    u32x4 uu[16]; \
                    _Pragma("unroll") for (int j = 0; j < 16; ++j) uu[j] = lds_ld16(ub, (16 * hb + j) * 1024 + lane * 16); \
                    SB(); \
                    _Pragma("unroll") for (int j = 0; j < 8; ++j) ax = smf(px[8 * hb + j], cat8(uu[2 * j], uu[2 * j + 1]), ax, idx); \
                } \
                _Pragma("unroll") for (int j = 0; j < 4; ++j) ax = smf(pm[j], cat8(tf[2 * j], tf[2 * j + 1]), ax, idx); \
                sa = ax[0] + ax[2]; sb = ax[1] + ax[3]; \
            } while (0)
#define C_PUBLISH() do { \
                MT(c1); \
                const float give = (lane & 1) ? sa : sb; \
                const float got = u2f(__builtin_amdgcn_update_dpp(0, f2u(give), 0xB1, 0xf, 0xf, true)); \
                const unsigned pk = nosent(pk_bf16((lane & 1) ? got : sa, (lane & 1) ? sb : got)); \
                u32x4 v; \
                v.x = pk; \
                v.y = __builtin_amdgcn_update_dpp(0, pk, 0x102, 0xf, 0xf, true); \
                v.z = __builtin_amdgcn_update_dpp(0, pk, 0x104, 0xf, 0xf, true); \
                v.w = __builtin_amdgcn_update_dpp(0, pk, 0x106, 0xf, 0xf, true); \
                st16_sc1(v, rc0, st_voff, ost); \
                tb = now_rt(); \
            } while (0)
            bool done = false, fast = false;
            unsigned tb = 0;
            {
                u32x4 tf[8], pm[4], px[16];
                f32x4 ax = zero4();
                asm volatile("" : "+v"(ax));
                wait_rt(tp);
                MT(c0);
#pragma unroll
                for (int j = 0; j < 16; ++j) { C_LDX(j, ox0, ox1); SB(); }
#pragma unroll
                for (int j = 0; j < 4; ++j) { pm[j] = ld16_sc1(rc0, lane * 16 + j * 1024, om); SB(); tf[2 * j] = ld16_sc1(rt, lane * 16 + 2 * j * 1024, ot); SB(); tf[2 * j + 1] = ld16_sc1(rt, lane * 16 + (2 * j + 1) * 1024, ot); SB(); }
                u32x4 ul[8];
#pragma unroll
                for (int j = 0; j < 16; ++j) {
                    const u32x4 u0 = j < 12 ? uf[j < 12 ? 2 * j : 0] : ul[j < 12 ? 0 : 2 * j - 24], u1 = j < 12 ? uf[j < 12 ? 2 * j + 1 : 0] : ul[j < 12 ? 0 : 2 * j - 23];
                    ax = smf(px[j], cat8(u0, u1), ax, idx);
                    SB();
                    if (j < 8) ul[j] = lds_ld16(ub, (24 + j) * 1024 + lane * 16);
                    if (second) C_LDX(j, oy0, oy1);
                    SB();
                }
#pragma unroll
                for (int j = 0; j < 4; ++j) { ax = smf(pm[j], cat8(tf[2 * j], tf[2 * j + 1]), ax, idx); SB(); }
                sa = ax[0] + ax[2]; sb = ax[1] + ax[3];
                done = fast = !any_nan(sa, sb);
                if (done) {
                    C_PUBLISH();
                    asm volatile("" :: "v"(px[0]), "v"(px[1]), "v"(px[2]), "v"(px[3]), "v"(px[4]), "v"(px[5]), "v"(px[6]), "v"(px[7]), "v"(px[8]), "v"(px[9]), "v"(px[10]), "v"(px[11]), "v"(px[12]),
                                 "v"(px[13]), "v"(px[14]), "v"(px[15]) : "memory");
                } else {
                    ++sp;
#pragma unroll
                    for (int j = 0; j < 4; ++j) { pm[j] = ld16_sc1(rc0, lane * 16 + j * 1024, on); tf[2 * j] = ld16_sc1(rt, lane * 16 + 2 * j * 1024, ot); tf[2 * j + 1] = ld16_sc1(rt, lane * 16 + (2 * j + 1) * 1024, ot); }
                    SB();
                    C_PASS2();
                    done = !any_nan(sa, sb);
                }
            }
            while (!done) {
                ++sp;
                u32x4 tf[8], pm[4], px[16];
#pragma unroll
                for (int j = 0; j < 16; ++j) { C_LDX(j, ox0, ox1); SB(); }
#pragma unroll
                for (int j = 0; j < 4; ++j) { pm[j] = ld16_sc1(rc0, lane * 16 + j * 1024, om); SB(); tf[2 * j] = ld16_sc1(rt, lane * 16 + 2 * j * 1024, ot); SB(); tf[2 * j + 1] = ld16_sc1(rt, lane * 16 + (2 * j + 1) * 1024, ot); SB(); }
                C_PASS2();
                if (!any_nan(sa, sb) || chk) break;
                ox0 ^= opx_x; ox1 ^= opx_y; om ^= 4096;
                if (sp >= 3) {
                    for (int k = 0;; ++k) {
                        asm volatile("" ::: "memory");
                        unsigned m = 0;
#pragma unroll
                        for (int j = 0; j < 8; ++j) m = mx5(mx5(mx5(m, ld16_sc1(rc0, lane * 16 + j * 1024, ox0)), ld16_sc1(rc0, lane * 16 + j * 1024, ox1)), ld16_sc1(rt, lane * 16 + j * 1024, ot));
#pragma unroll
                        for (int j = 0; j < 4; ++j) m = mx5(m, ld16_sc1(rc0, lane * 16 + j * 1024, om));
                        if (__all(m != SENT)) break;
                        if (k > cap) { flag_set(p, FL_TMO, 1u); cap = 0; break; }
                    }
                    chk = true;
                }
            }
            if (!fast) C_PUBLISH();
            MT(c2); MTW(); PACC(k01, c0, c1); PACC(k12, c1, c2); PDUMP(64 + c, k01, k12, (unsigned)sp, 0u);
            if (lane == 0) st4_sc1(nosent(tb), rc0, s * 4, physo(STB_BASE + c * 256));
            if (lane == 0) flag_set(p, FL_CB + c, s + 1);
            if (s >= 4) nfail += sp;
            raw_barrier();
            if (s == 2) tmask = (unsigned)__builtin_amdgcn_readfirstlane(lds_vol_ld<unsigned>(lds + 65544));
        }
    } else {
        const int udly = TUNEW(3, 0);
        for (int s = 0; s + 1 < NSEG; ++s) {
            if (wave >= 4 && s + 2 < NSEG) {
                char* nb = lds + ((s + 1) & 1) * 32768;
                for (int i = 0; i < udly; ++i) __builtin_amdgcn_s_sleep(2);
                if (lds_vol_ld<unsigned>(lds + 65536 + vu_par(s + 1) * 4) < vu_cnt(s + 1)) flags_wait(p, FL_GU + vu_par(s + 1), 2, H, vu_cnt(s + 1), lane, cap);
                u32x4 t[8];
#pragma unroll
                for (int j = 0; j < 8; ++j) t[j] = ld16_sc1(ru, (tid - 256) * 16 + j * 4096, ((s + 1) * NCT + c) * 32768);
#pragma unroll
                for (int j = 0; j < 8; ++j) lds_st16(nb, (tid - 256) * 16 + j * 4096, t[j]);
            } else if (wave <= 2 && s + 2 < NSEG) {
                if (s == 3) tmask = (unsigned)__builtin_amdgcn_readfirstlane(lds_vol_ld<unsigned>(lds + 65544));
                for (int i = 0; i < udly; ++i) __builtin_amdgcn_s_sleep(2);
#pragma unroll
                for (int j = 0; j < 4; ++j) st16_sc1(sv, rt, (tid - 64) * 16 + j * 2048, t_off(s + 2, tmask) + c * 16384);
                WAIT_VM(0);
            } else if (wave == 3) {
                if (s >= 1) st16_sc1(sv, rc0, l16 * 16 + g * rstr, physo(mem_off((s - 1) & 3, 0, c)));
                if (s == 2) { __builtin_amdgcn_s_sleep(16); const unsigned tm_ = (unsigned)(wait_sides(p, lane, cap) >> 1); if (lane == 0) lds_vol_st<unsigned>(lds + 65544, tm_); }
                const int pr = vu_par(s + 2);
                if (s + 3 < NSEG && lds_vol_ld<unsigned>(lds + 65536 + pr * 4) < vu_cnt(s + 2)) {
                    __builtin_amdgcn_s_sleep(20);
                    const float v = -wave_max(-(float)flag_get(p, FL_GU + pr + 2 * (lane & 31)));
                    if (lane == 0) lds_vol_st<unsigned>(lds + 65536 + pr * 4, (unsigned)v);
                }
                WAIT_VM(0);
            }
            raw_barrier();
        }
    }
}

constexpr int NCHAIN = 104, NWG = 256;
DEV int bcast0(int v, char* lds) {
    if (threadIdx.x == 0) *(int*)lds = v;
    __syncthreads();
    const int r = __builtin_amdgcn_readfirstlane(*(int*)lds);
    __syncthreads();
    return r;
}
DEV int near_role(int t) { return t < 40 ? 64 + t : t < 104 ? t - 40 : 136 + (t - 104); }
DEV int far_role(int u) { return u < 24 ? 232 + u : u < 56 ? 168 + 2 * (u - 24) : u < 88 ? 104 + (u - 56) : u < 120 ? 168 + 2 * (u - 88) + 1 : 160 + (u - 120); }
DEV int get_role(const Params& p, char* lds) {
    int role = 0;
    if (threadIdx.x == 0) {
        const unsigned xcc = __builtin_amdgcn_s_getreg(20 | (3 << 11)) & 0xf;
        if (xcc < 4) { const int t = (int)flag_add(p, FL_TNEAR); role = t < 128 ? near_role(t) : far_role(255 - t); }
        else { const int u = (int)flag_add(p, FL_TFAR); role = u < 128 ? far_role(u) : near_role(255 - u); }
    }
    return bcast0(role, lds);
}

DEV void task_out(const Params& p, int s, int cg, char* lds, int& cap) {
    const int tid = threadIdx.x, lane = tid & 63, wave = __builtin_amdgcn_readfirstlane(tid >> 6);
    const int g = lane >> 4, l16 = lane & 15;
    const __amdgpu_buffer_rsrc_t rpx = mkrsrc(p.ws + OFF_PX, 5 * MB);
    const __amdgpu_buffer_rsrc_t rt = mkrsrc(p.ws + OFF_T, 44 * MB), ru = mkrsrc(p.ws + OFF_U, 80 * MB);
    const unsigned long long tmask = wait_sides(p, lane, cap) >> 1;
    const int c = cg * 8 + wave;
    flags_wait(p, FL_GU + vu_par(s), 2, H, vu_cnt(s), lane, cap);
    u32x4 uf[32];
#pragma unroll
    for (int j = 0; j < 32; ++j) uf[j] = ld16_sc1(ru, lane * 16 + j * 1024, (s * NCT + c) * 32768);
    if (wave == 0) { flags_wait(p, FL_CB + cg * 8, 1, 8, s + 1, lane, cap); flags_wait(p, FL_PX, 1, 64, s + 1, lane, cap); }
    __syncthreads();
    u32x4 tf[8];
    {
        u32x4 v[10];
#pragma unroll
        for (int i = 0; i < 8; ++i) v[i] = ld16_sc1(rpx, tid * 16 + i * 8192, s * 65536);
#pragma unroll
        for (int i = 0; i < 2; ++i) v[8 + i] = ld16_sc1(rpx, tid * 16 + i * 8192, 4 * MB + s * 16384);
#pragma unroll
        for (int j = 0; j < 8; ++j) tf[j] = ld16_sc1(rt, lane * 16 + j * 1024, t_off(s, tmask) + c * 16384);
#pragma unroll
        for (int i = 0; i < 10; ++i) lds_st16(lds, tid * 16 + i * 8192, v[i]);
    }
    raw_barrier();
    f32x4 d0 = {}, d1 = {};
#pragma unroll
    for (int hh = 0; hh < 32; ++hh) {
        d0 = mf16(uf[hh], lds_ld16(lds, hh * 2048 + lane * 16), d0);
        d1 = mf16(uf[hh], lds_ld16(lds, hh * 2048 + 1024 + lane * 16), d1);
    }
#pragma unroll
    for (int j = 0; j < 8; ++j) {
        d0 = mf16(tf[j], lds_ld16(lds, 65536 + (4 * j + g) * 512 + l16 * 16), d0);
        d1 = mf16(tf[j], lds_ld16(lds, 65536 + (4 * j + g) * 512 + 256 + l16 * 16), d1);
    }
    unsigned short* o = p.out + (size_t)(s * 32 + l16) * (2 * D) + D + c * 16 + 4 * g;
    *(u32x2*)o = u32x2{pk_bf16(d0[0], d0[1]), pk_bf16(d0[2], d0[3])};
    *(u32x2*)(o + 16 * 2 * D) = u32x2{pk_bf16(d1[0], d1[1]), pk_bf16(d1[2], d1[3])};
    __syncthreads();
}
DEV void task_out_fine(const Params& p, int s, int c, char* lds, int& cap) {
    const int tid = threadIdx.x, lane = tid & 63, wave = __builtin_amdgcn_readfirstlane(tid >> 6);
    const int g = lane >> 4, l16 = lane & 15;
    const __amdgpu_buffer_rsrc_t rpx = mkrsrc(p.ws + OFF_PX, 5 * MB);
    const __amdgpu_buffer_rsrc_t rt = mkrsrc(p.ws + OFF_T, 44 * MB), ru = mkrsrc(p.ws + OFF_U, 80 * MB);
    const unsigned long long tmask = wait_sides(p, lane, cap) >> 1;
    flags_wait(p, FL_GU + vu_par(s), 2, H, vu_cnt(s), lane, cap);
    u32x4 uf[4], tf;
#pragma unroll
    for (int j = 0; j < 4; ++j) uf[j] = ld16_sc1(ru, lane * 16 + (4 * wave + j) * 1024, (s * NCT + c) * 32768);
    if (wave == 0) flags_wait(p, FL_CB + c, 0, 1, s, lane, cap);
    __syncthreads();
    flags_wait(p, FL_PX + 8 * wave, 1, 8, s + 1, lane, cap);
    u32x4 pf[4][2], pm[2];
#pragma unroll
    for (int j = 0; j < 4; ++j) { pf[j][0] = ld16_sc1(rpx, lane * 16 + (4 * wave + j) * 2048, s * 65536); pf[j][1] = ld16_sc1(rpx, lane * 16 + (4 * wave + j) * 2048 + 1024, s * 65536); }
    pm[0] = ld16_sc1(rpx, (4 * wave + g) * 512 + l16 * 16, 4 * MB + s * 16384); pm[1] = ld16_sc1(rpx, (4 * wave + g) * 512 + 256 + l16 * 16, 4 * MB + s * 16384);
    for (int sp = 0;; ++sp) {
        asm volatile("" ::: "memory");
        tf = ld16_sc1(rt, lane * 16 + wave * 1024, t_off(s, tmask) + c * 16384);
        if (__all(mx4(tf) != SENT)) break;
        if (sp > cap) { flag_set(p, FL_TMO, 1u); cap = 0; break; }
    }
    f32x4 d0 = mf16(tf, pm[0], zero4()), d1 = mf16(tf, pm[1], zero4());
#pragma unroll
    for (int j = 0; j < 4; ++j) { d0 = mf16(uf[j], pf[j][0], d0); d1 = mf16(uf[j], pf[j][1], d1); }
    *(f32x4*)(lds + wave * 2048 + lane * 16) = d0; *(f32x4*)(lds + wave * 2048 + 1024 + lane * 16) = d1;
    __syncthreads();
    if (wave < 2) {
        f32x4 a = {};
#pragma unroll
        for (int w = 0; w < 8; ++w) a += *(const f32x4*)(lds + w * 2048 + wave * 1024 + lane * 16);
        *(u32x2*)(p.out + (size_t)(s * 32 + 16 * wave + l16) * (2 * D) + D + c * 16 + 4 * g) = u32x2{pk_bf16(a[0], a[1]), pk_bf16(a[2], a[3])};
    }
}

struct cf { float x, y; };
DEV cf cmul(cf a, cf b) { return cf{a.x * b.x - a.y * b.y, a.x * b.y + a.y * b.x}; }
DEV cf cmulc(cf a, cf b) { return cf{a.x * b.x + a.y * b.y, a.y * b.x - a.x * b.y}; }
DEV cf cadd(cf a, cf b) { return cf{a.x + b.x, a.y + b.y}; }
DEV cf csub(cf a, cf b) { return cf{a.x - b.x, a.y - b.y}; }
template <int SG> DEV cf mulmi(cf a) { return SG < 0 ? cf{a.y, -a.x} : cf{-a.y, a.x}; }
template <int SG> DEV void dft4(cf& a, cf& b, cf& c, cf& d) {
    cf t0 = cadd(a, c), t1 = csub(a, c), t2 = cadd(b, d), t3 = mulmi<SG>(csub(b, d));
    a = cadd(t0, t2); c = csub(t0, t2); b = cadd(t1, t3); d = csub(t1, t3);
}
template <int SG> DEV void dft8(cf* v) {
    constexpr float R = 0.70710678118654752f;
    dft4<SG>(v[0], v[2], v[4], v[6]);
    dft4<SG>(v[1], v[3], v[5], v[7]);
    cf o1 = cf{(v[3].x - SG * v[3].y) * R, (v[3].y + SG * v[3].x) * R};
    cf o2 = mulmi<SG>(v[5]);
    cf o3 = cf{(-v[7].x - SG * v[7].y) * R, (-v[7].y + SG * v[7].x) * R};
    cf e0 = v[0], e1 = v[2], e2 = v[4], e3 = v[6], o0 = v[1];
    v[0] = cadd(e0, o0); v[4] = csub(e0, o0);
    v[1] = cadd(e1, o1); v[5] = csub(e1, o1);
    v[2] = cadd(e2, o2); v[6] = csub(e2, o2);
    v[3] = cadd(e3, o3); v[7] = csub(e3, o3);
}
template <int SG> DEV void dft16(cf* v) {
    constexpr float C1 = 0.92387953251128674f, S1 = 0.38268343236508977f, R = 0.70710678118654752f;
#pragma unroll
    for (int b = 0; b < 4; ++b) dft4<SG>(v[b], v[4 + b], v[8 + b], v[12 + b]);
    const cf w1 = {C1, SG * S1}, w2 = {R, SG * R}, w3 = {S1, SG * C1}, w6 = {-R, SG * R}, w9 = {-C1, -SG * S1};
    v[5] = cmul(v[5], w1); v[6] = cmul(v[6], w2); v[7] = cmul(v[7], w3);
    v[9] = cmul(v[9], w2); v[10] = mulmi<SG>(v[10]); v[11] = cmul(v[11], w6);
    v[13] = cmul(v[13], w3); v[14] = cmul(v[14], w6); v[15] = cmul(v[15], w9);
#pragma unroll
    for (int c = 0; c < 4; ++c) dft4<SG>(v[4 * c], v[4 * c + 1], v[4 * c + 2], v[4 * c + 3]);
    cf t;
    t = v[1]; v[1] = v[4]; v[4] = t;  t = v[2]; v[2] = v[8]; v[8] = t;  t = v[3]; v[3] = v[12]; v[12] = t;
    t = v[6]; v[6] = v[9]; v[9] = t;  t = v[7]; v[7] = v[13]; v[13] = t; t = v[11]; v[11] = v[14]; v[14] = t;
}
template <int SG> DEV void dft5(cf* v) {
    constexpr float C1 = 0.30901699437494742f, C2 = -0.80901699437494742f, S1 = 0.95105651629515357f, S2 = 0.58778525229247313f;
    cf a1 = cadd(v[1], v[4]), b1 = csub(v[1], v[4]), a2 = cadd(v[2], v[3]), b2 = csub(v[2], v[3]);
    cf r1 = {v[0].x + C1 * a1.x + C2 * a2.x, v[0].y + C1 * a1.y + C2 * a2.y};
    cf r2 = {v[0].x + C2 * a1.x + C1 * a2.x, v[0].y + C2 * a1.y + C1 * a2.y};
    cf i1 = mulmi<SG>(cf{S1 * b1.x + S2 * b2.x, S1 * b1.y + S2 * b2.y});
    cf i2 = mulmi<SG>(cf{S2 * b1.x - S1 * b2.x, S2 * b1.y - S1 * b2.y});
    v[0] = cadd(v[0], cadd(a1, a2));
    v[1] = cadd(r1, i1); v[4] = csub(r1, i1); v[2] = cadd(r2, i2); v[3] = csub(r2, i2);
}
DEV cf twid(int num, int den) { float s, c; sincospif(-2.0f * (float)num / (float)den, &s, &c); return cf{c, s}; }

constexpr int FRP = 648;
template <int SG> DEV void fft640x8(cf* a, cf* b, const cf* tab, int tid) {
    if (tid < 320) {
        const int q = tid / 40, j = tid % 40; cf v[16];
#pragma unroll
        for (int r = 0; r < 16; ++r) v[r] = a[q * FRP + j + 40 * r];
        dft16<SG>(v);
#pragma unroll
        for (int r = 0; r < 16; ++r) b[q * FRP + 16 * j + (r ^ (j & 15))] = v[r];
    }
    __syncthreads();
    for (int t = tid; t < 640; t += 512) {
        const int q = t / 80, j = t % 80, k = j & 15; cf v[8];
#pragma unroll
        for (int r = 0; r < 8; ++r) { const int n = j + 80 * r; v[r] = b[q * FRP + (n & ~15) + ((n & 15) ^ ((n >> 4) & 15))]; }
#pragma unroll
        for (int r = 1; r < 8; ++r) { const cf w = tab[5 * k * r]; v[r] = SG < 0 ? cmul(v[r], w) : cmulc(v[r], w); }
        dft8<SG>(v);
#pragma unroll
        for (int r = 0; r < 8; ++r) a[q * FRP + (j >> 4) * 128 + k + 16 * r] = v[r];
    }
    __syncthreads();
    for (int t = tid; t < 1024; t += 512) {
        const int q = t >> 7, j = t & 127; cf v[5];
#pragma unroll
        for (int r = 0; r < 5; ++r) v[r] = a[q * FRP + j + 128 * r];
#pragma unroll
        for (int r = 1; r < 5; ++r) { const cf w = tab[j * r]; v[r] = SG < 0 ? cmul(v[r], w) : cmulc(v[r], w); }
        dft5<SG>(v);
#pragma unroll
        for (int r = 0; r < 5; ++r) b[q * FRP + j + 128 * r] = v[r];
    }
    __syncthreads();
}
DEV void make_tab640(cf* tab, int tid) { for (int i = tid; i < 640; i += 512) tab[i] = twid(i, 640); }

DEV void task_fft1(const Params& p, int t, char* lds) {
    const int tid = threadIdx.x;
    cf* a = (cf*)lds; cf* b = a + 8 * FRP; cf* tab = b + 8 * FRP;
    make_tab640(tab, tid);
    for (int i = tid; i < 8 * 80; i += 512) {
        const int q = i / 80, ch = i % 80;
        const u32x4 u = *(const u32x4*)(p.ax + (size_t)(16 * t + 2 * q) * D + ch * 8), w = *(const u32x4*)(p.ax + (size_t)(16 * t + 2 * q + 1) * D + ch * 8);
        cf* d = a + q * FRP + ch * 8;
        d[0] = cf{bf_lo(u.x), bf_lo(w.x)}; d[1] = cf{bf_hi(u.x), bf_hi(w.x)}; d[2] = cf{bf_lo(u.y), bf_lo(w.y)}; d[3] = cf{bf_hi(u.y), bf_hi(w.y)};
        d[4] = cf{bf_lo(u.z), bf_lo(w.z)}; d[5] = cf{bf_hi(u.z), bf_hi(w.z)}; d[6] = cf{bf_lo(u.w), bf_lo(w.w)}; d[7] = cf{bf_hi(u.w), bf_hi(w.w)};
    }
    __syncthreads();
    fft640x8<-1>(a, b, tab, tid);
    const __amdgpu_buffer_rsrc_t rx = mkrsrc(p.ws + OFF_X1, 6 * MB);
    for (int i = tid; i < DF * 8; i += 512) {
        const int k = i >> 3, q = i & 7;
        const cf z = b[q * FRP + k], y = b[q * FRP + (k ? 640 - k : 0)];
        f32x4 o = {0.5f * (z.x + y.x), 0.5f * (z.y - y.y), 0.5f * (z.y + y.y), 0.5f * (y.x - z.x)};
        st16_sc1(__builtin_bit_cast(u32x4, o), rx, (k * S + 16 * t + 2 * q) * 8, 0);
    }
    unsigned* wt = (unsigned*)a;
    for (int i = tid; i < 16 * DF; i += 512) { const int r = i / DF, k = i % DF; wt[k * 17 + r] = (unsigned)p.wr[(size_t)(16 * t + r) * DF + k] | ((unsigned)p.wi[(size_t)(16 * t + r) * DF + k] << 16); }
    __syncthreads();
    const __amdgpu_buffer_rsrc_t rw = mkrsrc(p.ws + OFF_WT, 3 * MB);
    for (int i = tid; i < DF * 4; i += 512) { const int k = i >> 2, r4 = (i & 3) * 4;
        u32x4 v = {wt[k * 17 + r4], wt[k * 17 + r4 + 1], wt[k * 17 + r4 + 2], wt[k * 17 + r4 + 3]};
        st16_sc1(v, rw, (k * S + 16 * t + r4) * 4, 0); }
    __syncthreads();
}

constexpr int CP = 2048 + 128;
DEV int cpad(int i) { return i + (i >> 4); }
DEV void fft2_twiddles(cf (&t1)[16], cf (&t2)[16]) {
    const int m = threadIdx.x & 127, n2 = m >> 4;
#pragma unroll
    for (int i = 1; i < 16; ++i) { t1[i] = twid(m * i, 2048); t2[i] = twid(n2 * i, 128); }
}
DEV void task_fft2(const Params& p, int t, char* lds, cf (&t1)[16], cf (&t2)[16]) {
    int tid = threadIdx.x;
    asm volatile("" : "+v"(tid));
#pragma unroll
    for (int i = 1; i < 16; ++i) asm volatile("" : "+v"(t1[i].x), "+v"(t1[i].y), "+v"(t2[i].x), "+v"(t2[i].y));
    const int col = tid >> 7, m = tid & 127;
    const int k = 4 * t + col;
    const bool act = k < DF;
    cf* buf = (cf*)lds + col * CP;
    const __amdgpu_buffer_rsrc_t rx = mkrsrc(p.ws + OFF_X1, (unsigned)(DF * S * 8)), ry = mkrsrc(p.ws + OFF_Y1, (unsigned)(DF * S * 8)), rw = mkrsrc(p.ws + OFF_WT, (unsigned)(DF * S * 4));
    cf v[16];
#pragma unroll
    for (int n0 = 0; n0 < 16; ++n0) { u32x2 u = ld8_sc1(rx, (k * S + 128 * n0 + m) * 8, 0); v[n0] = __builtin_bit_cast(cf, u); }
    const int f0b = m & 15, n2 = m >> 4;
    const int f1c = m >> 4;
    unsigned wv[16];
#pragma unroll
    for (int u = 0; u < 2; ++u)
#pragma unroll
        for (int f2 = 0; f2 < 8; ++f2) wv[u * 8 + f2] = __builtin_amdgcn_raw_buffer_load_b32(rw, (k * S + f0b + 16 * (f1c + 8 * u) + 256 * f2) * 4, 0, 16);
    dft16<-1>(v);
#pragma unroll
    for (int f0 = 1; f0 < 16; ++f0) v[f0] = cmul(v[f0], t1[f0]);
#pragma unroll
    for (int f0 = 0; f0 < 16; ++f0) buf[cpad(m * 16 + f0)] = v[f0];
    __syncthreads();
#pragma unroll
    for (int n1 = 0; n1 < 16; ++n1) v[n1] = buf[cpad((8 * n1 + n2) * 16 + f0b)];
    dft16<-1>(v);
#pragma unroll
    for (int f1 = 1; f1 < 16; ++f1) v[f1] = cmul(v[f1], t2[f1]);
    __syncthreads();
#pragma unroll
    for (int f1 = 0; f1 < 16; ++f1) buf[cpad((f1 * 8 + n2) * 16 + f0b)] = v[f1];
    __syncthreads();
#pragma unroll
    for (int u = 0; u < 2; ++u)
#pragma unroll
        for (int j = 0; j < 8; ++j) v[u * 8 + j] = buf[cpad(((f1c + 8 * u) * 8 + j) * 16 + f0b)];
    dft8<-1>(v); dft8<-1>(v + 8);
    constexpr float NRM = 1.0f / (2048.0f * 640.0f);
#pragma unroll
    for (int i = 0; i < 16; ++i) v[i] = cmul(v[i], cf{bf_lo(wv[i]) * NRM, bf_hi(wv[i]) * NRM});
    dft8<1>(v); dft8<1>(v + 8);
#pragma unroll
    for (int u = 0; u < 2; ++u)
#pragma unroll
        for (int j = 0; j < 8; ++j) buf[cpad(((f1c + 8 * u) * 8 + j) * 16 + f0b)] = v[u * 8 + j];
    __syncthreads();
#pragma unroll
    for (int f1 = 0; f1 < 16; ++f1) v[f1] = buf[cpad((f1 * 8 + n2) * 16 + f0b)];
#pragma unroll
    for (int f1 = 1; f1 < 16; ++f1) v[f1] = cmulc(v[f1], t2[f1]);
    dft16<1>(v);
#pragma unroll
    for (int n1 = 0; n1 < 16; ++n1) buf[cpad((8 * n1 + n2) * 16 + f0b)] = v[n1];
    __syncthreads();
#pragma unroll
    for (int f0 = 0; f0 < 16; ++f0) v[f0] = buf[cpad(m * 16 + f0)];
#pragma unroll
    for (int f0 = 1; f0 < 16; ++f0) v[f0] = cmulc(v[f0], t1[f0]);
    dft16<1>(v);
    if (act) {
#pragma unroll
        for (int n0 = 0; n0 < 16; ++n0) st8_sc1(__builtin_bit_cast(u32x2, v[n0]), ry, (k * S + 128 * n0 + m) * 8, 0);
    }
    __syncthreads();
}

DEV void task_fft3(const Params& p, int t, char* lds) {
    const int tid = threadIdx.x;
    cf* a = (cf*)lds; cf* b = a + 8 * FRP; cf* tab = b + 8 * FRP;
    make_tab640(tab, tid);
    const __amdgpu_buffer_rsrc_t ry = mkrsrc(p.ws + OFF_Y1, 6 * MB);
    for (int i = tid; i < DF * 8; i += 512) {
        const int k = i >> 3, q = i & 7;
        f32x4 o = __builtin_bit_cast(f32x4, ld16_sc1(ry, (k * S + 16 * t + 2 * q) * 8, 0));
        if (k == 0 || k == 320) { o[1] = 0.f; o[3] = 0.f; }
        a[q * FRP + k] = cf{o[0] - o[3], o[1] + o[2]};
        if (k > 0 && k < 320) a[q * FRP + 640 - k] = cf{o[0] + o[3], o[2] - o[1]};
    }
    __syncthreads();
    fft640x8<1>(a, b, tab, tid);
    for (int i = tid; i < 8 * 80; i += 512) {
        const int q = i / 80, ch = i % 80;
        const cf* d = b + q * FRP + ch * 8;
        u32x4 u = {pk_bf16(d[0].x, d[1].x), pk_bf16(d[2].x, d[3].x), pk_bf16(d[4].x, d[5].x), pk_bf16(d[6].x, d[7].x)};
        u32x4 w = {pk_bf16(d[0].y, d[1].y), pk_bf16(d[2].y, d[3].y), pk_bf16(d[4].y, d[5].y), pk_bf16(d[6].y, d[7].y)};
        *(u32x4*)(p.out + (size_t)(16 * t + 2 * q) * (2 * D) + ch * 8) = u;
        *(u32x4*)(p.out + (size_t)(16 * t + 2 * q + 1) * (2 * D) + ch * 8) = w;
    }
    __syncthreads();
}

constexpr int NT1 = 128, NT2 = 81, NT3 = 128, NFINE = 2, NTO = (NSEG - NFINE) * 5;
__global__ void __launch_bounds__(512, 2) k_main(Params p) {
    extern __shared__ __attribute__((aligned(16))) char lds[];
#define TL(slot) do {} while (0)
    const int role = get_role(p, lds);
    TL(1);
    if (role < 32) { role_qk(p, role, lds); TL(7); return; }
    if (role < 64) { role_vo(p, role - 32, lds); TL(7); return; }
    if (role < NCHAIN) { role_comb(p, role - 64, lds); TL(7); return; }
    const int w = role - NCHAIN, tid = threadIdx.x, lane = tid & 63;
    if (w >= 32 && w < 56) {
        const u32x4* src[5] = {(const u32x4*)p.bx, (const u32x4*)(p.ws + OFF_PX), (const u32x4*)p.ax, (const u32x4*)p.wr, (const u32x4*)p.wi};
        unsigned acc = 0;
#pragma unroll
        for (int k = 0; k < 5; ++k) {
            const int n = (k == 1 ? 5 * (int)MB : (k < 3 ? S * D : S * DF) * 2) / 128;
#pragma unroll 4
            for (int i = (w - 32) * 512 + tid; i < n; i += 24 * 512) acc ^= src[k][i * 8].x;
        }
        if (!TUNEW(9, 0)) { asm volatile("" :: "v"(acc)); return; }
        int cap = SPIN_CAP;
        const unsigned long long tmask = wait_sides(p, lane, cap) >> 1;
#pragma unroll 4
        for (int i = (w - 32) * 512 + tid + 3 * NCT * 64; i < NSEG * NCT * 64; i += 24 * 512)
            acc ^= *(const unsigned*)(p.ws + OFF_T + t_off(i / (NCT * 64), tmask) + ((i >> 6) % NCT) * 16384 + (i & 63) * 128);
        asm volatile("" :: "v"(acc));
        return;
    }
    if (w < 32) role_gemm_qk(p, w, lds);
    else if (w < 64) {}
    else if (w < 128) role_gemm_vu(p, (w - 64) >> 1, w & 1, lds);
    else if (tid < 64 && w - 128 < 1 + NTPG) probe_page(p, w - 128, lane);
    TL(2);
    int cap = SPIN_CAP;
#define NEXT_TASK(Q) (__syncthreads(), bcast0(threadIdx.x == 0 ? (int)flag_add(p, Q) : 0, lds))
    if (w < 128) {
        int t = NEXT_TASK(FL_QUEUE);
        while (t < NT1) {
            task_fft1(p, t, lds);
            WAIT_VM(0); __syncthreads();
            if (tid == 0) flag_add(p, FL_F1);
            t = NEXT_TASK(FL_QUEUE);
        }
        TL(3);
        cf tw1[16], tw2[16];
        if (t < NT1 + NT2) fft2_twiddles(tw1, tw2);
        while (t < NT1 + NT2) {
            if (tid < 64) flags_wait(p, FL_F1, 0, 1, NT1, lane, cap);
            __syncthreads();
            task_fft2(p, t - NT1, lds, tw1, tw2);
            WAIT_VM(0); __syncthreads();
            if (tid == 0) flag_add(p, FL_F2);
            t = NEXT_TASK(FL_QUEUE);
        }
        TL(4);
        while (t < NT1 + NT2 + NT3) {
            if (tid < 64) flags_wait(p, FL_F2, 0, 1, NT2, lane, cap);
            __syncthreads();
            task_fft3(p, t - NT1 - NT2, lds);
            t = NEXT_TASK(FL_QUEUE);
        }
        TL(5);
        t -= NT1 + NT2 + NT3;
        if (t < NFINE * NCT) task_out_fine(p, NSEG - NFINE + t / NCT, t % NCT, lds, cap);
    } else {
        int t = NEXT_TASK(FL_QOUT);
        while (t < NTO) {
            task_out(p, t / 5, t % 5, lds, cap);
            t = NEXT_TASK(FL_QOUT);
        }
    }
    TL(7);
}

extern "C" void kernel_launch(void* const* d_in, const int* in_sizes, int n_in, void* d_out, int out_size, void* d_ws, size_t ws_size, hipStream_t stream) {
    Params p;
    p.ax = (const unsigned short*)d_in[0]; p.wr = (const unsigned short*)d_in[1]; p.wi = (const unsigned short*)d_in[2];
    p.bx = (const unsigned short*)d_in[3]; p.mem0 = (const unsigned short*)d_in[4];
    p.wq = (const unsigned short*)d_in[5]; p.wk = (const unsigned short*)d_in[6]; p.wv = (const unsigned short*)d_in[7]; p.wo = (const unsigned short*)d_in[8];
    p.out = (unsigned short*)d_out;
    p.ws = (char*)(((size_t)d_ws + 2 * MB - 1) & ~(size_t)(2 * MB - 1));
    constexpr int LDS = 163840;
    (void)hipFuncSetAttribute((const void*)k_main, hipFuncAttributeMaxDynamicSharedMemorySize, LDS);
    (void)hipMemsetAsync(p.ws + OFF_CTRL, 0, FLAG_BYTES, stream);
    k_main<<<NWG, 512, LDS, stream>>>(p);
}
```

```cpp
#include <hip/hip_runtime.h>
#include <hip/hip_bf16.h>

typedef __bf16 bf16x8 __attribute__((ext_vector_type(8)));
typedef __bf16 bf16x16 __attribute__((ext_vector_type(16)));
typedef __bf16 bf16x2 __attribute__((ext_vector_type(2)));
typedef short s16x4 __attribute__((ext_vector_type(4)));
typedef float f32x2 __attribute__((ext_vector_type(2)));
typedef float f32x4 __attribute__((ext_vector_type(4)));
typedef float f32x16 __attribute__((ext_vector_type(16)));
typedef unsigned u32x2 __attribute__((ext_vector_type(2)));
typedef unsigned u32x4 __attribute__((ext_vector_type(4)));
typedef unsigned u32x8 __attribute__((ext_vector_type(8)));
#define LAS __attribute__((address_space(3)))
#define GAS __attribute__((address_space(1)))
#define DEV __device__ __forceinline__

constexpr int S = 2048, D = 640, DF = 321, H = 32, DH = 128, NSEG = 64, NCT = 40;
constexpr unsigned SENT = 0xFFFFFFFFu;
constexpr int SPIN_CAP = 1 << 15;

constexpr size_t MB = 1 << 20;
constexpr size_t OFF_CTRL = 0;
constexpr size_t OFF_COMM0 = 2 * MB;
constexpr size_t OFF_QX = 6 * MB;
constexpr size_t OFF_KX = OFF_QX + 16 * MB;
constexpr size_t OFF_U = OFF_KX + 16 * MB;
constexpr size_t OFF_T = OFF_U + 80 * MB;
constexpr int NTPG = 22;
constexpr size_t OFF_PX = OFF_T + 44 * MB;
constexpr size_t OFF_PXM = OFF_PX + 4 * MB;
constexpr size_t OFF_X1 = OFF_PXM + 1 * MB;
constexpr size_t OFF_Y1 = OFF_X1 + 6 * MB;
constexpr size_t OFF_WT = OFF_Y1 + 6 * MB;
constexpr size_t WS_NEED = OFF_WT + 3 * MB;

constexpr int RM = 4, RP = 2;
DEV unsigned mema_off(int sl, int r) { return (sl * 4 + r) * 8192; }
DEV unsigned memb_off(int sl, int r) { return (16 + sl) * 8192 + r * 2048; }
DEV unsigned mem_off(int sl, int r, int c) { return c < 32 ? mema_off(sl, r) + c * 256 : memb_off(sl, r) + (c - 32) * 256; }
DEV unsigned px_base(int sl, int r) { return (20 + (sl * 2 + r) * 2) * 8192; }
DEV unsigned pm_base(int sl, int r) { return (36 + sl) * 8192 + r * 4096; }
constexpr unsigned ANN_BASE = 40 * 8192, STA_BASE = 41 * 8192, STB_BASE = 42 * 8192, COMM_CHUNKS = 44;
DEV unsigned rowi(unsigned m, unsigned t) { return 4 * (m >> 1) + 2 * t + (m & 1); }
DEV unsigned physo(unsigned o) { return ((o >> 13) << 14) + (o & 8191u); }
DEV unsigned t_off(int s, unsigned long long tmask) { const int pg = s / 3; return pg * (2u << 20) + (s - 3 * pg) * (NCT * 16384) + (((unsigned)(tmask >> pg) & 1u) << 13); }
constexpr int FL_TNEAR = 0, FL_TFAR = 1, FL_QUEUE = 2, FL_READY = 3, FL_F1 = 4, FL_F2 = 5, FL_TMO = 6, FL_QOUT = 7, FL_SIDE = 8, FL_GQ = 32, FL_GU = 96, FL_PX = 160, FL_CB = 224, FL_END = 264;
constexpr int FLAG_BYTES = FL_END * 128;
constexpr int TUNE_W = 16384;
struct Params {
    const unsigned short* ax; const unsigned short* wr; const unsigned short* wi;
    const unsigned short* bx; const unsigned short* mem0;
    const unsigned short* wq; const unsigned short* wk; const unsigned short* wv; const unsigned short* wo;
    unsigned short* out; char* ws;
};

DEV __amdgpu_buffer_rsrc_t mkrsrc(const void* p, unsigned bytes) { return __builtin_amdgcn_make_buffer_rsrc((void*)p, 0, bytes, 0x00020000); }
DEV u32x4 ld16_sc1(__amdgpu_buffer_rsrc_t r, unsigned voff, unsigned soff) { return __builtin_amdgcn_raw_buffer_load_b128(r, voff, soff, 16); }
DEV u32x2 ld8_sc1(__amdgpu_buffer_rsrc_t r, unsigned voff, unsigned soff) { return __builtin_amdgcn_raw_buffer_load_b64(r, voff, soff, 16); }
DEV void st16_sc1(u32x4 v, __amdgpu_buffer_rsrc_t r, unsigned voff, unsigned soff) { __builtin_amdgcn_raw_buffer_store_b128(v, r, voff, soff, 16); asm volatile("s_nop 1" :: "v"(v) : "memory"); }
DEV void st8_sc1(u32x2 v, __amdgpu_buffer_rsrc_t r, unsigned voff, unsigned soff) { __builtin_amdgcn_raw_buffer_store_b64(v, r, voff, soff, 16); }
DEV void st4_sc1(unsigned v, __amdgpu_buffer_rsrc_t r, unsigned voff, unsigned soff) { __builtin_amdgcn_raw_buffer_store_b32(v, r, voff, soff, 16); }
DEV unsigned pk_bf16(float a, float b) { f32x2 f = {a, b}; bf16x2 p = __builtin_convertvector(f, bf16x2); return __builtin_bit_cast(unsigned, p); }
DEV float bf_lo(unsigned u) { return __builtin_bit_cast(float, u << 16); }
DEV float bf_hi(unsigned u) { return __builtin_bit_cast(float, u & 0xFFFF0000u); }
DEV unsigned nosent(unsigned u) { return u < 0xFFFFFFFEu ? u : 0xFFFFFFFEu; }
DEV unsigned mx4(u32x4 v) { return max(max(v.x, v.y), max(v.z, v.w)); }
DEV f32x4 zero4() { return f32x4{0.f, 0.f, 0.f, 0.f}; }
DEV u32x4 mk4(u32x2 a, u32x2 b) { return u32x4{a.x, a.y, b.x, b.y}; }
DEV bf16x8 as_bf8(u32x4 v) { return __builtin_bit_cast(bf16x8, v); }
DEV u32x2 tr_read(const char* lds, unsigned off) { s16x4 v = __builtin_amdgcn_ds_read_tr16_b64_v4i16((LAS s16x4*)(lds + off)); return __builtin_bit_cast(u32x2, v); }
DEV u32x4 lds_ld16(const char* lds, unsigned off) { return *(const u32x4*)(lds + off); }
DEV void lds_st16(char* lds, unsigned off, u32x4 v) { *(u32x4*)(lds + off) = v; }
DEV void glds16(const void* g, char* lds) { __builtin_amdgcn_global_load_lds((const GAS unsigned*)g, (LAS unsigned*)lds, 16, 0, 0); }
#define WAIT_VM(n) asm volatile("s_waitcnt vmcnt(" #n ")" ::: "memory")
#define WAIT_LGKM0() asm volatile("s_waitcnt lgkmcnt(0)" ::: "memory")
DEV void raw_barrier() { asm volatile("" ::: "memory"); __builtin_amdgcn_s_waitcnt(0xC07F); __builtin_amdgcn_s_barrier(); asm volatile("" ::: "memory"); }
DEV float max3f(float a, float b, float c) { return __builtin_fmaxf(__builtin_fmaxf(a, b), c); }
template <class T> DEV T lds_vol_ld(const char* p) { return *(volatile LAS T*)p; }
template <class T> DEV void lds_vol_st(char* p, T v) { *(volatile LAS T*)p = v; }

DEV unsigned swz(unsigned row) { return ((row & 3u) << 2) | ((row >> 2) & 3u); }
DEV unsigned img_off(unsigned row, unsigned ch) { return 256u * row + 16u * (ch ^ swz(row)); }
DEV void bdma16(__amdgpu_buffer_rsrc_t r, char* lds, unsigned voff, unsigned soff) { __builtin_amdgcn_raw_ptr_buffer_load_lds(r, (LAS void*)lds, 16, voff, soff, 0, 0); }
template <int NROWS>
DEV void stage_img(__amdgpu_buffer_rsrc_t r, unsigned elem0, unsigned pitch, char* img, int wave, int lane) {
    const unsigned row0 = 4 * wave + (lane >> 4), voff = (row0 * pitch + (((lane & 15) ^ swz(row0)) << 3)) * 2;
#pragma unroll
    for (int it = 0; it < NROWS / 32; ++it) bdma16(r, img + (wave + 8 * it) * 1024, voff, (elem0 + it * 32 * pitch) * 2);
}
DEV unsigned lds_addr(const char* p) { return (unsigned)(size_t)(const LAS char*)p; }
DEV void tr8(u32x2 (&o)[8], const unsigned (&a)[8]) {
    asm volatile("ds_read_b64_tr_b16 %0, %8\n\tds_read_b64_tr_b16 %1, %9\n\tds_read_b64_tr_b16 %2, %10\n\tds_read_b64_tr_b16 %3, %11\n\t"
                 "ds_read_b64_tr_b16 %4, %12\n\tds_read_b64_tr_b16 %5, %13\n\tds_read_b64_tr_b16 %6, %14\n\tds_read_b64_tr_b16 %7, %15\n\ts_waitcnt lgkmcnt(0)"
                 : "=&v"(o[0]), "=&v"(o[1]), "=&v"(o[2]), "=&v"(o[3]), "=&v"(o[4]), "=&v"(o[5]), "=&v"(o[6]), "=&v"(o[7])
                 : "v"(a[0]), "v"(a[1]), "v"(a[2]), "v"(a[3]), "v"(a[4]), "v"(a[5]), "v"(a[6]), "v"(a[7]) : "memory");
}
DEV unsigned a16(const char* img, unsigned r0, unsigned c, int lane) {
    const unsigned q = (lane & 15) >> 2, p = lane & 3;
    return lds_addr(img) + img_off(r0 + q, 2 * c + (p >> 1)) + 8 * (p & 1);
}
DEV unsigned a32(const char* img, unsigned r0, unsigned c, int lane) {
    const unsigned blk = (lane >> 4) & 1, q = (lane & 15) >> 2, p = lane & 3;
    return lds_addr(img) + img_off(r0 + q, 4 * c + 2 * blk + (p >> 1)) + 8 * (p & 1);
}
DEV unsigned a16p(const char* img, unsigned k0, unsigned c, int lane) {
    const unsigned q = (lane & 15) >> 2, p = lane & 3;
    const unsigned row = (k0 & ~15u) + 8 * (q >> 1) + 2 * ((k0 & 15u) >> 2) + (q & 1);
    return lds_addr(img) + img_off(row, 2 * c + (p >> 1)) + 8 * (p & 1);
}
template <bool PERM>
DEV void load_bsp2(bf16x16& x, bf16x16& y, const char* imgx, unsigned cx, const char* imgy, unsigned cy, int lane) {
    const unsigned g = lane >> 4;
    unsigned a[8]; u32x2 o[8];
#pragma unroll
    for (int i = 0; i < 4; ++i) {
        const unsigned k0 = 32 * (i >> 1) + 8 * g + 4 * (i & 1);
        a[i] = PERM ? a16p(imgx, k0, cx, lane) : a16(imgx, k0, cx, lane); a[4 + i] = PERM ? a16p(imgy, k0, cy, lane) : a16(imgy, k0, cy, lane);
    }
    tr8(o, a);
    u32x8 r = {o[0].x, o[0].y, o[1].x, o[1].y, o[2].x, o[2].y, o[3].x, o[3].y}, t = {o[4].x, o[4].y, o[5].x, o[5].y, o[6].x, o[6].y, o[7].x, o[7].y};
    x = __builtin_bit_cast(bf16x16, r); y = __builtin_bit_cast(bf16x16, t);
}
DEV f32x4 smf(u32x4 a, bf16x16 b, f32x4 c, int idx) { return __builtin_amdgcn_smfmac_f32_16x16x64_bf16(as_bf8(a), b, c, idx, 0, 0); }
DEV f32x4 mf16(u32x4 a, u32x4 b, f32x4 c) { return __builtin_amdgcn_mfma_f32_16x16x32_bf16(as_bf8(a), as_bf8(b), c, 0, 0, 0); }
DEV f32x16 mf32(u32x4 a, u32x4 b, f32x16 c) { return __builtin_amdgcn_mfma_f32_32x32x16_bf16(as_bf8(a), as_bf8(b), c, 0, 0, 0); }
DEV float u2f(unsigned u) { return __builtin_bit_cast(float, u); }
DEV unsigned f2u(float f) { return __builtin_bit_cast(unsigned, f); }
DEV float xmax16_32(float x) {
    auto r = __builtin_amdgcn_permlane16_swap(f2u(x), f2u(x), false, false);
    x = fmaxf(u2f(r[0]), u2f(r[1]));
    auto r2 = __builtin_amdgcn_permlane32_swap(f2u(x), f2u(x), false, false);
    return fmaxf(u2f(r2[0]), u2f(r2[1]));
}
DEV float xsum16_32(float x) {
    auto r = __builtin_amdgcn_permlane16_swap(f2u(x), f2u(x), false, false);
    x = u2f(r[0]) + u2f(r[1]);
    auto r2 = __builtin_amdgcn_permlane32_swap(f2u(x), f2u(x), false, false);
    return u2f(r2[0]) + u2f(r2[1]);
}

#define ST(role, s, slot) do {} while (0)
#define MT(x) do {} while (0)
#define MTW() do {} while (0)
#define PACC(acc, a, b) do {} while (0)
#define PDUMP(role, x, y, z, w) do {} while (0)
#define WS(k) do {} while (0)
#define WSDECL do {} while (0)
#define WSDUMP() do {} while (0)
#define TLR(role, slot) do {} while (0)
DEV unsigned* flag_ptr(const Params& p, int line) { return (unsigned*)(p.ws + OFF_CTRL) + line * 32; }
DEV void flag_set(const Params& p, int line, unsigned v) { __hip_atomic_store(flag_ptr(p, line), v, __ATOMIC_RELAXED, __HIP_MEMORY_SCOPE_AGENT); }
DEV unsigned flag_add(const Params& p, int line) { return __hip_atomic_fetch_add(flag_ptr(p, line), 1u, __ATOMIC_RELAXED, __HIP_MEMORY_SCOPE_AGENT); }
DEV unsigned flag_get(const Params& p, int line) { return __hip_atomic_load(flag_ptr(p, line), __ATOMIC_RELAXED, __HIP_MEMORY_SCOPE_AGENT); }
DEV unsigned flags_wait(const Params& p, int line0, int stride, int n, unsigned want, int lane, int& cap) {
    unsigned v = 0;
    for (int sp = 0;; ++sp) {
        v = flag_get(p, line0 + stride * (lane < n ? lane : 0));
        if (__all(v >= want)) break;
        if (sp > cap) { flag_set(p, FL_TMO, 1u); cap = 0; break; }
        __builtin_amdgcn_s_sleep(4);
    }
    asm volatile("" ::: "memory");
    return v;
}
#define TUNEW(i, dflt) (dflt)
DEV void probe_page(const Params& p, int pg, int lane) {
    const unsigned xcc = __builtin_amdgcn_s_getreg(20 | (3 << 11)) & 0xf;
    unsigned best[2] = {1u << 30, 1u << 30};
    char* base = p.ws + (pg < 1 ? OFF_COMM0 : OFF_T + (pg - 1) * 2 * MB) + 2 * MB - 16384;
    const __amdgpu_buffer_rsrc_t r = mkrsrc(base, 16384);
    const u32x4 sv = {SENT, SENT, SENT, SENT};
    st16_sc1(sv, r, lane * 128, 0); st16_sc1(sv, r, lane * 128, 8192);
    WAIT_VM(0);
#pragma unroll 1
    for (int i = 0; i < (pg < 3 ? TUNEW(11, 7) : TUNEW(10, 12)); ++i) {
#pragma unroll
        for (int c = 0; c < 2; ++c) {
            const char* q = base + c * 8192 + i * 512;
            unsigned long long t0, t1; unsigned v;
            asm volatile("s_memtime %0\n\ts_waitcnt lgkmcnt(0)\n\tglobal_load_dword %2, %3, off sc1\n\ts_waitcnt vmcnt(0)\n\ts_memtime %1\n\ts_waitcnt lgkmcnt(0)"
                         : "=&s"(t0), "=&s"(t1), "=&v"(v) : "v"(q) : "memory");
            best[c] = min(best[c], (unsigned)(t1 - t0));
        }
    }
    unsigned side = best[0] < best[1] ? 0u : 1u;
    if (xcc >= 4) side ^= 1u;
    if (lane == 0) flag_set(p, FL_SIDE + pg, 1u + side);
}
DEV unsigned long long wait_sides(const Params& p, int lane, int& cap, int n = 1 + NTPG) {
    const unsigned v = flags_wait(p, FL_SIDE, 1, n, 1u, lane, cap);
    return __ballot(lane < n && v == 2u);
}

constexpr unsigned WBYTES = D * H * DH * 2;
#define STAGE_RING(RING, EXTRA, ISSUE, READ) do { \
        _Pragma("unroll") for (int bt = 0; bt < RING - 1; ++bt) { char* img = lds + bt * 32768; ISSUE; } \
        _Pragma("unroll") for (int bq = 0; bq < 10; ++bq) { \
            if (bq + RING - 1 <= 10) asm volatile("s_waitcnt vmcnt(%0)" :: "i"(4 * (RING - 2)) : "memory"); \
            else asm volatile("s_waitcnt vmcnt(%0)" :: "i"(4 * (9 - bq) + (EXTRA)) : "memory"); \
            raw_barrier(); \
            if (bq + RING - 1 < 10) { const int bt = bq + RING - 1; char* img = lds + (bt % RING) * 32768; ISSUE; } \
            else if (bq + RING - 1 == 10) { AFTER_W; } \
            { const int bt = bq; const char* img = lds + (bt % RING) * 32768; READ; } \
        } \
        raw_barrier(); \
    } while (0)
constexpr int NSPLIT = 16;
DEV int vu_seg(int par, int i) { return i < NSPLIT / 2 ? par + 2 * i : min(i + NSPLIT / 2, NSEG - 1); }
DEV int vu_par(int seg) { return seg < NSPLIT ? seg & 1 : 0; }
DEV unsigned vu_cnt(int seg) { return seg < NSPLIT ? (seg >> 1) + 1 : seg - NSPLIT / 2 + 1; }
constexpr int XT_BYTES = 40960;
DEV void stage_x(__amdgpu_buffer_rsrc_t rx, int seg, char* img, int wave, int lane) {
    const int tok = 8 * (wave & 3) + (lane >> 3), c = (lane & 7) ^ ((tok >> 1) & 7);
    const unsigned voff = (tok * D + (wave >> 2) * 64 + c * 8) * 2;
#pragma unroll
    for (int it = 0; it < 5; ++it) bdma16(rx, img + (wave + 8 * it) * 1024, voff, (seg * 32 * D + it * 128) * 2);
}
DEV void stage_x1(__amdgpu_buffer_rsrc_t rx, int seg, char* img, int wave, int lane, int it) {
    const int tok = 8 * (wave & 3) + (lane >> 3), c = (lane & 7) ^ ((tok >> 1) & 7);
    bdma16(rx, img + (wave + 8 * it) * 1024, (tok * D + (wave >> 2) * 64 + c * 8) * 2, (seg * 32 * D + it * 128) * 2);
}
DEV unsigned xfrag_off(int ks, int lane) {
    const int tok = lane & 31, hi = lane >> 5, c = 2 * (ks & 3) + hi;
    return (ks >> 2) * 4096 + tok * 128 + ((c ^ ((tok >> 1) & 7)) << 4);
}

DEV void role_gemm_qk(const Params& p, int h, char* lds) {
    constexpr int par = 0;
    const int tid = threadIdx.x, lane = tid & 63, wave = __builtin_amdgcn_readfirstlane(tid >> 6);
    u32x4 w[40];
    const __amdgpu_buffer_rsrc_t rw0 = mkrsrc(p.wq, WBYTES), rw1 = mkrsrc(p.wk, WBYTES), rx = mkrsrc(p.bx, S * D * 2);
    for (int i = 0; i < par * TUNEW(7, 0); ++i) __builtin_amdgcn_s_sleep(2);
    char* xbuf = lds + 40960;
#define XSLOT(i) (lds + ((i) % 3 == 0 ? 3 * XT_BYTES : ((i) % 3 - 1) * XT_BYTES))
    (void)xbuf;
#define AFTER_W stage_x(rx, par, XSLOT(0), wave, lane)
    STAGE_RING(3, 5,
        do { stage_img<64>(rw0, bt * 64 * H * DH + h * DH, H * DH, img, wave, lane); stage_img<64>(rw1, bt * 64 * H * DH + h * DH, H * DH, img + 16384, wave, lane); } while (0),
        do { unsigned a[8]; u32x2 o[8];
             _Pragma("unroll") for (int i = 0; i < 8; ++i) a[i] = a32(img + (wave >> 2) * 16384, 16 * (i >> 1) + 8 * (lane >> 5) + 4 * (i & 1), wave & 3, lane);
             tr8(o, a);
             _Pragma("unroll") for (int ks = 0; ks < 4; ++ks) w[bt * 4 + ks] = mk4(o[2 * ks], o[2 * ks + 1]); } while (0));
#undef AFTER_W
    TLR(104 + h, 6);
    const __amdgpu_buffer_rsrc_t ro = mkrsrc(p.ws + (wave < 4 ? OFF_QX : OFF_KX), TUNEW(4, 0) ? 0 : 16 * MB);
    stage_x(rx, 1, XSLOT(1), wave, lane);
    for (int i = 0; i < NSEG; ++i) {
        const int seg = i;
        char* img = XSLOT(i);
        if (i == 0) WAIT_VM(5); else if (i <= 2) WAIT_VM(0); else WAIT_VM(7);
        raw_barrier();
        if (tid == 0 && i >= 1 && i != 3) flag_set(p, FL_GQ + 2 * h + par, i <= 2 ? i : i - 1);
        constexpr bool various = true;
        f32x16 acc = {};
        {
            u32x4 xf[2][4];
#pragma unroll
            for (int j = 0; j < 4; ++j) xf[0][j] = lds_ld16(img, xfrag_off(j, lane));
#pragma unroll
            for (int bq = 0; bq < 10; ++bq) {
                if (bq + 1 < 10) {
#pragma unroll
                    for (int j = 0; j < 4; ++j) xf[(bq + 1) & 1][j] = lds_ld16(img, xfrag_off(4 * (bq + 1) + j, lane));
                }
                __builtin_amdgcn_sched_barrier(0);
#pragma unroll
                for (int j = 0; j < 4; ++j) acc = mf32(w[4 * bq + j], xf[bq & 1][j], acc);
                __builtin_amdgcn_sched_barrier(0);
                if ((bq & 1) && various) stage_x1(rx, min(seg + 2, NSEG - 1), XSLOT(i + 2), wave, lane, bq >> 1);
            }
        }
#pragma unroll
        for (int a = 0; a < 4; a += 2) {
            unsigned x0 = pk_bf16(acc[4 * a], acc[4 * a + 1]), x1 = pk_bf16(acc[4 * a + 2], acc[4 * a + 3]);
            unsigned y0 = pk_bf16(acc[4 * a + 4], acc[4 * a + 5]), y1 = pk_bf16(acc[4 * a + 6], acc[4 * a + 7]);
            auto r0 = __builtin_amdgcn_permlane32_swap(x0, y0, false, false);
            auto r1 = __builtin_amdgcn_permlane32_swap(x1, y1, false, false);
            u32x4 v = {r0[0], r1[0], r0[1], r1[1]};
            st16_sc1(v, ro, ((4 * (wave & 3) + a) * 32 + lane) * 16, (seg * H + h) * 8192);
        }
    }
    WAIT_VM(0);
    raw_barrier();
    if (tid == 0) flag_set(p, FL_GQ + 2 * h + par, NSEG);
}

DEV void role_gemm_vu(const Params& p, int h, int par, char* lds) {
    const int tid = threadIdx.x, lane = tid & 63, wave = __builtin_amdgcn_readfirstlane(tid >> 6);
    const int et = wave & 3, kh = wave >> 2, g = lane >> 4;
    u32x4 wv[20], wo[5][4];
    const __amdgpu_buffer_rsrc_t rw0 = mkrsrc(p.wv, WBYTES), rw1 = mkrsrc(p.wo, WBYTES), rx = mkrsrc(p.bx, S * D * 2);
    for (int i = 0; i < par * TUNEW(7, 0); ++i) __builtin_amdgcn_s_sleep(2);
#define XS2(i) (lds + (((i) & 1) ? 0 : 3 * XT_BYTES))
#define AFTER_W stage_x(rx, par, XS2(0), wave, lane)
    STAGE_RING(3, 5,
        do { stage_img<32>(rw0, bt * 32 * H * DH + h * DH, H * DH, img, wave, lane); stage_img<32>(rw0, (320 + bt * 32) * H * DH + h * DH, H * DH, img + 8192, wave, lane);
             stage_img<64>(rw1, (h * DH + (bt & 1) * 64) * D + (bt >> 1) * 128, D, img + 16384, wave, lane); } while (0),
        do { unsigned a[8]; u32x2 o[8];
             _Pragma("unroll") for (int i = 0; i < 4; ++i) { a[i] = a32(img + kh * 8192, 16 * (i >> 1) + 8 * (lane >> 5) + 4 * (i & 1), et, lane);
                 a[4 + i] = a16(img + 16384, 32 * (i >> 1) + 16 * (g >> 1) + 4 * (g & 1) + 8 * (i & 1), wave, lane); }
             tr8(o, a);
             _Pragma("unroll") for (int ks = 0; ks < 2; ++ks) { wv[bt * 2 + ks] = mk4(o[2 * ks], o[2 * ks + 1]); wo[bt >> 1][2 * (bt & 1) + ks] = mk4(o[4 + 2 * ks], o[5 + 2 * ks]); } } while (0));
#undef AFTER_W
    TLR(168 + 2 * h + par, 6);
    char* xch = lds + XT_BYTES;
    char* vimg = xch + 32768;
    const __amdgpu_buffer_rsrc_t ru = mkrsrc(p.ws + OFF_U, TUNEW(4, 0) ? 0 : 80 * MB);
    f32x4 k0, k1;
    const int nit = par ? NSPLIT / 2 : NSEG - NSPLIT / 2;
#define VU_P1(i, MID) do { \
        const char* img = XS2(i) + kh * 20480; \
        f32x16 acc = {}; \
        u32x4 xf[2][4]; \
        _Pragma("unroll") for (int j = 0; j < 4; ++j) xf[0][j] = lds_ld16(img, xfrag_off(j, lane)); \
        _Pragma("unroll") for (int bq = 0; bq < 5; ++bq) { \
            if (bq + 1 < 5) { _Pragma("unroll") for (int j = 0; j < 4; ++j) xf[(bq + 1) & 1][j] = lds_ld16(img, xfrag_off(4 * (bq + 1) + j, lane)); } \
            __builtin_amdgcn_sched_barrier(0); \
            _Pragma("unroll") for (int j = 0; j < 4; ++j) acc = mf32(wv[4 * bq + j], xf[bq & 1][j], acc); \
            __builtin_amdgcn_sched_barrier(0); \
            MID; \
        } \
        const f32x4 lo0 = {acc[0], acc[1], acc[2], acc[3]}, lo1 = {acc[4], acc[5], acc[6], acc[7]}, hi0 = {acc[8], acc[9], acc[10], acc[11]}, hi1 = {acc[12], acc[13], acc[14], acc[15]}; \
        char* xo = xch + ((i) & 1) * 16384 + wave * 2048 + lane * 16; \
        *(f32x4*)xo = KH ? lo0 : hi0; *(f32x4*)(xo + 1024) = KH ? lo1 : hi1; \
        k0 = KH ? hi0 : lo0; k1 = KH ? hi1 : lo1; \
    } while (0)
#define VU_FIN(i) do { \
        const char* xi = xch + ((i) & 1) * 16384 + (wave ^ 4) * 2048 + lane * 16; \
        const f32x4 a = k0 + *(const f32x4*)xi, b = k1 + *(const f32x4*)(xi + 1024); \
        lds_st16(vimg + ((i) & 1) * 8192, ((et * 4 + 2 * kh + (lane >> 5)) * 32 + (lane & 31)) * 16, u32x4{pk_bf16(a[0], a[1]), pk_bf16(a[2], a[3]), pk_bf16(b[0], b[1]), pk_bf16(b[2], b[3])}); \
    } while (0)
#define VU_P2(i, MID) do { \
        u32x4 vf[2][4]; \
        unsigned vb_ = XT_BYTES + 32768 + ((i) & 1) * 8192 + (g * 32 + (lane & 15)) * 16; asm volatile("" : "+v"(vb_)); \
        _Pragma("unroll") for (int tt = 0; tt < 2; ++tt) _Pragma("unroll") for (int ks = 0; ks < 4; ++ks) vf[tt][ks] = lds_ld16(lds + vb_, ks * 2048 + tt * 256); \
        f32x4 d0[2], d1[2]; \
        _Pragma("unroll") for (int bq = 0; bq <= 5; ++bq) { \
            if (bq < 5) { \
                d0[bq & 1] = zero4(); d1[bq & 1] = zero4(); \
                _Pragma("unroll") for (int ks = 0; ks < 4; ++ks) { d0[bq & 1] = mf16(vf[0][ks], wo[bq < 5 ? bq : 0][ks], d0[bq & 1]); d1[bq & 1] = mf16(vf[1][ks], wo[bq < 5 ? bq : 0][ks], d1[bq & 1]); } \
            } \
            __builtin_amdgcn_sched_barrier(0); \
            if (bq > 0) { const f32x4 e0 = d0[(bq - 1) & 1], e1 = d1[(bq - 1) & 1]; \
                st16_sc1(u32x4{pk_bf16(e0[0], e0[1]), pk_bf16(e0[2], e0[3]), pk_bf16(e1[0], e1[1]), pk_bf16(e1[2], e1[3])}, ru, lane * 16, (vu_seg(par, i) * NCT * H + h) * 1024 + (8 * (bq - 1) + wave) * 32768); } \
            if (bq < 5) { MID; } \
            __builtin_amdgcn_sched_barrier(0); \
        } \
    } while (0)
#define VU_XP stage_x1(rx, vu_seg(par, i + 1), XS2(i + 1), wave, lane, bq)
#define VU_LOOP() do { \
          \
        stage_x(rx, par + 2, XS2(1), wave, lane); \
        WAIT_VM(5); raw_barrier(); \
        VU_P1(0, (void)0); raw_barrier(); \
        stage_x(rx, par + 4, XS2(0), wave, lane); \
        VU_FIN(0); raw_barrier(); \
        VU_P2(0, (void)0); \
        WAIT_VM(10);   raw_barrier(); \
        VU_P1(1, (void)0); \
        WAIT_VM(0); raw_barrier(); \
        if (tid == 0) flag_set(p, FL_GU + 2 * h + par, 1); \
        { const int i = 2; VU_FIN(1); VU_P1(2, VU_XP); WAIT_VM(0); raw_barrier(); } \
          \
        for (int i = 3; i < nit; ++i) { \
            if (KH) { VU_FIN(i - 1); VU_P2(i - 2, VU_XP); VU_P1(i, (void)0); if (i <= 4) WAIT_VM(0); else WAIT_VM(1); } \
            else { VU_P1(i, do { VU_XP; if (bq == 1) VU_FIN(i - 1); } while (0)); VU_P2(i - 2, (void)0); if (i <= 4) WAIT_VM(0); else WAIT_VM(5); } \
            raw_barrier(); \
            if (tid == 0 && i != 5) flag_set(p, FL_GU + 2 * h + par, i <= 4 ? i - 1 : i - 2); \
        } \
        VU_FIN(nit - 1); \
        for (int i = nit - 2; i < nit; ++i) { VU_P2(i, (void)0); raw_barrier(); } \
    } while (0)
    if (kh) { constexpr bool KH = true; VU_LOOP(); } else { constexpr bool KH = false; VU_LOOP(); }
    WAIT_VM(0);
    raw_barrier();
    if (tid == 0) flag_set(p, FL_GU + 2 * h + par, nit);
}

DEV unsigned now_rt() { return (unsigned)__builtin_amdgcn_s_memrealtime(); }
DEV unsigned clamp_rt(unsigned t) { const unsigned n = now_rt(); const int d = (int)(t - n); return (d > 400 || d < 0) ? n : t; }
DEV void wait_rt(unsigned t) {
    int d;
    while ((d = (int)(t - now_rt())) > 3) {}
    if (d > 0) asm volatile("s_nop 3");
    if (d > 1) asm volatile("s_nop 3");
    if (d > 2) asm volatile("s_nop 3");
    asm volatile("" ::: "memory");
}
#define PIN_S(x) do { x = __builtin_amdgcn_readfirstlane(x); asm volatile("" : "+s"(x)); } while (0)
#define SB() __builtin_amdgcn_sched_barrier(0)
DEV unsigned mx5(unsigned m, u32x4 v) { return max(max(max(m, v.x), v.y), max(v.z, v.w)); }
DEV unsigned read_ann(__amdgpu_buffer_rsrc_t r, unsigned soff, int cap) {
    unsigned v = 0;
    for (int sp = 0; sp <= cap; ++sp) {
        asm volatile("" ::: "memory");
        v = __builtin_amdgcn_readfirstlane(__builtin_amdgcn_raw_buffer_load_b32(r, 0, soff, 16));
        if (v != SENT) break;
    }
    return v;
}
DEV float wave_max(float x) {
    x = fmaxf(x, u2f(__builtin_amdgcn_update_dpp(0, f2u(x), 0x128, 0xf, 0xf, true)));
    x = fmaxf(x, u2f(__builtin_amdgcn_update_dpp(0, f2u(x), 0x124, 0xf, 0xf, true)));
    x = fmaxf(x, u2f(__builtin_amdgcn_update_dpp(0, f2u(x), 0x122, 0xf, 0xf, true)));
    x = fmaxf(x, u2f(__builtin_amdgcn_update_dpp(0, f2u(x), 0x121, 0xf, 0xf, true)));
    return xmax16_32(x);
}
DEV void dma_mem(__amdgpu_buffer_rsrc_t rc0, char* mimg, int lane, unsigned oa, unsigned ob) {
    const unsigned oa2 = oa + 4096;
    __builtin_amdgcn_raw_ptr_buffer_load_lds(rc0, (LAS void*)mimg, 16, lane * 16, oa, 0, 16);
    __builtin_amdgcn_raw_ptr_buffer_load_lds(rc0, (LAS void*)mimg, 16, lane * 16, oa, 1024, 16);
    __builtin_amdgcn_raw_ptr_buffer_load_lds(rc0, (LAS void*)mimg, 16, lane * 16, oa, 2048, 16);
    __builtin_amdgcn_raw_ptr_buffer_load_lds(rc0, (LAS void*)mimg, 16, lane * 16, oa, 3072, 16);
    __builtin_amdgcn_raw_ptr_buffer_load_lds(rc0, (LAS void*)(mimg + 4096), 16, lane * 16, oa2, 0, 16);
    __builtin_amdgcn_raw_ptr_buffer_load_lds(rc0, (LAS void*)(mimg + 4096), 16, lane * 16, oa2, 1024, 16);
    __builtin_amdgcn_raw_ptr_buffer_load_lds(rc0, (LAS void*)(mimg + 4096), 16, lane * 16, oa2, 2048, 16);
    __builtin_amdgcn_raw_ptr_buffer_load_lds(rc0, (LAS void*)(mimg + 4096), 16, lane * 16, oa2, 3072, 16);
    __builtin_amdgcn_raw_ptr_buffer_load_lds(rc0, (LAS void*)(mimg + 8192), 16, lane * 16, ob, 0, 16);
    __builtin_amdgcn_raw_ptr_buffer_load_lds(rc0, (LAS void*)(mimg + 8192), 16, lane * 16, ob, 1024, 16);
}
DEV void fill_mimg(char* mimg, int lane) {
    WAIT_VM(0);
#pragma unroll
    for (int i = 0; i < 10; ++i) lds_st16(mimg, i * 1024 + lane * 16, u32x4{SENT, SENT, SENT, SENT});
    __builtin_amdgcn_s_waitcnt(0xC07F);
}
DEV bf16x16 cat8(u32x4 a, u32x4 b) { u32x8 r = {a.x, a.y, a.z, a.w, b.x, b.y, b.z, b.w}; return __builtin_bit_cast(bf16x16, r); }
#define RDF(i) af[i] = lds_ld16(mimg, (i) * 1024 + lane * 16)
DEV bool any_nan(float a, float b) { return __builtin_amdgcn_fcmpf(a, b, 8  ) != 0; }
#define PROJ_READ(WAITN, TICK) do { \
        WS(0); \
        WAITN(4); \
        for (int sp_ = 0;; ++sp_) { \
            asm volatile("" ::: "memory"); \
            const unsigned pr_ = lds_vol_ld<unsigned>(mimg + (lane < NCT ? lane : 0) * 256 + 128 + (lane & 31) * 4); \
            af[4] = lds_vol_ld<u32x4>(mimg + 4096 + lane * 16); af[5] = lds_vol_ld<u32x4>(mimg + 5120 + lane * 16); \
            if (__all(pr_ != SENT)) break; \
            asm volatile(""); \
            if (sp_ > cap) break; \
            TICK; \
        } \
        if (wave == 0 && ISQK) MT(tsp); \
        WS(1); \
    } while (0)
#define PROJ_MM(DO) do { RDF(0); RDF(1); SB(); DO(4); SB(); RDF(2); RDF(3); RDF(6); SB(); DO(5); SB(); RDF(7); RDF(8); RDF(9); SB(); \
        DO(0); DO(1); DO(2); DO(3); DO(6); DO(7); DO(8); DO(9); SB(); } while (0)
#define WAIT_VALID(TICK) do { \
        for (int sp_ = 0;; ++sp_) { \
            asm volatile("" ::: "memory"); \
            unsigned m_ = 0; \
            _Pragma("unroll") for (int st = 0; st < 10; ++st) m_ = mx5(m_, lds_ld16(mimg, st * 1024 + lane * 16)); \
            if (__all(m_ != SENT)) break; \
            if (sp_ > cap) { flag_set(p, FL_TMO, 1u); cap = 0; break; } \
            TICK; \
        } } while (0)
#define NOWAIT(n) (void)0
#define POLL_TICK do { if (tick == tnext) { tnext += 6; if (tick > 24) WAIT_VM(0); dma_mem(rc0, mimg, lane, oa, ob); } ++tick; } while (0)

constexpr float SCALE = 0.08838834764831845f;
constexpr float LOG2E = 1.4426950408889634f;
constexpr float MASKV = -999292928.0f;
DEV void round_scores(f32x4& d) {
    unsigned a = pk_bf16(d[0], d[1]), b = pk_bf16(d[2], d[3]);
    a = pk_bf16(bf_lo(a) * SCALE, bf_hi(a) * SCALE); b = pk_bf16(bf_lo(b) * SCALE, bf_hi(b) * SCALE);
    d = f32x4{bf_lo(a), bf_hi(a), bf_lo(b), bf_hi(b)};
}
DEV float max4(f32x4 d) { return fmaxf(fmaxf(d[0], d[1]), fmaxf(d[2], d[3])); }
DEV void exp4(f32x4& d, float mx) {
#pragma unroll
    for (int i = 0; i < 4; ++i) d[i] = __builtin_amdgcn_exp2f((d[i] - mx) * LOG2E);
}
DEV float sum4(f32x4 d) { return (d[0] + d[1]) + (d[2] + d[3]); }
DEV unsigned pk_pair(f32x4 d) { return pk_bf16(d[0] + d[2], d[1] + d[3]); }

DEV void role_qk(const Params& p, int h, char* lds) {
    constexpr bool ISQK = true; (void)ISQK;
    const int tid = threadIdx.x, lane = tid & 63, wave = __builtin_amdgcn_readfirstlane(tid >> 6);
    const int g = lane >> 4, l16 = lane & 15;
    unsigned* ctrl = (unsigned*)(p.ws + OFF_CTRL) + TUNE_W; (void)ctrl;
    bf16x16 wq[10], wk[10];
    const __amdgpu_buffer_rsrc_t rw0 = mkrsrc(p.wq, WBYTES), rw1 = mkrsrc(p.wk, WBYTES);
    for (int i = 0; i < TUNEW(8, 0); ++i) __builtin_amdgcn_s_sleep(2);
#define AFTER_W (void)0
    STAGE_RING(5, 0,
        do { stage_img<64>(rw0, bt * 64 * H * DH + h * DH, H * DH, img, wave, lane); stage_img<64>(rw1, bt * 64 * H * DH + h * DH, H * DH, img + 16384, wave, lane); } while (0),
        load_bsp2<true>(wq[bt], wk[bt], img, wave, img + 16384, wave, lane));
    TLR(h, 2);
    int cap = SPIN_CAP;
    const unsigned side0 = (flags_wait(p, FL_SIDE, 0, 1, 1u, lane, cap) == 2u ? 1u : 0u) << 13;
    flags_wait(p, FL_READY, 0, 1, NCT, lane, cap);
    const __amdgpu_buffer_rsrc_t rc0 = mkrsrc(p.ws + OFF_COMM0 + __builtin_amdgcn_readfirstlane(side0), 2 * MB - 8192);
    const __amdgpu_buffer_rsrc_t rq = mkrsrc(p.ws + OFF_QX, 32 * MB);
    const __amdgpu_buffer_rsrc_t rpx = mkrsrc(p.ws + OFF_PX, 5 * MB);
    char* mimg = lds;
    char* ximg = lds + 40960;
    char* misc = lds + 46080;
    unsigned* hist = (unsigned*)(misc + 32);
    char* qk = lds + 49152;
    const int idx = (lane & 2) ? 0xEEEE : 0x4444;
    unsigned long long tsp = 0; (void)tsp;
    WSDECL;
    {
        TLR(h, 3);
        flags_wait(p, FL_GQ + 2 * h, 0, 1, 1u, lane, cap);
        TLR(h, 4);
        u32x4 a = ld16_sc1(rq, tid * 16, h * 8192), b = ld16_sc1(rq, tid * 16, 16 * MB + h * 8192);
        lds_st16(qk, tid * 16, a); lds_st16(qk, 8192 + tid * 16, b);
        if (tid == 0) { lds_vol_st<int>(misc, 90); lds_vol_st<int>(misc + 4, 60); lds_vol_st<int>(misc + 8, 0); lds_vol_st<int>(misc + 12, 0); }
    }
    raw_barrier();
    u32x4 af[10];
    f32x4 aq, ak;
    char* xw = ximg + (16 * wave + l16) * 32 + g * 4;
#define Q_DO(st) aq = smf(af[st], wq[st], aq, idx)
#define K_DO(st) ak = smf(af[st], wk[st], ak, idx)
#define QK_DO(st) do { Q_DO(st); K_DO(st); } while (0)
#define QK_ALL() do { QK_DO(4); QK_DO(5); QK_DO(0); QK_DO(1); QK_DO(2); QK_DO(3); QK_DO(6); QK_DO(7); QK_DO(8); QK_DO(9); } while (0)
#define QK_PROJ(WAITN, TICK, HOOK, HOOK2) do { \
        float q0_, q1_; \
        for (int pass_ = 0;; ++pass_) { \
            aq = zero4(); ak = zero4(); asm volatile("" : "+v"(aq), "+v"(ak)); \
            PROJ_READ(WAITN, TICK); \
            HOOK; \
            PROJ_MM(QK_DO); \
            HOOK2; \
            q0_ = aq[0] + aq[2]; q1_ = aq[1] + aq[3]; \
            if (!any_nan(q0_, q1_)) break; \
            asm volatile(""); \
            if (pass_) break; \
            WAIT_VALID(TICK); \
        } \
        *(unsigned*)xw = pk_bf16(q0_, q1_); *(unsigned*)(xw + 16) = pk_pair(ak); \
        WS(4); } while (0)
#define PF_DECL const unsigned o = (tid - 64) * 16, so = (min(s + 1, NSEG - 1) * H + h) * 8192; u32x4 a0, a1, b0, b1; unsigned gv
#define QK_PF do { a0 = ld16_sc1(rq, o, so); a1 = ld16_sc1(rq, o + 4096, so); b0 = ld16_sc1(rq, o, 16 * MB + so); b1 = ld16_sc1(rq, o + 4096, 16 * MB + so); gv = flag_get(p, FL_GQ + 2 * h); SB(); } while (0)
#define PF_STORE do { char* nq = qk + ((s + 1) & 1) * 16384; lds_st16(nq, o, a0); lds_st16(nq, o + 4096, a1); lds_st16(nq, 8192 + o, b0); lds_st16(nq, 8192 + 4096 + o, b1); } while (0)
#define PF_CHECK do { if (s + 2 < NSEG && !__all(gv >= (unsigned)s + 3u)) flags_wait(p, FL_GQ + 2 * h, 0, 1, s + 3, lane, cap); } while (0)
    if (wave == 0) {
        const unsigned gq = l16 >> 2, r = l16 & 3, key0 = 16 * (r >> 1) + 8 * (gq >> 1) + 2 * (gq & 1) + (r & 1);
        const unsigned koff = 8192 + (g * 32 + key0) * 16;
        const bool hi8 = l16 >= 8;
        const unsigned px_off = ((g >> 1) * 16 + rowi(l16 & 7, g & 1)) * 16 + (l16 >> 3) * 8;
        const unsigned pm_off = rowi(l16 & 7, g & 1) * 16 + (h & 1) * 8 + (g >> 1) * 4 + (l16 >> 3) * 4096;
        const unsigned foff = (8 * g + (l16 >> 2)) * 32 + (lane & 1) * 8;
        unsigned long long t1 = 0, t2 = 0, t3 = 0, t4 = 0; unsigned a12 = 0, a23 = 0, a34 = 0, a01 = 0; (void)t1; (void)t2; (void)t3; (void)t4; (void)a12; (void)a23; (void)a34; (void)a01;
        for (int s = 0; s < NSEG; ++s) {
            const char* kimg = qk + (s & 1) * 16384 + koff;
            unsigned opx = physo(px_base(s & 3, 0) + h * 512), opx1 = opx + 32768, opm = physo(pm_base(s & 3, 0) + (h >> 1) * 256);
            PIN_S(opx); PIN_S(opx1); PIN_S(opm);
            raw_barrier();
            MT(t1);
            u32x4 Fq[4], Fk[4], kf[8];
#define RD_KF do { _Pragma("unroll") for (int ks = 0; ks < 4; ++ks) { kf[2 * ks] = lds_ld16(kimg, ks * 2048); kf[2 * ks + 1] = lds_ld16(kimg, ks * 2048 + 64); } SB(); } while (0)
            QK_PROJ(NOWAIT, (void)0, (void)0, RD_KF);
            MT(t2);
            raw_barrier();
            MT(t3); WS(5);
            if (s + 1 < NSEG) {
#pragma unroll
                for (int ks = 0; ks < 4; ++ks) { u32x2 a = tr_read(ximg + foff, ks * 1024), b = tr_read(ximg + foff, ks * 1024 + 128); Fq[ks] = u32x4{a.x, a.y, b.x, b.y}; }
#pragma unroll
                for (int ks = 0; ks < 4; ++ks) { u32x2 c = tr_read(ximg + foff, ks * 1024 + 16), d = tr_read(ximg + foff, ks * 1024 + 128 + 16); Fk[ks] = u32x4{c.x, c.y, d.x, d.y}; }
                SB();
                f32x4 d0 = mf16(kf[0], Fq[0], zero4()), d1 = mf16(kf[1], Fq[0], zero4());
                d0 = mf16(kf[2], Fq[1], d0); d1 = mf16(kf[3], Fq[1], d1);
                d0 = mf16(kf[4], Fq[2], d0); d1 = mf16(kf[5], Fq[2], d1); d0 = mf16(kf[6], Fq[3], d0); d1 = mf16(kf[7], Fq[3], d1);
                SB();
                f32x4 dm = mf16(Fk[0], Fq[0], zero4());
                dm = mf16(Fk[1], Fq[1], dm); dm = mf16(Fk[2], Fq[2], dm); dm = mf16(Fk[3], Fq[3], dm);
                SB();
                float x0 = hi8 ? d1[0] : d0[0], x1 = hi8 ? d1[1] : d0[1], x2 = hi8 ? d1[2] : d0[2], x3 = hi8 ? d1[3] : d0[3];
                float mx = max3f(max3f(x0, x1, x2), x3, x3);
                mx = max3f(mx, mx, u2f(__builtin_amdgcn_update_dpp(0, f2u(mx), 0x128, 0xf, 0xf, true)));
                SB();
                float y0 = g >= 2 ? dm[2] : dm[0], y1 = g >= 2 ? dm[3] : dm[1];
                mx = xmax16_32(max3f(mx, y0, y1));
                constexpr float CS = SCALE * LOG2E;
                const float nm = -mx * CS;
                x0 = __builtin_amdgcn_exp2f(__builtin_fmaf(x0, CS, nm)); x1 = __builtin_amdgcn_exp2f(__builtin_fmaf(x1, CS, nm));
                x2 = __builtin_amdgcn_exp2f(__builtin_fmaf(x2, CS, nm)); x3 = __builtin_amdgcn_exp2f(__builtin_fmaf(x3, CS, nm));
                y0 = __builtin_amdgcn_exp2f(__builtin_fmaf(y0, CS, nm)); y1 = __builtin_amdgcn_exp2f(__builtin_fmaf(y1, CS, nm));
                float sm = (x0 + x1) + (x2 + x3);
                sm = sm + u2f(__builtin_amdgcn_update_dpp(0, f2u(sm), 0x128, 0xf, 0xf, true)) + (y0 + y1);
                const float inv = __builtin_amdgcn_rcpf(xsum16_32(sm));
                const u32x2 pxv = {nosent(pk_bf16(x0 * inv, x1 * inv)), nosent(pk_bf16(x2 * inv, x3 * inv))};
                st8_sc1(pxv, rc0, px_off, opx);
                st4_sc1(nosent(pk_bf16(y0 * inv, y1 * inv)), rc0, pm_off, opm);
                st8_sc1(pxv, rc0, px_off, opx1);
                MT(t4); WS(7); WSDUMP();
                { const unsigned ta = now_rt(); if (lane == 0) st4_sc1(nosent(ta), rc0, s * 4, physo(STA_BASE + h * 256)); }
                MTW(); PACC(a12, t1, t2); PACC(a23, t2, t3); PACC(a34, t3, t4); PACC(a01, t1, tsp); PDUMP(h, a12, a23, a34, a01);
            }
        }
    } else if (wave <= 2) {
        const int tt = wave - 1;
        const int tok = 16 * tt + l16;
        flags_wait(p, FL_GQ + 2 * h, 0, 1, 2u, lane, cap);
        for (int s = 0; s < NSEG; ++s) {
            PF_DECL;
            raw_barrier();
            QK_PROJ(NOWAIT, (void)0, QK_PF, (void)0);
            raw_barrier();
            WS(7); WSDUMP();
            __builtin_amdgcn_s_sleep(2);
            WAIT_VM(5);
            if (lane == 0 && s > 0 && s <= NSEG - 4) flag_set(p, FL_PX + 2 * h + tt, s);
            const char* kimg = qk + (s & 1) * 16384 + 8192 + (g * 32 + l16) * 16;
            const char* qimg = qk + (s & 1) * 16384 + (g * 32 + tok) * 16;
            f32x4 d0 = {}, d1 = {}, dm = {};
#pragma unroll
            for (int ks = 0; ks < 4; ++ks) {
                const unsigned e0 = 32 * ks + 8 * g + (l16 >> 2);
                u32x2 a = tr_read(ximg, e0 * 32 + (lane & 3) * 8), b = tr_read(ximg, (e0 + 4) * 32 + (lane & 3) * 8);
                const u32x4 qf = lds_ld16(qimg, ks * 2048);
                d0 = mf16(lds_ld16(kimg, ks * 2048), qf, d0);
                d1 = mf16(lds_ld16(kimg, ks * 2048 + 256), qf, d1);
                dm = mf16(u32x4{a.x, a.y, b.x, b.y}, qf, dm);
            }
            round_scores(d0); round_scores(d1); round_scores(dm);
#pragma unroll
            for (int r = 0; r < 4; ++r) { if (4 * g + r > tok) d0[r] = MASKV; if (16 + 4 * g + r > tok) d1[r] = MASKV; }
            if (g < 2) dm = f32x4{-3.0e38f, -3.0e38f, -3.0e38f, -3.0e38f};
            const float mx = xmax16_32(fmaxf(fmaxf(max4(d0), max4(d1)), max4(dm)));
            exp4(d0, mx); exp4(d1, mx); exp4(dm, mx);
            const float inv = 1.0f / xsum16_32(sum4(d0) + sum4(d1) + sum4(dm));
            u32x4 v = {pk_bf16(d0[0] * inv, d0[1] * inv), pk_bf16(d0[2] * inv, d0[3] * inv), pk_bf16(d1[0] * inv, d1[1] * inv), pk_bf16(d1[2] * inv, d1[3] * inv)};
            st16_sc1(v, rpx, lane * 16, ((s * H + h) * 2 + tt) * 1024);
            if (g >= 2) {
                st4_sc1(pk_bf16(dm[0] * inv, dm[1] * inv), rpx, tok * 16 + (g - 2) * 4, 4 * MB + (s * H + h) * 512);
                st4_sc1(pk_bf16(dm[2] * inv, dm[3] * inv), rpx, tok * 16 + 8 + (g - 2) * 4, 4 * MB + (s * H + h) * 512);
            }
            PF_STORE;
            if (s >= NSEG - 4) { WAIT_VM(0); if (lane == 0) flag_set(p, FL_PX + 2 * h + tt, s + 1); }
            PF_CHECK;
        }
    } else if (wave <= 4) {
        flags_wait(p, FL_GQ + 2 * h, 0, 1, 2u, lane, cap);
        const float mg1 = 0.25f * (float)TUNEW(0, 48), mg2 = 0.25f * (float)TUNEW(1, 16);
        float d1e = 90.0f, d2e = 60.0f;
        for (int s = 0; s < NSEG; ++s) {
            PF_DECL;
            raw_barrier();
            QK_PROJ(NOWAIT, (void)0, QK_PF, (void)0);
            raw_barrier();
            WS(7); WSDUMP();
            __builtin_amdgcn_s_sleep(6);
            WAIT_VM(0);
            PF_STORE;
            if (h == 0 && wave == 4 && s >= 2) {
                const unsigned tb = hist[2 * (s - 1)], tx = hist[2 * (s - 1) + 1];
                const unsigned va = __builtin_amdgcn_raw_buffer_load_b32(rc0, (lane & 31) * 256, physo(STA_BASE) + (s - 1) * 4, 16);
                const unsigned vb = __builtin_amdgcn_raw_buffer_load_b32(rc0, physo((lane < NCT ? lane : 0) * 256), physo(STB_BASE) + (s - 1) * 4, 16);
                if (__all(va != SENT && vb != SENT)) {
                    const float a = wave_max((float)(int)(va - tb)), b = wave_max((float)(int)(vb - tx));
                    if (tb != SENT) d1e += 0.5f * (fminf(a, d1e + 2.0f) - d1e);
                    d2e += 0.5f * (fminf(b, d2e + 2.0f) - d2e);
                    if (lane == 0) { lds_vol_st<int>(misc, (int)(d1e + mg1)); lds_vol_st<int>(misc + 4, (int)(d2e + mg2)); }
                }
            }
            if (wave == 3 && s >= 1) {
                st16_sc1(u32x4{SENT, SENT, SENT, SENT}, rc0, (lane & 31) * 16 + (lane >> 5) * 32768, physo(px_base((s - 1) & 3, 0) + h * 512));
                if (lane < 32) st8_sc1(u32x2{SENT, SENT}, rc0, l16 * 16 + (h & 1) * 8 + (g & 1) * 4096, physo(pm_base((s - 1) & 3, 0) + (h >> 1) * 256));
            }
            PF_CHECK;
        }
    } else if (wave < 7) {
        const int tfirst = wave == 6 ? TUNEW(5, 0) : TUNEW(5, 0) + 2;
        for (int s = 0; s < NSEG; ++s) {
            unsigned oa = physo(mema_off(s & 3, 0)), ob = physo(memb_off(s & 3, 0));
            PIN_S(oa); PIN_S(ob);
            int tick = 0, tnext = tfirst;
            raw_barrier();
            QK_PROJ(NOWAIT, POLL_TICK, (void)0, (void)0);
            raw_barrier();
            WS(7); WSDUMP();
            WAIT_VM(0);
            if (lane == 0) lds_vol_st<int>(misc + 8 + (wave - 5) * 4, s + 1);
        }
    } else {
        int nfail = 0;
        const int tfirst = TUNEW(5, 0) + 4, gap2 = TUNEW(6, 3);
        for (int s = 0; s < NSEG; ++s) {
            const unsigned tm0 = s > 0 ? read_ann(rc0, physo(ANN_BASE) + (s - 1) * 128 + 4, cap) : 0u;
            while (lds_vol_ld<int>(misc + 8) < s || lds_vol_ld<int>(misc + 12) < s) {}
            fill_mimg(mimg, lane);
            unsigned oa = physo(mema_off(s & 3, 0)), ob = physo(memb_off(s & 3, 0)), oann = physo(ANN_BASE + s * 128);
            PIN_S(oa); PIN_S(ob); PIN_S(oann);
            const unsigned tm = s > 0 ? clamp_rt(tm0) : now_rt();
            const int dd1 = __builtin_amdgcn_readfirstlane(lds_vol_ld<int>(misc)), dd2 = __builtin_amdgcn_readfirstlane(lds_vol_ld<int>(misc + 4));
            int tick = 0, tnext = tfirst;
            wait_rt(tm);
            dma_mem(rc0, mimg, lane, oa, ob);
            const unsigned c0 = (unsigned)__builtin_amdgcn_s_memtime();
            for (int i = 0; i < gap2; ++i) asm volatile("s_nop 7");
            dma_mem(rc0, mimg, lane, oa, ob);
            WAIT_VM(12);
            raw_barrier();
#define QK_ANN do { if (h == 0) { \
                const unsigned te = tm + ((((unsigned)__builtin_amdgcn_s_memtime() - c0) * 2731u) >> 16), x1 = nosent(te + dd1), x2 = nosent(x1 + dd2); \
                if (lane == 0) { st8_sc1(u32x2{x1, x2}, rc0, 0, oann); hist[2 * s] = pass_ ? SENT : te; hist[2 * s + 1] = x1; } } } while (0)
            QK_PROJ(NOWAIT, POLL_TICK, QK_ANN, (void)0);
            raw_barrier();
            WS(7); WSDUMP();
            PDUMP(32 + h, c1 - c0, c2 - c1, (c0 - cpre) - (tm - rpre) * 24, tm - rpre);
            if (s >= 4) nfail += tick;
        }
    }
}

DEV void role_vo(const Params& p, int h, char* lds) {
    constexpr bool ISQK = false; (void)ISQK;
    const int tid = threadIdx.x, lane = tid & 63, wave = __builtin_amdgcn_readfirstlane(tid >> 6);
    const int g = lane >> 4, l16 = lane & 15, j4 = wave & 3;
    bf16x16 w[20];
    const __amdgpu_buffer_rsrc_t rw0 = mkrsrc(p.wv, WBYTES), rw1 = mkrsrc(p.wo, WBYTES);
    for (int i = 0; i < TUNEW(8, 0); ++i) __builtin_amdgcn_s_sleep(2);
    STAGE_RING(5, 0,
        do { stage_img<64>(rw0, bt * 64 * H * DH + h * DH, H * DH, img, wave, lane); stage_img<64>(rw1, (h * DH + (bt & 1) * 64) * D + (bt >> 1) * 128, D, img + 16384, wave, lane); } while (0),
        do { if (wave < 4) load_bsp2<true>(w[4 * (bt >> 1) + (bt & 1)], w[4 * (bt >> 1) + 2 + (bt & 1)], img, 2 * j4, img, 2 * j4 + 1, lane);
             else load_bsp2<false>(w[4 * (bt >> 1) + (bt & 1)], w[4 * (bt >> 1) + 2 + (bt & 1)], img + 16384, 2 * j4, img + 16384, 2 * j4 + 1, lane); } while (0));
#undef AFTER_W
    TLR(32 + h, 2);
    int cap = SPIN_CAP;
    const unsigned long long sides = wait_sides(p, lane, cap, 3);
    unsigned long long tmask = sides >> 1;
    flags_wait(p, FL_READY, 0, 1, NCT, lane, cap);
    const __amdgpu_buffer_rsrc_t rc0 = mkrsrc(p.ws + OFF_COMM0 + (((unsigned)sides & 1u) << 13), 2 * MB - 8192);
    const __amdgpu_buffer_rsrc_t rt = mkrsrc(p.ws + OFF_T, 44 * MB);
    char* mimg = lds;
    char* vimg = lds + 40960;
    const int idx = (lane & 2) ? 0xEEEE : 0x4444;
    unsigned long long tsp = 0; (void)tsp;
    WSDECL;
    if (wave < 4) {
        u32x4 af[10];
        f32x4 a0, a1;
        const unsigned e_ = 32 * j4 + l16;
        char* vw = vimg + (2 * (e_ >> 2) + (e_ & 1)) * 32 + (4 * g + 2 * ((e_ >> 1) & 1)) * 2;
#define VW(st, tl) w[4 * ((st) >> 1) + 2 * (tl) + ((st) & 1)]
#define VO_DO(st) do { a0 = smf(af[st], VW(st, 0), a0, idx); a1 = smf(af[st], VW(st, 1), a1, idx); } while (0)
        for (int s = 0; s < NSEG; ++s) {
            raw_barrier();
            float v0_, v1_;
            for (int pass_ = 0;; ++pass_) {
                a0 = zero4(); a1 = zero4(); asm volatile("" : "+v"(a0), "+v"(a1));
                PROJ_READ(NOWAIT, (void)0);
                PROJ_MM(VO_DO);
                v0_ = a0[0] + a0[2]; v1_ = a0[1] + a0[3];
                if (!any_nan(v0_, v1_)) break;
                asm volatile("");
                if (pass_) break;
                WAIT_VALID((void)0);
            }
            *(unsigned*)vw = pk_bf16(v0_, v1_); *(unsigned*)(vw + 256) = pk_pair(a1);
            WS(4);
            raw_barrier();
            WS(5); WSDUMP();
        }
    } else {
        const int gap2 = TUNEW(6, 3);
        const unsigned vr_off = (8 * g + (l16 >> 2)) * 32 + (lane & 3) * 8;
        const unsigned t_voff = (8 * (g >> 1) + 2 * j4 + (g & 1)) * 16384 + l16 * 16, t_voff2 = g < 2 ? (32 + 2 * j4 + g) * 16384 + l16 * 16 : 0x7FFFFFF0u;
        for (int s = 0; s < NSEG; ++s) {
            if (wave == 7) fill_mimg(mimg, lane);
            unsigned ot = t_off(s, tmask) + h * 256, ot2 = ot + 16 * 16384;
            unsigned oa = physo(mema_off(s & 3, 0)), ob = physo(memb_off(s & 3, 0));
            PIN_S(oa); PIN_S(ob); PIN_S(ot); PIN_S(ot2);
            if (wave == 7) {
                if (s > 0) wait_rt(clamp_rt(read_ann(rc0, physo(ANN_BASE + (s - 1) * 128 + 4), cap)));
                dma_mem(rc0, mimg, lane, oa, ob);
                for (int i = 0; i < gap2; ++i) asm volatile("s_nop 7");
                asm volatile("s_nop 7");
                dma_mem(rc0, mimg, lane, oa, ob);
                WAIT_VM(12);
            }
            raw_barrier();
            WS(0);
            {
                int tick = 0, tnext = 3 * (j4 ^ 3);
                for (int sp = 0; sp <= cap; ++sp) {
                    const unsigned pr = lds_vol_ld<unsigned>(mimg + (lane < NCT ? lane : 0) * 256 + 128 + (lane & 31) * 4);
                    if (__all(pr != SENT)) break;
                    if (tick == tnext) { tnext += 12; WAIT_VM(0); dma_mem(rc0, mimg, lane, oa, ob); }
                    ++tick;
                }
                if (wave != 7) WAIT_VM(0);
            }
            if (wave == 7) {
                for (int sp = 0;; ++sp) {
                    WAIT_VM(0);
                    unsigned m = 0;
#pragma unroll
                    for (int st = 0; st < 10; ++st) m = mx5(m, lds_ld16(mimg, st * 1024 + lane * 16));
                    if (__all(m != SENT)) break;
                    if (sp > cap) { flag_set(p, FL_TMO, 1u); cap = 0; break; }
                    dma_mem(rc0, mimg, lane, oa, ob);
                }
            }
            f32x4 d[10];
#pragma unroll
            for (int i = 0; i < 10; ++i) { d[i] = zero4(); asm volatile("" : "+v"(d[i])); }
            raw_barrier();
            WS(5);
            u32x4 A[2];
#pragma unroll
            for (int ks = 0; ks < 2; ++ks) { u32x2 a = tr_read(vimg + vr_off, ks * 1024), b = tr_read(vimg + vr_off, ks * 1024 + 128); A[ks] = u32x4{a.x, a.y, b.x, b.y}; }
            unsigned tw[10];
#define T_MM(i) do { d[i] = smf(A[0], w[2 * (i)], d[i], idx); d[i] = smf(A[1], w[2 * (i) + 1], d[i], idx); } while (0)
#define T_PK(i) tw[i] = nosent(pk_bf16(d[i][0] + d[i][2], d[i][1] + d[i][3]))
#define T_ST4(i, so) do { \
                auto a = __builtin_amdgcn_permlane16_swap(tw[i], tw[(i) + 1], false, false); \
                auto b = __builtin_amdgcn_permlane16_swap(tw[(i) + 2], tw[(i) + 3], false, false); \
                auto c = __builtin_amdgcn_permlane32_swap(a[0], b[0], false, false); \
                auto e = __builtin_amdgcn_permlane32_swap(a[1], b[1], false, false); \
                st16_sc1(u32x4{c[0], c[1], e[0], e[1]}, rt, t_voff, so); } while (0)
            T_MM(0); T_MM(1); SB();
            T_MM(2); T_PK(0); SB(); T_MM(3); T_PK(1); SB(); T_MM(4); T_PK(2); SB(); T_MM(5); T_PK(3); SB();
            T_MM(6); T_ST4(0, ot); SB(); T_MM(7); T_PK(4); T_PK(5); SB(); T_MM(8); T_PK(6); SB(); T_MM(9); T_PK(7); SB();
            T_ST4(4, ot2); T_PK(8); T_PK(9);
            {
                auto a = __builtin_amdgcn_permlane16_swap(tw[8], tw[9], false, false);
                auto c = __builtin_amdgcn_permlane32_swap(a[0], a[0], false, false);
                auto e = __builtin_amdgcn_permlane32_swap(a[1], a[1], false, false);
                st16_sc1(u32x4{c[0], c[1], e[0], e[1]}, rt, t_voff2, ot);
            }
            if (s == 2) tmask = wait_sides(p, lane, cap) >> 1;
            WS(7); WSDUMP();
        }
    }
}

DEV void role_comb(const Params& p, int c, char* lds) {
    const int tid = threadIdx.x, lane = tid & 63, wave = __builtin_amdgcn_readfirstlane(tid >> 6);
    const int g = lane >> 4, l16 = lane & 15;
    unsigned* ctrl = (unsigned*)(p.ws + OFF_CTRL) + TUNE_W; (void)ctrl;
    const __amdgpu_buffer_rsrc_t rt = mkrsrc(p.ws + OFF_T, 44 * MB), ru = mkrsrc(p.ws + OFF_U, 80 * MB);
    const int idx = (lane & 2) ? 0xEEEE : 0x4444;
    int cap = SPIN_CAP;
    const u32x4 sv = {SENT, SENT, SENT, SENT};
    {
        const __amdgpu_buffer_rsrc_t r = mkrsrc(p.ws + OFF_COMM0, COMM_CHUNKS * 16384);
        for (unsigned i = (c * 512 + tid) * 16; i < COMM_CHUNKS * 16384; i += NCT * 8192) st16_sc1(sv, r, i, 0);
        WAIT_VM(0);
        raw_barrier();
        if (tid == 0) flag_add(p, FL_READY);
    }
    const unsigned long long sides = wait_sides(p, lane, cap, 3);
    unsigned long long tmask = sides >> 1;
    const __amdgpu_buffer_rsrc_t rc0 = mkrsrc(p.ws + OFF_COMM0 + (((unsigned)sides & 1u) << 13), 2 * MB - 8192);
    st16_sc1(sv, rt, tid * 16, t_off(0, tmask) + c * 16384);
    st16_sc1(sv, rt, tid * 16, t_off(1, tmask) + c * 16384);
    flags_wait(p, FL_READY, 0, 1, NCT, lane, cap);
    WAIT_VM(0);
    raw_barrier();
    TLR(64 + c, 2);
    const unsigned rstr = c < 32 ? 16384u : 2048u;
    if (tid < 64) {
        const unsigned m = 2 * (l16 >> 2) + (l16 & 1), t = (l16 >> 1) & 1;
        const u32x4 src = *(const u32x4*)(p.mem0 + m * D + 16 * c + 8 * t);
        st16_sc1(u32x4{nosent(src.x), nosent(src.y), nosent(src.z), nosent(src.w)}, rc0, l16 * 16 + g * rstr, physo(mem_off(0, 0, c)));
    }
    if (tid < 2) lds_vol_st<unsigned>(lds + 65536 + tid * 4, 0u);
    flags_wait(p, FL_GU, 2, H, 1u, lane, cap);
#pragma unroll
    for (int j = 0; j < 4; ++j) lds_st16(lds, tid * 16 + j * 8192, ld16_sc1(ru, tid * 16 + j * 8192, c * 32768));
    raw_barrier();
    TLR(64 + c, 3);
    if (wave == 0) {
        int nfail = 0;
        constexpr bool second = true;
        int nft = 0, nfm = 0, nfx = 0; (void)nft; (void)nfm; (void)nfx;
        const unsigned st_voff = (l16 & 6) == 0 ? rowi(2 * g + (l16 & 1), l16 >> 3) * 16 : 0x7FFFFFF0u;
        unsigned long long c0 = 0, c1 = 0, c2 = 0; unsigned k01 = 0, k12 = 0; (void)c0; (void)c1; (void)c2; (void)k01; (void)k12;
        for (int s = 0; s + 1 < NSEG; ++s) {
            const char* ub = lds + (s & 1) * 32768;
            u32x4 uf[24];
#pragma unroll
            for (int j = 0; j < 24; ++j) uf[j] = lds_ld16(ub, j * 1024 + lane * 16);
            unsigned ot = t_off(s, tmask) + c * 16384;
            unsigned ox0 = physo(px_base(s & 3, 0)), ox1 = ox0 + 16384, om = physo(pm_base(s & 3, 0));
            unsigned ost = physo(mem_off((s + 1) & 3, 0, c));
            const unsigned opx_x = ox0 ^ (ox0 + 32768), opx_y = ox1 ^ (ox1 + 32768);
            PIN_S(ot); PIN_S(ox0); PIN_S(ox1); PIN_S(om); PIN_S(ost);
            unsigned tp = clamp_rt(read_ann(rc0, physo(ANN_BASE + s * 128), cap));
            int sp = 0;
            float sa, sb;
            bool chk = false;
            unsigned oy0 = ox0 + 32768, oy1 = ox1 + 32768, on = om + 4096;
            PIN_S(oy0); PIN_S(oy1); PIN_S(on);
#define C_LDX(j, o0, o1) px[j] = ld16_sc1(rc0, lane * 16 + ((j) & 7) * 1024, (j) < 8 ? (o0) : (o1))
#define C_PASS2() do { \
                f32x4 ax = zero4(); \
                _Pragma("unroll") for (int hb = 0; hb < 2; ++hb) { \
                    u32x4 uu[16]; \
                    _Pragma("unroll") for (int j = 0; j < 16; ++j) uu[j] = lds_ld16(ub, (16 * hb + j) * 1024 + lane * 16); \
                    SB(); \
                    _Pragma("unroll") for (int j = 0; j < 8; ++j) ax = smf(px[8 * hb + j], cat8(uu[2 * j], uu[2 * j + 1]), ax, idx); \
                } \
                _Pragma("unroll") for (int j = 0; j < 4; ++j) ax = smf(pm[j], cat8(tf[2 * j], tf[2 * j + 1]), ax, idx); \
                sa = ax[0] + ax[2]; sb = ax[1] + ax[3]; \
            } while (0)
#define C_PUBLISH() do { \
                MT(c1); \
                const float give = (lane & 1) ? sa : sb; \
                const float got = u2f(__builtin_amdgcn_update_dpp(0, f2u(give), 0xB1, 0xf, 0xf, true)); \
                const unsigned pk = nosent(pk_bf16((lane & 1) ? got : sa, (lane & 1) ? sb : got)); \
                u32x4 v; \
                v.x = pk; \
                v.y = __builtin_amdgcn_update_dpp(0, pk, 0x102, 0xf, 0xf, true); \
                v.z = __builtin_amdgcn_update_dpp(0, pk, 0x104, 0xf, 0xf, true); \
                v.w = __builtin_amdgcn_update_dpp(0, pk, 0x106, 0xf, 0xf, true); \
                st16_sc1(v, rc0, st_voff, ost); \
                tb = now_rt(); \
            } while (0)
            bool done = false, fast = false;
            unsigned tb = 0;
            {
                u32x4 tf[8], pm[4], px[16];
                f32x4 ax = zero4();
                asm volatile("" : "+v"(ax));
                wait_rt(tp);
                MT(c0);
#pragma unroll
                for (int j = 0; j < 16; ++j) { C_LDX(j, ox0, ox1); SB(); }
#pragma unroll
                for (int j = 0; j < 4; ++j) { pm[j] = ld16_sc1(rc0, lane * 16 + j * 1024, om); SB(); tf[2 * j] = ld16_sc1(rt, lane * 16 + 2 * j * 1024, ot); SB(); tf[2 * j + 1] = ld16_sc1(rt, lane * 16 + (2 * j + 1) * 1024, ot); SB(); }
                u32x4 ul[8];
#pragma unroll
                for (int j = 0; j < 16; ++j) {
                    const u32x4 u0 = j < 12 ? uf[j < 12 ? 2 * j : 0] : ul[j < 12 ? 0 : 2 * j - 24], u1 = j < 12 ? uf[j < 12 ? 2 * j + 1 : 0] : ul[j < 12 ? 0 : 2 * j - 23];
                    ax = smf(px[j], cat8(u0, u1), ax, idx);
                    SB();
                    if (j < 8) ul[j] = lds_ld16(ub, (24 + j) * 1024 + lane * 16);
                    if (second) C_LDX(j, oy0, oy1);
                    SB();
                }
#pragma unroll
                for (int j = 0; j < 4; ++j) { ax = smf(pm[j], cat8(tf[2 * j], tf[2 * j + 1]), ax, idx); SB(); }
                sa = ax[0] + ax[2]; sb = ax[1] + ax[3];
                done = fast = !any_nan(sa, sb);
                if (done) {
                    C_PUBLISH();
                    asm volatile("" :: "v"(px[0]), "v"(px[1]), "v"(px[2]), "v"(px[3]), "v"(px[4]), "v"(px[5]), "v"(px[6]), "v"(px[7]), "v"(px[8]), "v"(px[9]), "v"(px[10]), "v"(px[11]), "v"(px[12]),
                                 "v"(px[13]), "v"(px[14]), "v"(px[15]) : "memory");
                } else {
                    ++sp;
#pragma unroll
                    for (int j = 0; j < 4; ++j) { pm[j] = ld16_sc1(rc0, lane * 16 + j * 1024, on); tf[2 * j] = ld16_sc1(rt, lane * 16 + 2 * j * 1024, ot); tf[2 * j + 1] = ld16_sc1(rt, lane * 16 + (2 * j + 1) * 1024, ot); }
                    SB();
                    C_PASS2();
                    done = !any_nan(sa, sb);
                }
            }
            while (!done) {
                ++sp;
                u32x4 tf[8], pm[4], px[16];
#pragma unroll
                for (int j = 0; j < 16; ++j) { C_LDX(j, ox0, ox1); SB(); }
#pragma unroll
                for (int j = 0; j < 4; ++j) { pm[j] = ld16_sc1(rc0, lane * 16 + j * 1024, om); SB(); tf[2 * j] = ld16_sc1(rt, lane * 16 + 2 * j * 1024, ot); SB(); tf[2 * j + 1] = ld16_sc1(rt, lane * 16 + (2 * j + 1) * 1024, ot); SB(); }
                C_PASS2();
                if (!any_nan(sa, sb) || chk) break;
                ox0 ^= opx_x; ox1 ^= opx_y; om ^= 4096;
                if (sp >= 3) {
                    for (int k = 0;; ++k) {
                        asm volatile("" ::: "memory");
                        unsigned m = 0;
#pragma unroll
                        for (int j = 0; j < 8; ++j) m = mx5(mx5(mx5(m, ld16_sc1(rc0, lane * 16 + j * 1024, ox0)), ld16_sc1(rc0, lane * 16 + j * 1024, ox1)), ld16_sc1(rt, lane * 16 + j * 1024, ot));
#pragma unroll
                        for (int j = 0; j < 4; ++j) m = mx5(m, ld16_sc1(rc0, lane * 16 + j * 1024, om));
                        if (__all(m != SENT)) break;
                        if (k > cap) { flag_set(p, FL_TMO, 1u); cap = 0; break; }
                    }
                    chk = true;
                }
            }
            if (!fast) C_PUBLISH();
            MT(c2); MTW(); PACC(k01, c0, c1); PACC(k12, c1, c2); PDUMP(64 + c, k01, k12, (unsigned)sp, 0u);
            if (lane == 0) st4_sc1(nosent(tb), rc0, s * 4, physo(STB_BASE + c * 256));
            if (lane == 0) flag_set(p, FL_CB + c, s + 1);
            if (s == 2) tmask = wait_sides(p, lane, cap) >> 1;
            if (s >= 4) nfail += sp;
            raw_barrier();
        }
    } else {
        const int udly = TUNEW(3, 0);
        for (int s = 0; s + 1 < NSEG; ++s) {
            if (wave >= 4 && s + 2 < NSEG) {
                char* nb = lds + ((s + 1) & 1) * 32768;
                for (int i = 0; i < udly; ++i) __builtin_amdgcn_s_sleep(2);
                if (lds_vol_ld<unsigned>(lds + 65536 + vu_par(s + 1) * 4) < vu_cnt(s + 1)) flags_wait(p, FL_GU + vu_par(s + 1), 2, H, vu_cnt(s + 1), lane, cap);
                u32x4 t[8];
#pragma unroll
                for (int j = 0; j < 8; ++j) t[j] = ld16_sc1(ru, (tid - 256) * 16 + j * 4096, ((s + 1) * NCT + c) * 32768);
#pragma unroll
                for (int j = 0; j < 8; ++j) lds_st16(nb, (tid - 256) * 16 + j * 4096, t[j]);
            } else if (wave <= 2 && s + 2 < NSEG) {
                if (s == 3) tmask = wait_sides(p, lane, cap) >> 1;
                for (int i = 0; i < udly; ++i) __builtin_amdgcn_s_sleep(2);
#pragma unroll
                for (int j = 0; j < 4; ++j) st16_sc1(sv, rt, (tid - 64) * 16 + j * 2048, t_off(s + 2, tmask) + c * 16384);
                WAIT_VM(0);
            } else if (wave == 3) {
                if (s >= 1) st16_sc1(sv, rc0, l16 * 16 + g * rstr, physo(mem_off((s - 1) & 3, 0, c)));
                const int pr = vu_par(s + 2);
                if (s + 3 < NSEG && lds_vol_ld<unsigned>(lds + 65536 + pr * 4) < vu_cnt(s + 2)) {
                    __builtin_amdgcn_s_sleep(20);
                    const float v = -wave_max(-(float)flag_get(p, FL_GU + pr + 2 * (lane & 31)));
                    if (lane == 0) lds_vol_st<unsigned>(lds + 65536 + pr * 4, (unsigned)v);
                }
                WAIT_VM(0);
            }
            raw_barrier();
        }
    }
}

constexpr int NCHAIN = 104, NWG = 256;
DEV int bcast0(int v, char* lds) {
    if (threadIdx.x == 0) *(int*)lds = v;
    __syncthreads();
    const int r = __builtin_amdgcn_readfirstlane(*(int*)lds);
    __syncthreads();
    return r;
}
DEV int near_role(int t) { return t < 40 ? 64 + t : t < 104 ? t - 40 : 136 + (t - 104); }
DEV int far_role(int u) { return u < 24 ? 232 + u : u < 56 ? 168 + 2 * (u - 24) : u < 88 ? 104 + (u - 56) : u < 120 ? 168 + 2 * (u - 88) + 1 : 160 + (u - 120); }
DEV int get_role(const Params& p, char* lds) {
    int role = 0;
    if (threadIdx.x == 0) {
        const unsigned xcc = __builtin_amdgcn_s_getreg(20 | (3 << 11)) & 0xf;
        if (xcc < 4) { const int t = (int)flag_add(p, FL_TNEAR); role = t < 128 ? near_role(t) : far_role(255 - t); }
        else { const int u = (int)flag_add(p, FL_TFAR); role = u < 128 ? far_role(u) : near_role(255 - u); }
    }
    return bcast0(role, lds);
}

DEV void task_out(const Params& p, int s, int cg, char* lds, int& cap) {
    const int tid = threadIdx.x, lane = tid & 63, wave = __builtin_amdgcn_readfirstlane(tid >> 6);
    const int g = lane >> 4, l16 = lane & 15;
    const __amdgpu_buffer_rsrc_t rpx = mkrsrc(p.ws + OFF_PX, 5 * MB);
    const __amdgpu_buffer_rsrc_t rt = mkrsrc(p.ws + OFF_T, 44 * MB), ru = mkrsrc(p.ws + OFF_U, 80 * MB);
    const unsigned long long tmask = wait_sides(p, lane, cap) >> 1;
    const int c = cg * 8 + wave;
    flags_wait(p, FL_GU + vu_par(s), 2, H, vu_cnt(s), lane, cap);
    u32x4 uf[32];
#pragma unroll
    for (int j = 0; j < 32; ++j) uf[j] = ld16_sc1(ru, lane * 16 + j * 1024, (s * NCT + c) * 32768);
    if (wave == 0) { flags_wait(p, FL_CB + cg * 8, 1, 8, s + 1, lane, cap); flags_wait(p, FL_PX, 1, 64, s + 1, lane, cap); }
    __syncthreads();
    u32x4 tf[8];
    {
        u32x4 v[10];
#pragma unroll
        for (int i = 0; i < 8; ++i) v[i] = ld16_sc1(rpx, tid * 16 + i * 8192, s * 65536);
#pragma unroll
        for (int i = 0; i < 2; ++i) v[8 + i] = ld16_sc1(rpx, tid * 16 + i * 8192, 4 * MB + s * 16384);
#pragma unroll
        for (int j = 0; j < 8; ++j) tf[j] = ld16_sc1(rt, lane * 16 + j * 1024, t_off(s, tmask) + c * 16384);
#pragma unroll
        for (int i = 0; i < 10; ++i) lds_st16(lds, tid * 16 + i * 8192, v[i]);
    }
    raw_barrier();
    f32x4 d0 = {}, d1 = {};
#pragma unroll
    for (int hh = 0; hh < 32; ++hh) {
        d0 = mf16(uf[hh], lds_ld16(lds, hh * 2048 + lane * 16), d0);
        d1 = mf16(uf[hh], lds_ld16(lds, hh * 2048 + 1024 + lane * 16), d1);
    }
#pragma unroll
    for (int j = 0; j < 8; ++j) {
        d0 = mf16(tf[j], lds_ld16(lds, 65536 + (4 * j + g) * 512 + l16 * 16), d0);
        d1 = mf16(tf[j], lds_ld16(lds, 65536 + (4 * j + g) * 512 + 256 + l16 * 16), d1);
    }
    unsigned short* o = p.out + (size_t)(s * 32 + l16) * (2 * D) + D + c * 16 + 4 * g;
    *(u32x2*)o = u32x2{pk_bf16(d0[0], d0[1]), pk_bf16(d0[2], d0[3])};
    *(u32x2*)(o + 16 * 2 * D) = u32x2{pk_bf16(d1[0], d1[1]), pk_bf16(d1[2], d1[3])};
    __syncthreads();
}
DEV void task_out_fine(const Params& p, int s, int c, char* lds, int& cap) {
    const int tid = threadIdx.x, lane = tid & 63, wave = __builtin_amdgcn_readfirstlane(tid >> 6);
    const int g = lane >> 4, l16 = lane & 15;
    const __amdgpu_buffer_rsrc_t rpx = mkrsrc(p.ws + OFF_PX, 5 * MB);
    const __amdgpu_buffer_rsrc_t rt = mkrsrc(p.ws + OFF_T, 44 * MB), ru = mkrsrc(p.ws + OFF_U, 80 * MB);
    const unsigned long long tmask = wait_sides(p, lane, cap) >> 1;
    flags_wait(p, FL_GU + vu_par(s), 2, H, vu_cnt(s), lane, cap);
    u32x4 uf[4], tf;
#pragma unroll
    for (int j = 0; j < 4; ++j) uf[j] = ld16_sc1(ru, lane * 16 + (4 * wave + j) * 1024, (s * NCT + c) * 32768);
    if (wave == 0) flags_wait(p, FL_CB + c, 0, 1, s, lane, cap);
    __syncthreads();
    flags_wait(p, FL_PX + 8 * wave, 1, 8, s + 1, lane, cap);
    u32x4 pf[4][2], pm[2];
#pragma unroll
    for (int j = 0; j < 4; ++j) { pf[j][0] = ld16_sc1(rpx, lane * 16 + (4 * wave + j) * 2048, s * 65536); pf[j][1] = ld16_sc1(rpx, lane * 16 + (4 * wave + j) * 2048 + 1024, s * 65536); }
    pm[0] = ld16_sc1(rpx, (4 * wave + g) * 512 + l16 * 16, 4 * MB + s * 16384); pm[1] = ld16_sc1(rpx, (4 * wave + g) * 512 + 256 + l16 * 16, 4 * MB + s * 16384);
    for (int sp = 0;; ++sp) {
        asm volatile("" ::: "memory");
        tf = ld16_sc1(rt, lane * 16 + wave * 1024, t_off(s, tmask) + c * 16384);
        if (__all(mx4(tf) != SENT)) break;
        if (sp > cap) { flag_set(p, FL_TMO, 1u); cap = 0; break; }
    }
    f32x4 d0 = mf16(tf, pm[0], zero4()), d1 = mf16(tf, pm[1], zero4());
#pragma unroll
    for (int j = 0; j < 4; ++j) { d0 = mf16(uf[j], pf[j][0], d0); d1 = mf16(uf[j], pf[j][1], d1); }
    *(f32x4*)(lds + wave * 2048 + lane * 16) = d0; *(f32x4*)(lds + wave * 2048 + 1024 + lane * 16) = d1;
    __syncthreads();
    if (wave < 2) {
        f32x4 a = {};
#pragma unroll
        for (int w = 0; w < 8; ++w) a += *(const f32x4*)(lds + w * 2048 + wave * 1024 + lane * 16);
        *(u32x2*)(p.out + (size_t)(s * 32 + 16 * wave + l16) * (2 * D) + D + c * 16 + 4 * g) = u32x2{pk_bf16(a[0], a[1]), pk_bf16(a[2], a[3])};
    }
}

struct cf { float x, y; };
DEV cf cmul(cf a, cf b) { return cf{a.x * b.x - a.y * b.y, a.x * b.y + a.y * b.x}; }
DEV cf cmulc(cf a, cf b) { return cf{a.x * b.x + a.y * b.y, a.y * b.x - a.x * b.y}; }
DEV cf cadd(cf a, cf b) { return cf{a.x + b.x, a.y + b.y}; }
DEV cf csub(cf a, cf b) { return cf{a.x - b.x, a.y - b.y}; }
template <int SG> DEV cf mulmi(cf a) { return SG < 0 ? cf{a.y, -a.x} : cf{-a.y, a.x}; }
template <int SG> DEV void dft4(cf& a, cf& b, cf& c, cf& d) {
    cf t0 = cadd(a, c), t1 = csub(a, c), t2 = cadd(b, d), t3 = mulmi<SG>(csub(b, d));
    a = cadd(t0, t2); c = csub(t0, t2); b = cadd(t1, t3); d = csub(t1, t3);
}
template <int SG> DEV void dft8(cf* v) {
    constexpr float R = 0.70710678118654752f;
    dft4<SG>(v[0], v[2], v[4], v[6]);
    dft4<SG>(v[1], v[3], v[5], v[7]);
    cf o1 = cf{(v[3].x - SG * v[3].y) * R, (v[3].y + SG * v[3].x) * R};
    cf o2 = mulmi<SG>(v[5]);
    cf o3 = cf{(-v[7].x - SG * v[7].y) * R, (-v[7].y + SG * v[7].x) * R};
    cf e0 = v[0], e1 = v[2], e2 = v[4], e3 = v[6], o0 = v[1];
    v[0] = cadd(e0, o0); v[4] = csub(e0, o0);
    v[1] = cadd(e1, o1); v[5] = csub(e1, o1);
    v[2] = cadd(e2, o2); v[6] = csub(e2, o2);
    v[3] = cadd(e3, o3); v[7] = csub(e3, o3);
}
template <int SG> DEV void dft16(cf* v) {
    constexpr float C1 = 0.92387953251128674f, S1 = 0.38268343236508977f, R = 0.70710678118654752f;
#pragma unroll
    for (int b = 0; b < 4; ++b) dft4<SG>(v[b], v[4 + b], v[8 + b], v[12 + b]);
    const cf w1 = {C1, SG * S1}, w2 = {R, SG * R}, w3 = {S1, SG * C1}, w6 = {-R, SG * R}, w9 = {-C1, -SG * S1};
    v[5] = cmul(v[5], w1); v[6] = cmul(v[6], w2); v[7] = cmul(v[7], w3);
    v[9] = cmul(v[9], w2); v[10] = mulmi<SG>(v[10]); v[11] = cmul(v[11], w6);
    v[13] = cmul(v[13], w3); v[14] = cmul(v[14], w6); v[15] = cmul(v[15], w9);
#pragma unroll
    for (int c = 0; c < 4; ++c) dft4<SG>(v[4 * c], v[4 * c + 1], v[4 * c + 2], v[4 * c + 3]);
    cf t;
    t = v[1]; v[1] = v[4]; v[4] = t;  t = v[2]; v[2] = v[8]; v[8] = t;  t = v[3]; v[3] = v[12]; v[12] = t;
    t = v[6]; v[6] = v[9]; v[9] = t;  t = v[7]; v[7] = v[13]; v[13] = t; t = v[11]; v[11] = v[14]; v[14] = t;
}
template <int SG> DEV void dft5(cf* v) {
    constexpr float C1 = 0.30901699437494742f, C2 = -0.80901699437494742f, S1 = 0.95105651629515357f, S2 = 0.58778525229247313f;
    cf a1 = cadd(v[1], v[4]), b1 = csub(v[1], v[4]), a2 = cadd(v[2], v[3]), b2 = csub(v[2], v[3]);
    cf r1 = {v[0].x + C1 * a1.x + C2 * a2.x, v[0].y + C1 * a1.y + C2 * a2.y};
    cf r2 = {v[0].x + C2 * a1.x + C1 * a2.x, v[0].y + C2 * a1.y + C1 * a2.y};
    cf i1 = mulmi<SG>(cf{S1 * b1.x + S2 * b2.x, S1 * b1.y + S2 * b2.y});
    cf i2 = mulmi<SG>(cf{S2 * b1.x - S1 * b2.x, S2 * b1.y - S1 * b2.y});
    v[0] = cadd(v[0], cadd(a1, a2));
    v[1] = cadd(r1, i1); v[4] = csub(r1, i1); v[2] = cadd(r2, i2); v[3] = csub(r2, i2);
}
DEV cf twid(int num, int den) { float s, c; sincospif(-2.0f * (float)num / (float)den, &s, &c); return cf{c, s}; }

constexpr int FRP = 648;
template <int SG> DEV void fft640x8(cf* a, cf* b, const cf* tab, int tid) {
    if (tid < 320) {
        const int q = tid / 40, j = tid % 40; cf v[16];
#pragma unroll
        for (int r = 0; r < 16; ++r) v[r] = a[q * FRP + j + 40 * r];
        dft16<SG>(v);
#pragma unroll
        for (int r = 0; r < 16; ++r) b[q * FRP + 16 * j + (r ^ (j & 15))] = v[r];
    }
    __syncthreads();
    for (int t = tid; t < 640; t += 512) {
        const int q = t / 80, j = t % 80, k = j & 15; cf v[8];
#pragma unroll
        for (int r = 0; r < 8; ++r) { const int n = j + 80 * r; v[r] = b[q * FRP + (n & ~15) + ((n & 15) ^ ((n >> 4) & 15))]; }
#pragma unroll
        for (int r = 1; r < 8; ++r) { const cf w = tab[5 * k * r]; v[r] = SG < 0 ? cmul(v[r], w) : cmulc(v[r], w); }
        dft8<SG>(v);
#pragma unroll
        for (int r = 0; r < 8; ++r) a[q * FRP + (j >> 4) * 128 + k + 16 * r] = v[r];
    }
    __syncthreads();
    for (int t = tid; t < 1024; t += 512) {
        const int q = t >> 7, j = t & 127; cf v[5];
#pragma unroll
        for (int r = 0; r < 5; ++r) v[r] = a[q * FRP + j + 128 * r];
#pragma unroll
        for (int r = 1; r < 5; ++r) { const cf w = tab[j * r]; v[r] = SG < 0 ? cmul(v[r], w) : cmulc(v[r], w); }
        dft5<SG>(v);
#pragma unroll
        for (int r = 0; r < 5; ++r) b[q * FRP + j + 128 * r] = v[r];
    }
    __syncthreads();
}
DEV void make_tab640(cf* tab, int tid) { for (int i = tid; i < 640; i += 512) tab[i] = twid(i, 640); }

DEV void task_fft1(const Params& p, int t, char* lds) {
    const int tid = threadIdx.x;
    cf* a = (cf*)lds; cf* b = a + 8 * FRP; cf* tab = b + 8 * FRP;
    make_tab640(tab, tid);
    for (int i = tid; i < 8 * 80; i += 512) {
        const int q = i / 80, ch = i % 80;
        const u32x4 u = *(const u32x4*)(p.ax + (size_t)(16 * t + 2 * q) * D + ch * 8), w = *(const u32x4*)(p.ax + (size_t)(16 * t + 2 * q + 1) * D + ch * 8);
        cf* d = a + q * FRP + ch * 8;
        d[0] = cf{bf_lo(u.x), bf_lo(w.x)}; d[1] = cf{bf_hi(u.x), bf_hi(w.x)}; d[2] = cf{bf_lo(u.y), bf_lo(w.y)}; d[3] = cf{bf_hi(u.y), bf_hi(w.y)};
        d[4] = cf{bf_lo(u.z), bf_lo(w.z)}; d[5] = cf{bf_hi(u.z), bf_hi(w.z)}; d[6] = cf{bf_lo(u.w), bf_lo(w.w)}; d[7] = cf{bf_hi(u.w), bf_hi(w.w)};
    }
    __syncthreads();
    fft640x8<-1>(a, b, tab, tid);
    const __amdgpu_buffer_rsrc_t rx = mkrsrc(p.ws + OFF_X1, 6 * MB);
    for (int i = tid; i < DF * 8; i += 512) {
        const int k = i >> 3, q = i & 7;
        const cf z = b[q * FRP + k], y = b[q * FRP + (k ? 640 - k : 0)];
        f32x4 o = {0.5f * (z.x + y.x), 0.5f * (z.y - y.y), 0.5f * (z.y + y.y), 0.5f * (y.x - z.x)};
        st16_sc1(__builtin_bit_cast(u32x4, o), rx, (k * S + 16 * t + 2 * q) * 8, 0);
    }
    unsigned* wt = (unsigned*)a;
    for (int i = tid; i < 16 * DF; i += 512) { const int r = i / DF, k = i % DF; wt[k * 17 + r] = (unsigned)p.wr[(size_t)(16 * t + r) * DF + k] | ((unsigned)p.wi[(size_t)(16 * t + r) * DF + k] << 16); }
    __syncthreads();
    const __amdgpu_buffer_rsrc_t rw = mkrsrc(p.ws + OFF_WT, 3 * MB);
    for (int i = tid; i < DF * 4; i += 512) { const int k = i >> 2, r4 = (i & 3) * 4;
        u32x4 v = {wt[k * 17 + r4], wt[k * 17 + r4 + 1], wt[k * 17 + r4 + 2], wt[k * 17 + r4 + 3]};
        st16_sc1(v, rw, (k * S + 16 * t + r4) * 4, 0); }
    __syncthreads();
}

constexpr int CP = 2048 + 128;
DEV int cpad(int i) { return i + (i >> 4); }
DEV void fft2_twiddles(cf (&t1)[16], cf (&t2)[16]) {
    const int m = threadIdx.x & 127, n2 = m >> 4;
#pragma unroll
    for (int i = 1; i < 16; ++i) { t1[i] = twid(m * i, 2048); t2[i] = twid(n2 * i, 128); }
}
DEV void task_fft2(const Params& p, int t, char* lds, cf (&t1)[16], cf (&t2)[16]) {
    int tid = threadIdx.x;
    asm volatile("" : "+v"(tid));
#pragma unroll
    for (int i = 1; i < 16; ++i) asm volatile("" : "+v"(t1[i].x), "+v"(t1[i].y), "+v"(t2[i].x), "+v"(t2[i].y));
    const int col = tid >> 7, m = tid & 127;
    const int k = 4 * t + col;
    const bool act = k < DF;
    cf* buf = (cf*)lds + col * CP;
    const __amdgpu_buffer_rsrc_t rx = mkrsrc(p.ws + OFF_X1, (unsigned)(DF * S * 8)), ry = mkrsrc(p.ws + OFF_Y1, (unsigned)(DF * S * 8)), rw = mkrsrc(p.ws + OFF_WT, (unsigned)(DF * S * 4));
    cf v[16];
#pragma unroll
    for (int n0 = 0; n0 < 16; ++n0) { u32x2 u = ld8_sc1(rx, (k * S + 128 * n0 + m) * 8, 0); v[n0] = __builtin_bit_cast(cf, u); }
    const int f0b = m & 15, n2 = m >> 4;
    const int f1c = m >> 4;
    unsigned wv[16];
#pragma unroll
    for (int u = 0; u < 2; ++u)
#pragma unroll
        for (int f2 = 0; f2 < 8; ++f2) wv[u * 8 + f2] = __builtin_amdgcn_raw_buffer_load_b32(rw, (k * S + f0b + 16 * (f1c + 8 * u) + 256 * f2) * 4, 0, 16);
    dft16<-1>(v);
#pragma unroll
    for (int f0 = 1; f0 < 16; ++f0) v[f0] = cmul(v[f0], t1[f0]);
#pragma unroll
    for (int f0 = 0; f0 < 16; ++f0) buf[cpad(m * 16 + f0)] = v[f0];
    __syncthreads();
#pragma unroll
    for (int n1 = 0; n1 < 16; ++n1) v[n1] = buf[cpad((8 * n1 + n2) * 16 + f0b)];
    dft16<-1>(v);
#pragma unroll
    for (int f1 = 1; f1 < 16; ++f1) v[f1] = cmul(v[f1], t2[f1]);
    __syncthreads();
#pragma unroll
    for (int f1 = 0; f1 < 16; ++f1) buf[cpad((f1 * 8 + n2) * 16 + f0b)] = v[f1];
    __syncthreads();
#pragma unroll
    for (int u = 0; u < 2; ++u)
#pragma unroll
        for (int j = 0; j < 8; ++j) v[u * 8 + j] = buf[cpad(((f1c + 8 * u) * 8 + j) * 16 + f0b)];
    dft8<-1>(v); dft8<-1>(v + 8);
    constexpr float NRM = 1.0f / (2048.0f * 640.0f);
#pragma unroll
    for (int i = 0; i < 16; ++i) v[i] = cmul(v[i], cf{bf_lo(wv[i]) * NRM, bf_hi(wv[i]) * NRM});
    dft8<1>(v); dft8<1>(v + 8);
#pragma unroll
    for (int u = 0; u < 2; ++u)
#pragma unroll
        for (int j = 0; j < 8; ++j) buf[cpad(((f1c + 8 * u) * 8 + j) * 16 + f0b)] = v[u * 8 + j];
    __syncthreads();
#pragma unroll
    for (int f1 = 0; f1 < 16; ++f1) v[f1] = buf[cpad((f1 * 8 + n2) * 16 + f0b)];
#pragma unroll
    for (int f1 = 1; f1 < 16; ++f1) v[f1] = cmulc(v[f1], t2[f1]);
    dft16<1>(v);
#pragma unroll
    for (int n1 = 0; n1 < 16; ++n1) buf[cpad((8 * n1 + n2) * 16 + f0b)] = v[n1];
    __syncthreads();
#pragma unroll
    for (int f0 = 0; f0 < 16; ++f0) v[f0] = buf[cpad(m * 16 + f0)];
#pragma unroll
    for (int f0 = 1; f0 < 16; ++f0) v[f0] = cmulc(v[f0], t1[f0]);
    dft16<1>(v);
    if (act) {
#pragma unroll
        for (int n0 = 0; n0 < 16; ++n0) st8_sc1(__builtin_bit_cast(u32x2, v[n0]), ry, (k * S + 128 * n0 + m) * 8, 0);
    }
    __syncthreads();
}

DEV void task_fft3(const Params& p, int t, char* lds) {
    const int tid = threadIdx.x;
    cf* a = (cf*)lds; cf* b = a + 8 * FRP; cf* tab = b + 8 * FRP;
    make_tab640(tab, tid);
    const __amdgpu_buffer_rsrc_t ry = mkrsrc(p.ws + OFF_Y1, 6 * MB);
    for (int i = tid; i < DF * 8; i += 512) {
        const int k = i >> 3, q = i & 7;
        f32x4 o = __builtin_bit_cast(f32x4, ld16_sc1(ry, (k * S + 16 * t + 2 * q) * 8, 0));
        if (k == 0 || k == 320) { o[1] = 0.f; o[3] = 0.f; }
        a[q * FRP + k] = cf{o[0] - o[3], o[1] + o[2]};
        if (k > 0 && k < 320) a[q * FRP + 640 - k] = cf{o[0] + o[3], o[2] - o[1]};
    }
    __syncthreads();
    fft640x8<1>(a, b, tab, tid);
    for (int i = tid; i < 8 * 80; i += 512) {
        const int q = i / 80, ch = i % 80;
        const cf* d = b + q * FRP + ch * 8;
        u32x4 u = {pk_bf16(d[0].x, d[1].x), pk_bf16(d[2].x, d[3].x), pk_bf16(d[4].x, d[5].x), pk_bf16(d[6].x, d[7].x)};
        u32x4 w = {pk_bf16(d[0].y, d[1].y), pk_bf16(d[2].y, d[3].y), pk_bf16(d[4].y, d[5].y), pk_bf16(d[6].y, d[7].y)};
        *(u32x4*)(p.out + (size_t)(16 * t + 2 * q) * (2 * D) + ch * 8) = u;
        *(u32x4*)(p.out + (size_t)(16 * t + 2 * q + 1) * (2 * D) + ch * 8) = w;
    }
    __syncthreads();
}

constexpr int NT1 = 128, NT2 = 81, NT3 = 128, NFINE = 2, NTO = (NSEG - NFINE) * 5;
__global__ void __launch_bounds__(512, 2) k_main(Params p) {
    extern __shared__ __attribute__((aligned(16))) char lds[];
#define TL(slot) do {} while (0)
    const int role = get_role(p, lds);
    TL(1);
    if (role < 32) { role_qk(p, role, lds); TL(7); return; }
    if (role < 64) { role_vo(p, role - 32, lds); TL(7); return; }
    if (role < NCHAIN) { role_comb(p, role - 64, lds); TL(7); return; }
    const int w = role - NCHAIN, tid = threadIdx.x, lane = tid & 63;
    if (w >= 32 && w < 56) {
        const u32x4* src[5] = {(const u32x4*)p.bx, (const u32x4*)(p.ws + OFF_PX), (const u32x4*)p.ax, (const u32x4*)p.wr, (const u32x4*)p.wi};
        unsigned acc = 0;
#pragma unroll
        for (int k = 0; k < 5; ++k) {
            const int n = (k == 1 ? 5 * (int)MB : (k < 3 ? S * D : S * DF) * 2) / 128;
#pragma unroll 4
            for (int i = (w - 32) * 512 + tid; i < n; i += 24 * 512) acc ^= src[k][i * 8].x;
        }
        if (!TUNEW(9, 0)) { asm volatile("" :: "v"(acc)); return; }
        int cap = SPIN_CAP;
        const unsigned long long tmask = wait_sides(p, lane, cap) >> 1;
#pragma unroll 4
        for (int i = (w - 32) * 512 + tid + 3 * NCT * 64; i < NSEG * NCT * 64; i += 24 * 512)
            acc ^= *(const unsigned*)(p.ws + OFF_T + t_off(i / (NCT * 64), tmask) + ((i >> 6) % NCT) * 16384 + (i & 63) * 128);
        asm volatile("" :: "v"(acc));
        return;
    }
    if (w < 32) role_gemm_qk(p, w, lds);
    else if (w < 64) {}
    else if (w < 128) role_gemm_vu(p, (w - 64) >> 1, w & 1, lds);
    else if (tid < 64 && w - 128 < 1 + NTPG) probe_page(p, w - 128, lane);
    TL(2);
    int cap = SPIN_CAP;
#define NEXT_TASK(Q) (__syncthreads(), bcast0(threadIdx.x == 0 ? (int)flag_add(p, Q) : 0, lds))
    if (w < 128) {
        int t = NEXT_TASK(FL_QUEUE);
        while (t < NT1) {
            task_fft1(p, t, lds);
            WAIT_VM(0); __syncthreads();
            if (tid == 0) flag_add(p, FL_F1);
            t = NEXT_TASK(FL_QUEUE);
        }
        TL(3);
        cf tw1[16], tw2[16];
        if (t < NT1 + NT2) fft2_twiddles(tw1, tw2);
        while (t < NT1 + NT2) {
            if (tid < 64) flags_wait(p, FL_F1, 0, 1, NT1, lane, cap);
            __syncthreads();
            task_fft2(p, t - NT1, lds, tw1, tw2);
            WAIT_VM(0); __syncthreads();
            if (tid == 0) flag_add(p, FL_F2);
            t = NEXT_TASK(FL_QUEUE);
        }
        TL(4);
        while (t < NT1 + NT2 + NT3) {
            if (tid < 64) flags_wait(p, FL_F2, 0, 1, NT2, lane, cap);
            __syncthreads();
            task_fft3(p, t - NT1 - NT2, lds);
            t = NEXT_TASK(FL_QUEUE);
        }
        TL(5);
        t -= NT1 + NT2 + NT3;
        if (t < NFINE * NCT) task_out_fine(p, NSEG - NFINE + t / NCT, t % NCT, lds, cap);
    } else {
        int t = NEXT_TASK(FL_QOUT);
        while (t < NTO) {
            task_out(p, t / 5, t % 5, lds, cap);
            t = NEXT_TASK(FL_QOUT);
        }
    }
    TL(7);
}

extern "C" void kernel_launch(void* const* d_in, const int* in_sizes, int n_in, void* d_out, int out_size, void* d_ws, size_t ws_size, hipStream_t stream) {
    Params p;
    p.ax = (const unsigned short*)d_in[0]; p.wr = (const unsigned short*)d_in[1]; p.wi = (const unsigned short*)d_in[2];
    p.bx = (const unsigned short*)d_in[3]; p.mem0 = (const unsigned short*)d_in[4];
    p.wq = (const unsigned short*)d_in[5]; p.wk = (const unsigned short*)d_in[6]; p.wv = (const unsigned short*)d_in[7]; p.wo = (const unsigned short*)d_in[8];
    p.out = (unsigned short*)d_out;
    p.ws = (char*)(((size_t)d_ws + 2 * MB - 1) & ~(size_t)(2 * MB - 1));
    constexpr int LDS = 163840;
    (void)hipFuncSetAttribute((const void*)k_main, hipFuncAttributeMaxDynamicSharedMemorySize, LDS);
    (void)hipMemsetAsync(p.ws + OFF_CTRL, 0, FLAG_BYTES, stream);
    k_main<<<NWG, 512, LDS, stream>>>(p);
}
```
